```python
import torch

def _softmax(x, axis=-1):
    return torch.softmax(x, dim=axis)

def _gelu(x):
    return torch.nn.functional.gelu(x, approximate='tanh')

def _silu(x):
    return torch.nn.functional.silu(x)

def _sigmoid(x):
    return torch.sigmoid(x)

def _layernorm(x, axis=-1, eps=1e-05):
    m = x.mean(dim=axis, keepdim=True)
    v = x.var(dim=axis, keepdim=True, unbiased=False)
    return (x - m) / torch.sqrt(v + eps)

def _scatter_add(tgt, idx, src, axis=0):
    return tgt.clone().index_add_(axis, idx, src)

def _segment_sum(src, seg, n):
    out = torch.zeros((n,) + src.shape[1:], dtype=src.dtype)
    return out.index_add_(0, seg, src)

def _pad(x, pw):
    flat = tuple((v for pair in reversed(pw) for v in pair))
    return torch.nn.functional.pad(x, flat)
kernel_name = 'adaLN_cond__ssm_conv_s4__dense_activation_silu__prompt'

def setup_inputs():

    _w = lambda a: (lambda t: t.to(torch.bfloat16) if t.is_floating_point() else t)(torch.as_tensor(a))

    def _A_setup():
        B, L, D, N = (1, 2048, 448, 256)
        A_re = -(lambda _x: torch.exp(torch.as_tensor(_x)))((lambda *s: torch.randn(*s, dtype=torch.bfloat16) * 3.2)(D, N))
        A_im = torch.pi * (lambda *s: torch.randn(*s, dtype=torch.bfloat16) * 0.05)(D, N)
        B_re = (lambda *s: torch.randn(*s, dtype=torch.bfloat16) * 0.1)(D, N)
        B_im = (lambda *s: torch.randn(*s, dtype=torch.bfloat16) * 0.1)(D, N)
        C_re = (lambda *s: torch.randn(*s, dtype=torch.bfloat16) * 0.1)(D, N)
        C_im = (lambda *s: torch.randn(*s, dtype=torch.bfloat16) * 0.1)(D, N)
        log_dt = (lambda *s: torch.randn(*s, dtype=torch.bfloat16) * 0.05)(D) * 0.5 - 4.0
        Dskip = (lambda *s: torch.randn(*s, dtype=torch.bfloat16) * 0.05)(D)
        return {'u': _w((lambda *s: torch.randn(*s, dtype=torch.bfloat16) * 0.05)(B, L, D)), 'A_re': _w(A_re), 'A_im': _w(A_im), 'B_re': _w(B_re), 'B_im': _w(B_im), 'C_re': _w(C_re), 'C_im': _w(C_im), 'log_dt': _w(log_dt), 'Dskip': _w(Dskip)}
    _A_in = _A_setup()

    def _B_setup():
        B, S, D, F = (1, 2048, 448, 1792)
        return {'x': _w((lambda *s: torch.randn(*s, dtype=torch.bfloat16) * 0.05)(B, S, D)), 'w1': _w((lambda *s: torch.randn(*s, dtype=torch.bfloat16) * 3.2)(D, F) * D ** (-0.5)), 'w2': _w((lambda *s: torch.randn(*s, dtype=torch.bfloat16) * 0.4)(F, D) * F ** (-0.5))}
    _B_in = _B_setup()
    return {'A_u': _A_in['u'], 'A_A_re': _A_in['A_re'], 'A_A_im': _A_in['A_im'], 'A_B_re': _A_in['B_re'], 'A_B_im': _A_in['B_im'], 'A_C_re': _A_in['C_re'], 'A_C_im': _A_in['C_im'], 'A_log_dt': _A_in['log_dt'], 'A_Dskip': _A_in['Dskip'], 'B_x': _B_in['x'], 'B_w1': _B_in['w1'], 'B_w2': _B_in['w2'], 'c': _w((lambda *s: torch.randn(*s, dtype=torch.bfloat16) * 0.2)(1, 448) * 8.0), 'w_mod': _w((lambda *s: torch.randn(*s, dtype=torch.bfloat16) * 0.2)(448, 6 * 448) * 448 ** (-0.5) * 8.0)}

def reference(A_u, A_A_re, A_A_im, A_B_re, A_B_im, A_C_re, A_C_im, A_log_dt, A_Dskip, B_x, B_w1, B_w2, c, w_mod):

    def _A_ref(u, A_re, A_im, B_re, B_im, C_re, C_im, log_dt, Dskip):
        Bsz, L, D = u.shape
        dt = torch.exp(log_dt)[:, None]
        A = A_re + 1j * A_im
        Bc = B_re + 1j * B_im
        Cc = C_re + 1j * C_im
        logdA = dt * A
        dB = dt * Bc
        l = (lambda x, dt: x.to(dt))(torch.arange(L), A_re.dtype)
        P = torch.exp(logdA[:, :, None] * l[None, None, :])
        K = torch.einsum('dn,dnl,dn->dl', Cc, P, dB).real
        L2 = 2 * L
        Kp = _pad(K, ((0, 0), (0, L)))
        up = (lambda x, *d: x.permute(*d))(u, 0, 2, 1)
        up = _pad(up, ((0, 0), (0, 0), (0, L)))
        Kf = (lambda x, axes=(-2, -1): torch.fft.rfft2(x.float(), dim=axes))(Kp[:, None, :], axes=(-2, -1))
        Uf = (lambda x, axes=(-2, -1): torch.fft.rfft2(x.float(), dim=axes))(up[:, :, None, :], axes=(-2, -1))
        Yf = Uf * Kf[None, :, :, :]
        y = (lambda x, s, axes=(-2, -1): torch.fft.irfft2(x, s=s, dim=axes).to(torch.bfloat16))(Yf, (1, L2), axes=(-2, -1))[:, :, 0, :L]
        y = (lambda x, *d: x.permute(*d))(y, 0, 2, 1) + u * Dskip[None, None, :]
        return y

    def _B_ref(x, w1, w2):
        h = torch.einsum('bsd,df->bsf', x, w1)
        h = _silu(h)
        out = torch.einsum('bsf,fd->bsd', h, w2)
        return out
    x = A_u
    mod = (_silu(c) @ w_mod).reshape(c.shape[0], 6, 1, -1)
    sa, ca, ga, sb, cb, gb = (mod[:, 0].clone(), mod[:, 1].clone(), mod[:, 2].clone(), mod[:, 3].clone(), mod[:, 4].clone(), mod[:, 5].clone())
    h = _layernorm(x, axis=-1) * (1 + ca) + sa
    x = x + ga * _A_ref(h, A_A_re, A_A_im, A_B_re, A_B_im, A_C_re, A_C_im, A_log_dt, A_Dskip)
    h = _layernorm(x, axis=-1) * (1 + cb) + sb
    x = x + gb * _B_ref(h, B_w1, B_w2)
    return x
```

```cpp
#include <hip/hip_runtime.h>
#include <hip/hip_bf16.h>
#include <stdint.h>

#define GAS __attribute__((address_space(1)))
#define LAS __attribute__((address_space(3)))
typedef unsigned v4u __attribute__((ext_vector_type(4)));
typedef unsigned v2u __attribute__((ext_vector_type(2)));
typedef float f32x2 __attribute__((ext_vector_type(2)));
typedef float f32x4 __attribute__((ext_vector_type(4)));
typedef float f32x16 __attribute__((ext_vector_type(16)));
typedef __bf16 bf16x8 __attribute__((ext_vector_type(8)));
typedef __bf16 bf16x2 __attribute__((ext_vector_type(2)));
typedef short s16x4 __attribute__((ext_vector_type(4)));
typedef unsigned short u16;

constexpr int L = 2048, D = 448, NM = 256, F = 1792;
constexpr int NPAIR = D / 2;
constexpr int TILE_DW = NPAIR * 32;

constexpr size_t WS_XP = 0;
constexpr size_t WS_X1P = WS_XP + (size_t)L * D * 2;
constexpr size_t WS_KB = WS_X1P + (size_t)L * D * 2;
constexpr size_t WS_ST = WS_KB + (size_t)L * D * 2;
constexpr size_t WS_MOD = WS_ST + (size_t)L * 8;
constexpr size_t WS_CS = WS_MOD + 16384;
constexpr size_t WS_BI = WS_CS + 8192;
constexpr size_t WS_W1T = WS_BI + 8192;
constexpr size_t WS_W2T = WS_W1T + (size_t)D * F * 2;
constexpr size_t WS_HT = WS_W2T + (size_t)D * F * 2;

__device__ __forceinline__ float bf2f(u16 b) { return __builtin_bit_cast(float, (unsigned)b << 16); }
__device__ __forceinline__ float lo2f(unsigned u) { return __builtin_bit_cast(float, u << 16); }
__device__ __forceinline__ float hi2f(unsigned u) { return __builtin_bit_cast(float, u & 0xffff0000u); }
__device__ __forceinline__ unsigned pack2(float lo, float hi) {
  bf16x2 v; v.x = (__bf16)lo; v.y = (__bf16)hi; return __builtin_bit_cast(unsigned, v);
}
__device__ __forceinline__ float rbf(float f) { return lo2f(pack2(f, 0.f)); }
__device__ __forceinline__ f32x16 mfma32(v4u a, v4u b, f32x16 c) {
  return __builtin_amdgcn_mfma_f32_32x32x16_bf16(__builtin_bit_cast(bf16x8, a), __builtin_bit_cast(bf16x8, b), c, 0, 0, 0);
}
__device__ __forceinline__ f32x4 mfma16(v4u a, v4u b, f32x4 c) {
  return __builtin_amdgcn_mfma_f32_16x16x32_bf16(__builtin_bit_cast(bf16x8, a), __builtin_bit_cast(bf16x8, b), c, 0, 0, 0);
}
__device__ __forceinline__ float dot2(unsigned a, unsigned b, float c) {
  return __builtin_amdgcn_fdot2_f32_bf16(__builtin_bit_cast(bf16x2, a), __builtin_bit_cast(bf16x2, b), c, false);
}
__device__ __forceinline__ v2u trread(LAS char* p) {
  return __builtin_bit_cast(v2u, __builtin_amdgcn_ds_read_tr16_b64_v4i16((LAS s16x4*)p));
}
__device__ __forceinline__ float sum_x8(float v) {
  return v + __builtin_bit_cast(float, __builtin_amdgcn_update_dpp(0, __builtin_bit_cast(int, v), 0x128, 0xf, 0xf, false));
}
__device__ __forceinline__ float sum_x16(float v) {
  const unsigned u = __builtin_bit_cast(unsigned, v);
  auto r = __builtin_amdgcn_permlane16_swap(u, u, false, false);
  return __builtin_bit_cast(float, (unsigned)r[0]) + __builtin_bit_cast(float, (unsigned)r[1]);
}
__device__ __forceinline__ float sum_x32(float v) {
  const unsigned u = __builtin_bit_cast(unsigned, v);
  auto r = __builtin_amdgcn_permlane32_swap(u, u, false, false);
  return __builtin_bit_cast(float, (unsigned)r[0]) + __builtin_bit_cast(float, (unsigned)r[1]);
}
__device__ __forceinline__ float sum_hi3(float v) { return sum_x32(sum_x16(sum_x8(v))); }

struct cplx { float re, im; };
__device__ __forceinline__ cplx cmul(cplx a, cplx b) {
  cplx r; r.re = a.re * b.re - a.im * b.im; r.im = a.re * b.im + a.im * b.re; return r;
}
__device__ __forceinline__ cplx cexp_l(float al, float th, float l) {
  const float mag = __builtin_amdgcn_exp2f(al * l);
  const float ph = __builtin_amdgcn_fractf(th * l);
  cplx r; r.re = mag * __builtin_amdgcn_cosf(ph); r.im = mag * __builtin_amdgcn_sinf(ph); return r;
}

constexpr int K1_NKG = D, K1_NLN = 64, K1_NMOD = 42, K1_NW2 = 56;
constexpr int K1_LDS = 4 * 32 * 272;

__device__ __forceinline__ void role_kgen(int d, const u16* Are, const u16* Aim, const u16* Bre, const u16* Bim,
                                          const u16* Cre, const u16* Cim, const u16* logdt, u16* KB, LAS char* smem) {
  const int tid = threadIdx.x, lane = tid & 63, w = __builtin_amdgcn_readfirstlane(tid >> 6);
  const int idx = d * NM + tid;
  const float are = bf2f(Are[idx]), aim = bf2f(Aim[idx]), bre = bf2f(Bre[idx]), bim = bf2f(Bim[idx]);
  const float cre = bf2f(Cre[idx]), cim = bf2f(Cim[idx]);
  const float dt = rbf(expf(bf2f(logdt[d])));
  const float al = (dt * are) * 1.4426950408889634f, th = (dt * aim) * 0.15915494309189535f;
  const cplx W = cmul(cplx{cre, cim}, cplx{dt * bre, dt * bim});
  const cplx E1 = cexp_l(al, th, 1.f), E8 = cexp_l(al, th, 8.f), E16 = cexp_l(al, th, 16.f), E256 = cexp_l(al, th, 256.f);
  const cplx E2 = cmul(E1, E1), E4 = cmul(E2, E2);
  LAS char* base = smem + w * (32 * 272);
  LAS unsigned* wr = (LAS unsigned*)(base + lane * 4);
  const LAS v4u* rd = (const LAS v4u*)(base + (lane & 31) * 272 + (lane >> 5) * 16);
  {
    cplx p0{1.f, 0.f}, p1 = E8;
#pragma unroll
    for (int j = 0; j < 32; j += 2) {
      wr[j * 68] = pack2(p0.re, p0.im);
      wr[(j + 1) * 68] = pack2(p1.re, p1.im);
      if (j < 30) { p0 = cmul(p0, E16); p1 = cmul(p1, E16); }
    }
  }
  v4u bf[8];
#pragma unroll
  for (int s = 0; s < 8; ++s) bf[s] = rd[2 * s];
  {
    auto put = [&](int row, cplx v) { wr[row * 68] = pack2(v.re, -v.im); };
    cplx v = W;
#pragma unroll
    for (int a = 0; a < 8; ++a) { put(a, v); if (a < 7) v = cmul(v, E1); }
    cplx q = cmul(W, E256);
    put(8, q); put(9, cmul(q, E2));
    { const cplx t = cmul(q, E4); put(10, t); put(12, t); put(13, cmul(t, E2)); }
    put(14, cmul(q, E8));
    q = cmul(q, E256);
    put(16, q); { const cplx t = cmul(q, E4); put(17, t); put(20, t); } put(21, cmul(q, E8));
    q = cmul(q, E256);
    put(18, q); { const cplx t = cmul(q, E4); put(19, t); put(22, t); } put(23, cmul(q, E8));
#pragma unroll
    for (int b = 0; b < 4; ++b) { q = cmul(q, E256); put(24 + b, q); put(28 + b, cmul(q, E8)); }
  }
  f32x16 acc = {};
#pragma unroll
  for (int s = 0; s < 8; ++s) acc = mfma32(rd[2 * s], bf[s], acc);
  __syncthreads();
  LAS f32x4* ex = (LAS f32x4*)smem;
#pragma unroll
  for (int g = 0; g < 4; ++g) ex[(w * 4 + g) * 64 + lane] = f32x4{acc[4 * g], acc[4 * g + 1], acc[4 * g + 2], acc[4 * g + 3]};
  __syncthreads();
  const f32x4 v = (ex[(0 * 4 + w) * 64 + lane] + ex[(1 * 4 + w) * 64 + lane]) + (ex[(2 * 4 + w) * 64 + lane] + ex[(3 * 4 + w) * 64 + lane]);
  const int j = lane & 31, h = lane >> 5;
  v2u* out = (v2u*)(KB + (size_t)d * L + 8 * j + 4 * h);
  if (w == 0) {
    out[0] = v2u{pack2(v.x, v.y), pack2(v.z, v.w)};
  } else if (w == 1) {
    out[64] = v2u{pack2(v.x, v.x), pack2(v.y, v.z)};
  } else if (w == 2) {
    out[128] = v2u{pack2(v.x, v.x), pack2(h ? v.y : v.x, v.y)};
    out[192] = v2u{pack2(v.z, v.z), pack2(h ? v.w : v.z, v.w)};
  } else {
#pragma unroll
    for (int b = 0; b < 4; ++b) {
      const float vb = v[b];
      const unsigned u = __builtin_bit_cast(unsigned, vb);
      auto r = __builtin_amdgcn_permlane32_swap(u, u, false, false);
      const float o = __builtin_bit_cast(float, (unsigned)r[0]);
      out[256 + 64 * b] = v2u{pack2((j & 1) ? vb : o, vb), pack2(vb, vb)};
    }
  }
}

__device__ __forceinline__ void role_ln(int tile, const u16* x, unsigned* XP, f32x2* ST, LAS char* smem) {
  const int tid = threadIdx.x, lane = tid & 63, w = __builtin_amdgcn_readfirstlane(tid >> 6);
  const int tq = lane & 7, cc = lane >> 3;
  const int nrd = (w < 3) ? 2 : 1;
  v4u v[2][4];
#pragma unroll
  for (int rd = 0; rd < 2; ++rd)
#pragma unroll
    for (int i = 0; i < 4; ++i) {
      v[rd][i] = v4u{0u, 0u, 0u, 0u};
      if (rd < nrd) v[rd][i] = *(const v4u*)(x + (size_t)(tile * 32 + 4 * tq + i) * D + (8 * (w + 4 * rd) + cc) * 8);
    }
#pragma unroll
  for (int rd = 0; rd < 2; ++rd)
    if (rd < nrd) {
#pragma unroll
      for (int c = 0; c < 4; ++c)
        *(v4u*)(XP + (size_t)tile * TILE_DW + (4 * (8 * (w + 4 * rd) + cc) + c) * 32 + 4 * tq) =
            v4u{v[rd][0][c], v[rd][1][c], v[rd][2][c], v[rd][3][c]};
    }
  LAS f32x4* red = (LAS f32x4*)smem;
  f32x4 s;
#pragma unroll
  for (int i = 0; i < 4; ++i) {
    float a = 0.f;
#pragma unroll
    for (int rd = 0; rd < 2; ++rd)
#pragma unroll
      for (int c = 0; c < 4; ++c) a += lo2f(v[rd][i][c]) + hi2f(v[rd][i][c]);
    s[i] = sum_hi3(a);
  }
  if (cc == 0) red[w * 8 + tq] = s;
  __syncthreads();
  const f32x4 mean = ((red[tq] + red[8 + tq]) + (red[16 + tq] + red[24 + tq])) * (1.f / D);
#pragma unroll
  for (int i = 0; i < 4; ++i) {
    float a = 0.f;
#pragma unroll
    for (int rd = 0; rd < 2; ++rd)
      if (rd < nrd) {
#pragma unroll
        for (int c = 0; c < 4; ++c) {
          const float d0 = lo2f(v[rd][i][c]) - mean[i], d1 = hi2f(v[rd][i][c]) - mean[i];
          a += d0 * d0 + d1 * d1;
        }
      }
    s[i] = sum_hi3(a);
  }
  if (cc == 0) red[32 + w * 8 + tq] = s;
  __syncthreads();
  if (tid < 8) {
    const f32x4 var = ((red[32 + tq] + red[40 + tq]) + (red[48 + tq] + red[56 + tq])) * (1.f / D);
#pragma unroll
    for (int i = 0; i < 4; ++i) {
      const float sd = rbf(sqrtf(rbf(rbf(var[i]) + 1e-5f)));
      ST[tile * 32 + 4 * tq + i] = f32x2{rbf(mean[i]), 1.f / sd};
    }
  }
}

__device__ __forceinline__ void role_mod(int blk, const u16* c, const u16* wmod, float* MOD, LAS char* smem) {
  const int tid = threadIdx.x, lane = tid & 63, w = __builtin_amdgcn_readfirstlane(tid >> 6);
  const int cg = tid & 7, rr = tid >> 3;
  v4u v[14]; float sc[14];
#pragma unroll
  for (int i = 0; i < 14; ++i) {
    v[i] = *(const v4u*)(wmod + (size_t)(32 * i + rr) * (6 * D) + blk * 64 + cg * 8);
    sc[i] = bf2f(c[32 * i + rr]);
  }
  float acc[8] = {};
#pragma unroll
  for (int i = 0; i < 14; ++i) {
    const float s = rbf(sc[i] / (1.f + __expf(-sc[i])));
#pragma unroll
    for (int k = 0; k < 4; ++k) { acc[2 * k] += s * lo2f(v[i][k]); acc[2 * k + 1] += s * hi2f(v[i][k]); }
  }
#pragma unroll
  for (int k = 0; k < 8; ++k) acc[k] = sum_hi3(acc[k]);
  LAS float* red = (LAS float*)smem;
  if (lane < 8) {
    *(LAS f32x4*)(red + w * 64 + cg * 8) = f32x4{acc[0], acc[1], acc[2], acc[3]};
    *(LAS f32x4*)(red + w * 64 + cg * 8 + 4) = f32x4{acc[4], acc[5], acc[6], acc[7]};
  }
  __syncthreads();
  if (tid < 64) {
    float r = rbf((red[tid] + red[64 + tid]) + (red[128 + tid] + red[192 + tid]));
    const int col = blk * 64 + tid, sec = col / D;
    if (sec == 1 || sec == 4) r = rbf(1.f + r);
    MOD[col] = r;
  }
}

__device__ __forceinline__ void role_w2(int ft, const u16* w2, v4u* W2T, LAS char* smem) {
  const int tid = threadIdx.x, lane = tid & 63, w = __builtin_amdgcn_readfirstlane(tid >> 6);
  v4u v[7];
#pragma unroll
  for (int i = 0; i < 7; ++i) v[i] = *(const v4u*)(w2 + (size_t)ft * 32 * D + (tid + 256 * i) * 8);
#pragma unroll
  for (int i = 0; i < 7; ++i) { const int c = tid + 256 * i, row = c / 56, col = c - row * 56; *(LAS v4u*)(smem + row * 960 + col * 16) = v[i]; }
  __syncthreads();
  const int g = lane >> 4, q = (lane & 15) >> 2, p = lane & 3;
  LAS char* rb = smem + (4 * (g >> 1) + q) * 960 + (16 * (g & 1) + 4 * p) * 2;
#pragma unroll
  for (int i = 0; i < 7; ++i) {
    const int t = w + 4 * i, dt = t >> 1, s = t & 1;
    const v2u a = trread(rb + (16 * s) * 960 + dt * 64), b = trread(rb + (16 * s + 8) * 960 + dt * 64);
    W2T[((((dt >> 1) * 56 + ft) * 2 + (dt & 1)) * 2 + s) * 64 + lane] = v4u{a.x, a.y, b.x, b.y};
  }
}

struct K1Args {
  const u16 *x, *Are, *Aim, *Bre, *Bim, *Cre, *Cim, *logdt, *c, *wmod, *w2;
  char* ws;
};
__global__ void __launch_bounds__(256) k1_prep(K1Args a) {
  __shared__ __attribute__((aligned(16))) char smem_[K1_LDS];
  LAS char* smem = (LAS char*)smem_;
  int b = blockIdx.x;
  if (b < K1_NKG) { role_kgen(b, a.Are, a.Aim, a.Bre, a.Bim, a.Cre, a.Cim, a.logdt, (u16*)(a.ws + WS_KB), smem); return; }
  b -= K1_NKG;
  if (b < K1_NLN) { role_ln(b, a.x, (unsigned*)(a.ws + WS_XP), (f32x2*)(a.ws + WS_ST), smem); return; }
  b -= K1_NLN;
  if (b < K1_NMOD) { role_mod(b, a.c, a.wmod, (float*)(a.ws + WS_MOD), smem); return; }
  b -= K1_NMOD;
  role_w2(b, a.w2, (v4u*)(a.ws + WS_W2T), smem);
}

constexpr int K2_KB = 0, K2_KBP = 4608;
constexpr int K2_HC = 2 * K2_KBP, K2_HCP = 4160;
constexpr int K2_EX = K2_HC + 8 * K2_HCP;
constexpr int K2_LDS = K2_EX + 32768;

__device__ __forceinline__ void role_conv(int p, const char* ws, const u16* Dskip, LAS char* smem) {
  const int tid = threadIdx.x, lane = tid & 63, w = __builtin_amdgcn_readfirstlane(tid >> 6);
  const unsigned* XP = (const unsigned*)(ws + WS_XP);
  const float* MOD = (const float*)(ws + WS_MOD);
  {
    const char* src = ws + WS_KB + (size_t)(2 * p + (w >> 1)) * (L * 2) + (w & 1) * 2048 + lane * 16;
    LAS char* dst = smem + K2_KB + (w >> 1) * K2_KBP + 480 + (w & 1) * 2048;
    __builtin_amdgcn_global_load_lds((const GAS void*)src, (LAS void*)dst, 16, 0, 0);
    __builtin_amdgcn_global_load_lds((const GAS void*)(src + 1024), (LAS void*)(dst + 1024), 16, 0, 0);
  }
  const int t0 = 8 * tid, tm = (tid ? t0 - 4 : 0);
  const v4u xa = *(const v4u*)(XP + (tm >> 5) * TILE_DW + p * 32 + (tm & 31));
  const v4u xb = *(const v4u*)(XP + (t0 >> 5) * TILE_DW + p * 32 + (t0 & 31));
  const v4u xc = *(const v4u*)(XP + (t0 >> 5) * TILE_DW + p * 32 + (t0 & 31) + 4);
  const f32x4* ST4 = (const f32x4*)(ws + WS_ST);
  f32x4 st[6];
#pragma unroll
  for (int i = 0; i < 2; ++i) st[i] = ST4[(tm >> 1) + i];
#pragma unroll
  for (int i = 0; i < 4; ++i) st[2 + i] = ST4[(t0 >> 1) + i];
  const int aq = lane & 15, q = lane >> 4;
  v4u xe[2];
#pragma unroll
  for (int i = 0; i < 2; ++i) { const int l0 = 256 * (2 * w + i) + 16 * aq + 4 * q; xe[i] = *(const v4u*)(XP + (l0 >> 5) * TILE_DW + p * 32 + (l0 & 31)); }
  const f32x2 sa = *(const f32x2*)(MOD + 2 * p), ca = *(const f32x2*)(MOD + D + 2 * p), ga = *(const f32x2*)(MOD + 2 * D + 2 * p);
  const unsigned dku = *(const unsigned*)(Dskip + 2 * p);
  if (tid < 60) *(LAS v4u*)(smem + K2_KB + (tid / 30) * K2_KBP + (tid % 30) * 16) = v4u{0u, 0u, 0u, 0u};
  if (tid >= 64 && tid < 80) *(LAS v4u*)(smem + K2_HC + ((tid - 64) >> 1) * K2_HCP + 4096 + (tid & 1) * 16) = v4u{0u, 0u, 0u, 0u};
  float h0[12], h1[12];
#pragma unroll
  for (int i = 1; i < 12; ++i) {
    const unsigned u = i < 4 ? xa[i] : (i < 8 ? xb[i - 4] : xc[i - 8]);
    const float m = st[i >> 1][(i & 1) * 2], r = st[i >> 1][(i & 1) * 2 + 1];
    h0[i] = (lo2f(u) - m) * r * ca.x + sa.x;
    h1[i] = (hi2f(u) - m) * r * ca.y + sa.y;
    if (i < 4 && tid == 0) { h0[i] = 0.f; h1[i] = 0.f; }
  }
  {
    LAS char* dst = smem + K2_HC + (2040 - t0) * 2;
#pragma unroll
    for (int sg = 0; sg < 4; ++sg) {
      *(LAS v4u*)(dst + sg * K2_HCP) = v4u{pack2(h0[11 - sg], h0[10 - sg]), pack2(h0[9 - sg], h0[8 - sg]), pack2(h0[7 - sg], h0[6 - sg]), pack2(h0[5 - sg], h0[4 - sg])};
      *(LAS v4u*)(dst + (4 + sg) * K2_HCP) = v4u{pack2(h1[11 - sg], h1[10 - sg]), pack2(h1[9 - sg], h1[8 - sg]), pack2(h1[7 - sg], h1[6 - sg]), pack2(h1[5 - sg], h1[4 - sg])};
    }
  }
  __syncthreads();
  const int ch = w >> 1, hf = w & 1;
  const int sg = (3 - aq) & 3;
  LAS char* fb = smem + K2_HC + (ch * 4 + sg) * K2_HCP + 2 * (15 - aq + 8 * q - sg) + 256 * (1 - hf);
  LAS char* gb = smem + K2_KB + ch * K2_KBP + 32 * aq + 16 * q + 256 * (1 - hf);
  f32x4 acc[8];
#pragma unroll
  for (int c = 0; c < 8; ++c) acc[c] = f32x4{0.f, 0.f, 0.f, 0.f};
#pragma unroll
  for (int t = 0; t < 4; ++t) {
    v4u fr[8], gr[8];
#pragma unroll
    for (int m = 0; m < 8; ++m) {
      const v2u a = *(const LAS v2u*)(fb + 64 * (59 - t - 8 * m)), b = *(const LAS v2u*)(fb + 64 * (59 - t - 8 * m) + 8);
      fr[m] = v4u{a.x, a.y, b.x, b.y};
      gr[m] = *(const LAS v4u*)(gb + 192 + 512 * m - 64 * t);
    }
#pragma unroll
    for (int s = 0; s < 8; ++s)
#pragma unroll
      for (int o = 0; o <= s; ++o) {
        if (s + o < 8) acc[s + o] = mfma16(fr[s], gr[o], acc[s + o]);
        if (o < s && s + o < 8) acc[s + o] = mfma16(fr[o], gr[s], acc[s + o]);
      }
  }
  LAS f32x4* ex = (LAS f32x4*)(smem + K2_EX);
#pragma unroll
  for (int c = 0; c < 8; ++c) ex[(w * 8 + c) * 64 + lane] = acc[c];
  __syncthreads();
  unsigned* X1P = (unsigned*)(ws + WS_X1P);
  const float dk0 = lo2f(dku), dk1 = hi2f(dku);
#pragma unroll
  for (int i = 0; i < 2; ++i) {
    const int c = 2 * w + i, l0 = 256 * c + 16 * aq + 4 * q;
    const f32x4 y0 = ex[(0 * 8 + c) * 64 + lane] + ex[(1 * 8 + c) * 64 + lane];
    const f32x4 y1 = ex[(2 * 8 + c) * 64 + lane] + ex[(3 * 8 + c) * 64 + lane];
    const v2u ha = *(const LAS v2u*)(smem + K2_HC + (2044 - l0) * 2), hb = *(const LAS v2u*)(smem + K2_HC + 4 * K2_HCP + (2044 - l0) * 2);
    const float hh0[4] = {hi2f(ha.y), lo2f(ha.y), hi2f(ha.x), lo2f(ha.x)}, hh1[4] = {hi2f(hb.y), lo2f(hb.y), hi2f(hb.x), lo2f(hb.x)};
    v4u o;
#pragma unroll
    for (int k = 0; k < 4; ++k)
      o[k] = pack2(lo2f(xe[i][k]) + ga.x * (y0[k] + hh0[k] * dk0), hi2f(xe[i][k]) + ga.y * (y1[k] + hh1[k] * dk1));
    *(v4u*)(X1P + (l0 >> 5) * TILE_DW + p * 32 + (l0 & 31)) = o;
  }
}

__device__ __forceinline__ void role_w1(int blk, const u16* w1, char* ws, LAS char* smem) {
  const int tid = threadIdx.x, lane = tid & 63, w = __builtin_amdgcn_readfirstlane(tid >> 6);
  const int cg = tid & 7, rr = tid >> 3;
  const float* MOD = (const float*)(ws + WS_MOD);
  v4u v[14]; float sb[14], cb[14];
#pragma unroll
  for (int i = 0; i < 14; ++i) {
    v[i] = *(const v4u*)(w1 + (size_t)(32 * i + rr) * F + blk * 64 + cg * 8);
    sb[i] = MOD[3 * D + 32 * i + rr]; cb[i] = MOD[4 * D + 32 * i + rr];
  }
  float cs[8] = {}, bi[8] = {};
#pragma unroll
  for (int i = 0; i < 14; ++i) {
    v4u o;
#pragma unroll
    for (int k = 0; k < 4; ++k) {
      const float a = lo2f(v[i][k]), b = hi2f(v[i][k]);
      o[k] = pack2(a * cb[i], b * cb[i]);
      cs[2 * k] += lo2f(o[k]); cs[2 * k + 1] += hi2f(o[k]);
      bi[2 * k] += sb[i] * a; bi[2 * k + 1] += sb[i] * b;
    }
    *(LAS v4u*)(smem + (32 * i + rr) * 144 + cg * 16) = o;
  }
#pragma unroll
  for (int k = 0; k < 8; ++k) { cs[k] = sum_hi3(cs[k]); bi[k] = sum_hi3(bi[k]); }
  LAS float* red = (LAS float*)(smem + 448 * 144);
  if (lane < 8) {
    *(LAS f32x4*)(red + w * 64 + cg * 8) = f32x4{cs[0], cs[1], cs[2], cs[3]};
    *(LAS f32x4*)(red + w * 64 + cg * 8 + 4) = f32x4{cs[4], cs[5], cs[6], cs[7]};
    *(LAS f32x4*)(red + 256 + w * 64 + cg * 8) = f32x4{bi[0], bi[1], bi[2], bi[3]};
    *(LAS f32x4*)(red + 256 + w * 64 + cg * 8 + 4) = f32x4{bi[4], bi[5], bi[6], bi[7]};
  }
  __syncthreads();
  if (tid < 128) {
    const int a = tid >> 6, t = tid & 63;
    const float r = (red[a * 256 + t] + red[a * 256 + 64 + t]) + (red[a * 256 + 128 + t] + red[a * 256 + 192 + t]);
    ((float*)(ws + (a ? WS_BI : WS_CS)))[blk * 64 + t] = r;
  }
  const int g = lane >> 4, q = (lane & 15) >> 2, p = lane & 3;
  LAS char* rb = smem + (2 * (g >> 1) + (q & 1) + 4 * (q >> 1)) * 144 + (16 * (g & 1) + 4 * p) * 2;
  v4u* W1T = (v4u*)(ws + WS_W1T);
#pragma unroll
  for (int i = 0; i < 14; ++i) {
    const int t = w + 4 * i, f2 = t & 1, s = t >> 1;
    const v2u a = trread(rb + (16 * s) * 144 + f2 * 64), b = trread(rb + (16 * s + 8) * 144 + f2 * 64);
    W1T[((blk * 2 + f2) * 28 + s) * 64 + lane] = v4u{a.x, a.y, b.x, b.y};
  }
}

__global__ void __launch_bounds__(256) k2_conv(char* ws, const u16* Dskip, const u16* w1) {
  __shared__ __attribute__((aligned(16))) char smem_[K2_LDS];
  LAS char* smem = (LAS char*)smem_;
  if (blockIdx.x < NPAIR) role_conv(blockIdx.x, ws, Dskip, smem);
  else role_w1(blockIdx.x - NPAIR, w1, ws, smem);
}

constexpr int K3_LDS = 112 * 1024 + 1024;
__global__ void __launch_bounds__(256) k3_gemm1(char* ws) {
  __shared__ __attribute__((aligned(16))) char smem_[K3_LDS];
  LAS char* smem = (LAS char*)smem_;
  const int tid = threadIdx.x, lane = tid & 63, w = __builtin_amdgcn_readfirstlane(tid >> 6);
  const int xcd = blockIdx.x & 7, r = blockIdx.x >> 3, tb = 2 * xcd + (r >= 14), fb = r >= 14 ? r - 14 : r;
  const unsigned* xsrc = (const unsigned*)(ws + WS_X1P) + (size_t)(tb * 4 + w) * TILE_DW + lane;
  v4u xf[28];
#pragma unroll
  for (int s = 0; s < 28; ++s)
#pragma unroll
    for (int i = 0; i < 4; ++i) xf[s][i] = xsrc[(4 * s + i) * 64];
  {
    const char* src = ws + WS_W1T + (size_t)fb * (112 * 1024) + w * 1024 + lane * 16;
#pragma unroll
    for (int i = 0; i < 28; ++i)
      __builtin_amdgcn_global_load_lds((const GAS void*)(src + i * 4096), (LAS void*)(smem + w * 1024 + i * 4096), 16, 0, 0);
  }
  {
    const float v = ((const float*)(ws + (tid >> 7 ? WS_BI : WS_CS)))[fb * 128 + (tid & 127)];
    ((LAS float*)(smem + 112 * 1024))[tid] = v;
  }
  float s1 = 0.f, s2 = 0.f;
#pragma unroll
  for (int s = 0; s < 28; ++s)
#pragma unroll
    for (int i = 0; i < 4; ++i) { s1 = dot2(xf[s][i], 0x3f803f80u, s1); s2 = dot2(xf[s][i], xf[s][i], s2); }
  s1 = sum_x32(s1) * (1.f / D); s2 = sum_x32(s2) * (1.f / D);
  const float var = fmaxf(s2 - s1 * s1, 0.f);
  const float rs = 1.f / rbf(sqrtf(rbf(rbf(var) + 1e-5f))), nmr = -rbf(s1) * rs;
  __syncthreads();
  const int h = lane >> 5;
  v4u* HT = (v4u*)(ws + WS_HT) + (size_t)(tb * 2 + (w >> 1)) * (56 * 256) + (w & 1) * 128 + lane;
#pragma unroll
  for (int ft = 0; ft < 4; ++ft) {
    f32x16 acc = {};
#pragma unroll
    for (int s = 0; s < 28; ++s) acc = mfma32(*(const LAS v4u*)(smem + (ft * 28 + s) * 1024 + lane * 16), xf[s], acc);
    unsigned o[8];
#pragma unroll
    for (int g = 0; g < 4; ++g) {
      const f32x4 cs = *(const LAS f32x4*)(smem + 112 * 1024 + (ft * 32 + 8 * g + 4 * h) * 4);
      const f32x4 bi = *(const LAS f32x4*)(smem + 112 * 1024 + 512 + (ft * 32 + 8 * g + 4 * h) * 4);
      float e[4];
#pragma unroll
      for (int i = 0; i < 4; ++i) {
        const float pre = acc[4 * g + i] * rs + (cs[i] * nmr + bi[i]);
        e[i] = pre * __builtin_amdgcn_rcpf(1.f + __builtin_amdgcn_exp2f(pre * -1.4426950408889634f));
      }
      o[2 * g] = pack2(e[0], e[1]); o[2 * g + 1] = pack2(e[2], e[3]);
    }
    HT[(fb * 4 + ft) * 256] = v4u{o[0], o[1], o[2], o[3]};
    HT[(fb * 4 + ft) * 256 + 64] = v4u{o[4], o[5], o[6], o[7]};
  }
}

constexpr int K4_LDS = 65536;
__global__ void __launch_bounds__(256) k4_gemm2(const char* ws, u16* out) {
  __shared__ __attribute__((aligned(16))) char smem_[K4_LDS];
  LAS char* smem = (LAS char*)smem_;
  const int tid = threadIdx.x, lane = tid & 63, w = __builtin_amdgcn_readfirstlane(tid >> 6);
  const int xcd = blockIdx.x & 7, r = blockIdx.x >> 3, tb = 4 * xcd + r / 7, cb = r % 7;
  const v4u* hp = (const v4u*)(ws + WS_HT) + ((size_t)tb * 56 + 14 * w) * 256 + lane;
  const v4u* wp = (const v4u*)(ws + WS_W2T) + ((size_t)cb * 56 + 14 * w) * 256 + lane;
  constexpr int WIN = 6;
  v4u hb[WIN][4], wb[WIN][4];
#pragma unroll
  for (int i = 0; i < WIN; ++i)
#pragma unroll
    for (int k = 0; k < 4; ++k) { hb[i][k] = hp[i * 256 + k * 64]; wb[i][k] = wp[i * 256 + k * 64]; }
  const int h = lane >> 5, tok = tb * 64 + (w & 1) * 32 + (lane & 31), d0 = cb * 64 + (w >> 1) * 32;
  unsigned xr[8]; f32x4 gbv[4];
#pragma unroll
  for (int g = 0; g < 4; ++g) {
    const unsigned* xs = (const unsigned*)(ws + WS_X1P) + (size_t)(tok >> 5) * TILE_DW + ((d0 + 8 * g + 4 * h) >> 1) * 32 + (tok & 31);
    xr[2 * g] = xs[0]; xr[2 * g + 1] = xs[32];
    gbv[g] = *(const f32x4*)((const float*)(ws + WS_MOD) + 5 * D + d0 + 8 * g + 4 * h);
  }
  __builtin_amdgcn_sched_barrier(0);
  f32x16 acc[2][2] = {};
#pragma unroll
  for (int i = 0; i < 14; ++i) {
    v4u hc[4], wc[4];
#pragma unroll
    for (int k = 0; k < 4; ++k) { hc[k] = hb[i % WIN][k]; wc[k] = wb[i % WIN][k]; }
#pragma unroll
    for (int s = 0; s < 2; ++s)
#pragma unroll
      for (int a = 0; a < 2; ++a)
#pragma unroll
        for (int b = 0; b < 2; ++b) acc[a][b] = mfma32(wc[a * 2 + s], hc[b * 2 + s], acc[a][b]);
    if (i + WIN < 14) {
#pragma unroll
      for (int k = 0; k < 4; ++k) { hb[i % WIN][k] = hp[(i + WIN) * 256 + k * 64]; wb[i % WIN][k] = wp[(i + WIN) * 256 + k * 64]; }
    }
    __builtin_amdgcn_sched_barrier(0);
  }
  LAS f32x4* ex = (LAS f32x4*)smem;
#pragma unroll
  for (int a = 0; a < 2; ++a)
#pragma unroll
    for (int b = 0; b < 2; ++b)
#pragma unroll
      for (int g = 0; g < 4; ++g)
        ex[((w * 4 + a * 2 + b) * 4 + g) * 64 + lane] = f32x4{acc[a][b][4 * g], acc[a][b][4 * g + 1], acc[a][b][4 * g + 2], acc[a][b][4 * g + 3]};
  __syncthreads();
  unsigned o[8];
#pragma unroll
  for (int g = 0; g < 4; ++g) {
    const f32x4 v = (ex[((0 * 4 + w) * 4 + g) * 64 + lane] + ex[((1 * 4 + w) * 4 + g) * 64 + lane]) +
                    (ex[((2 * 4 + w) * 4 + g) * 64 + lane] + ex[((3 * 4 + w) * 4 + g) * 64 + lane]);
    o[2 * g] = pack2(lo2f(xr[2 * g]) + gbv[g].x * v.x, hi2f(xr[2 * g]) + gbv[g].y * v.y);
    o[2 * g + 1] = pack2(lo2f(xr[2 * g + 1]) + gbv[g].z * v.z, hi2f(xr[2 * g + 1]) + gbv[g].w * v.w);
  }
#pragma unroll
  for (int g = 0; g < 4; g += 2) {
    auto r0 = __builtin_amdgcn_permlane32_swap(o[2 * g], o[2 * g + 2], false, false);
    auto r1 = __builtin_amdgcn_permlane32_swap(o[2 * g + 1], o[2 * g + 3], false, false);
    *(v4u*)(out + (size_t)tok * D + d0 + 8 * g + 8 * h) = v4u{(unsigned)r0[0], (unsigned)r1[0], (unsigned)r0[1], (unsigned)r1[1]};
  }
}

extern "C" void kernel_launch(void* const* d_in, const int* in_sizes, int n_in, void* d_out, int out_size, void* d_ws,
                              size_t ws_size, hipStream_t stream) {
  K1Args a;
  a.x = (const u16*)d_in[0]; a.Are = (const u16*)d_in[1]; a.Aim = (const u16*)d_in[2]; a.Bre = (const u16*)d_in[3];
  a.Bim = (const u16*)d_in[4]; a.Cre = (const u16*)d_in[5]; a.Cim = (const u16*)d_in[6]; a.logdt = (const u16*)d_in[7];
  a.c = (const u16*)d_in[12]; a.wmod = (const u16*)d_in[13]; a.w2 = (const u16*)d_in[11]; a.ws = (char*)d_ws;
  k1_prep<<<K1_NKG + K1_NLN + K1_NMOD + K1_NW2, 256, 0, stream>>>(a);
  k2_conv<<<NPAIR + 28, 256, 0, stream>>>((char*)d_ws, (const u16*)d_in[8], (const u16*)d_in[10]);
  k3_gemm1<<<224, 256, 0, stream>>>((char*)d_ws);
  k4_gemm2<<<224, 256, 0, stream>>>((const char*)d_ws, (u16*)d_out);
}
```

```cpp
#include <hip/hip_runtime.h>
#include <hip/hip_bf16.h>
#include <stdint.h>

#define GAS __attribute__((address_space(1)))
#define LAS __attribute__((address_space(3)))
typedef unsigned v4u __attribute__((ext_vector_type(4)));
typedef unsigned v2u __attribute__((ext_vector_type(2)));
typedef float f32x2 __attribute__((ext_vector_type(2)));
typedef float f32x4 __attribute__((ext_vector_type(4)));
typedef float f32x16 __attribute__((ext_vector_type(16)));
typedef __bf16 bf16x8 __attribute__((ext_vector_type(8)));
typedef __bf16 bf16x2 __attribute__((ext_vector_type(2)));
typedef short s16x4 __attribute__((ext_vector_type(4)));
typedef unsigned short u16;

constexpr int L = 2048, D = 448, NM = 256, F = 1792;
constexpr int NPAIR = D / 2;
constexpr int TILE_DW = NPAIR * 32;

constexpr size_t WS_XP = 0;
constexpr size_t WS_X1P = WS_XP + (size_t)L * D * 2;
constexpr size_t WS_KB = WS_X1P + (size_t)L * D * 2;
constexpr size_t WS_ST = WS_KB + (size_t)L * D * 2;
constexpr size_t WS_MOD = WS_ST + (size_t)L * 8;
constexpr size_t WS_CS = WS_MOD + 16384;
constexpr size_t WS_BI = WS_CS + 8192;
constexpr size_t WS_W1T = WS_BI + 8192;
constexpr size_t WS_W2T = WS_W1T + (size_t)D * F * 2;
constexpr size_t WS_HT = WS_W2T + (size_t)D * F * 2;

constexpr size_t WS_STAMP = (size_t)128 << 20;
#define STAMP(ws, k, i) do { if (threadIdx.x == 0) { unsigned long long t_ = __builtin_amdgcn_s_memrealtime(); \
  ((unsigned long long*)((char*)(ws) + WS_STAMP))[((k) * 2 + (i)) * 1024 + blockIdx.x] = t_; } } while (0)
__device__ __forceinline__ float bf2f(u16 b) { return __builtin_bit_cast(float, (unsigned)b << 16); }
__device__ __forceinline__ float lo2f(unsigned u) { return __builtin_bit_cast(float, u << 16); }
__device__ __forceinline__ float hi2f(unsigned u) { return __builtin_bit_cast(float, u & 0xffff0000u); }
__device__ __forceinline__ unsigned pack2(float lo, float hi) {
  bf16x2 v; v.x = (__bf16)lo; v.y = (__bf16)hi; return __builtin_bit_cast(unsigned, v);
}
__device__ __forceinline__ float rbf(float f) { return lo2f(pack2(f, 0.f)); }
__device__ __forceinline__ f32x16 mfma32(v4u a, v4u b, f32x16 c) {
  return __builtin_amdgcn_mfma_f32_32x32x16_bf16(__builtin_bit_cast(bf16x8, a), __builtin_bit_cast(bf16x8, b), c, 0, 0, 0);
}
__device__ __forceinline__ f32x4 mfma16(v4u a, v4u b, f32x4 c) {
  return __builtin_amdgcn_mfma_f32_16x16x32_bf16(__builtin_bit_cast(bf16x8, a), __builtin_bit_cast(bf16x8, b), c, 0, 0, 0);
}
__device__ __forceinline__ float dot2(unsigned a, unsigned b, float c) {
  return __builtin_amdgcn_fdot2_f32_bf16(__builtin_bit_cast(bf16x2, a), __builtin_bit_cast(bf16x2, b), c, false);
}
__device__ __forceinline__ v2u trread(LAS char* p) {
  return __builtin_bit_cast(v2u, __builtin_amdgcn_ds_read_tr16_b64_v4i16((LAS s16x4*)p));
}
__device__ __forceinline__ float sum_x8(float v) {
  return v + __builtin_bit_cast(float, __builtin_amdgcn_update_dpp(0, __builtin_bit_cast(int, v), 0x128, 0xf, 0xf, false));
}
__device__ __forceinline__ float sum_x16(float v) {
  const unsigned u = __builtin_bit_cast(unsigned, v);
  auto r = __builtin_amdgcn_permlane16_swap(u, u, false, false);
  return __builtin_bit_cast(float, (unsigned)r[0]) + __builtin_bit_cast(float, (unsigned)r[1]);
}
__device__ __forceinline__ float sum_x32(float v) {
  const unsigned u = __builtin_bit_cast(unsigned, v);
  auto r = __builtin_amdgcn_permlane32_swap(u, u, false, false);
  return __builtin_bit_cast(float, (unsigned)r[0]) + __builtin_bit_cast(float, (unsigned)r[1]);
}
__device__ __forceinline__ float sum_hi3(float v) { return sum_x32(sum_x16(sum_x8(v))); }

struct cplx { float re, im; };
__device__ __forceinline__ cplx cmul(cplx a, cplx b) {
  cplx r; r.re = a.re * b.re - a.im * b.im; r.im = a.re * b.im + a.im * b.re; return r;
}
__device__ __forceinline__ cplx cexp_l(float al, float th, float l) {
  const float mag = __builtin_amdgcn_exp2f(al * l);
  const float ph = __builtin_amdgcn_fractf(th * l);
  cplx r; r.re = mag * __builtin_amdgcn_cosf(ph); r.im = mag * __builtin_amdgcn_sinf(ph); return r;
}

constexpr int K1_NKG = D, K1_NLN = 64, K1_NMOD = 42, K1_NW2 = 56;
constexpr int K1_LDS = 4 * 32 * 272;

__device__ __forceinline__ void role_kgen(int d, const u16* Are, const u16* Aim, const u16* Bre, const u16* Bim,
                                          const u16* Cre, const u16* Cim, const u16* logdt, u16* KB, LAS char* smem) {
  const int tid = threadIdx.x, lane = tid & 63, w = __builtin_amdgcn_readfirstlane(tid >> 6);
  const int idx = d * NM + tid;
  const float are = bf2f(Are[idx]), aim = bf2f(Aim[idx]), bre = bf2f(Bre[idx]), bim = bf2f(Bim[idx]);
  const float cre = bf2f(Cre[idx]), cim = bf2f(Cim[idx]);
  const float dt = rbf(expf(bf2f(logdt[d])));
  const float al = (dt * are) * 1.4426950408889634f, th = (dt * aim) * 0.15915494309189535f;
  const cplx W = cmul(cplx{cre, cim}, cplx{dt * bre, dt * bim});
  const cplx E1 = cexp_l(al, th, 1.f), E8 = cexp_l(al, th, 8.f), E16 = cexp_l(al, th, 16.f), E256 = cexp_l(al, th, 256.f);
  const cplx E2 = cmul(E1, E1), E4 = cmul(E2, E2);
  LAS char* base = smem + w * (32 * 272);
  LAS unsigned* wr = (LAS unsigned*)(base + lane * 4);
  const LAS v4u* rd = (const LAS v4u*)(base + (lane & 31) * 272 + (lane >> 5) * 16);
  {
    cplx p0{1.f, 0.f}, p1 = E8;
#pragma unroll
    for (int j = 0; j < 32; j += 2) {
      wr[j * 68] = pack2(p0.re, p0.im);
      wr[(j + 1) * 68] = pack2(p1.re, p1.im);
      if (j < 30) { p0 = cmul(p0, E16); p1 = cmul(p1, E16); }
    }
  }
  v4u bf[8];
#pragma unroll
  for (int s = 0; s < 8; ++s) bf[s] = rd[2 * s];
  {
    auto put = [&](int row, cplx v) { wr[row * 68] = pack2(v.re, -v.im); };
    cplx v = W;
#pragma unroll
    for (int a = 0; a < 8; ++a) { put(a, v); if (a < 7) v = cmul(v, E1); }
    cplx q = cmul(W, E256);
    put(8, q); put(9, cmul(q, E2));
    { const cplx t = cmul(q, E4); put(10, t); put(12, t); put(13, cmul(t, E2)); }
    put(14, cmul(q, E8));
    q = cmul(q, E256);
    put(16, q); { const cplx t = cmul(q, E4); put(17, t); put(20, t); } put(21, cmul(q, E8));
    q = cmul(q, E256);
    put(18, q); { const cplx t = cmul(q, E4); put(19, t); put(22, t); } put(23, cmul(q, E8));
#pragma unroll
    for (int b = 0; b < 4; ++b) { q = cmul(q, E256); put(24 + b, q); put(28 + b, cmul(q, E8)); }
  }
  f32x16 acc = {};
#pragma unroll
  for (int s = 0; s < 8; ++s) acc = mfma32(rd[2 * s], bf[s], acc);
  __syncthreads();
  LAS f32x4* ex = (LAS f32x4*)smem;
#pragma unroll
  for (int g = 0; g < 4; ++g) ex[(w * 4 + g) * 64 + lane] = f32x4{acc[4 * g], acc[4 * g + 1], acc[4 * g + 2], acc[4 * g + 3]};
  __syncthreads();
  const f32x4 v = (ex[(0 * 4 + w) * 64 + lane] + ex[(1 * 4 + w) * 64 + lane]) + (ex[(2 * 4 + w) * 64 + lane] + ex[(3 * 4 + w) * 64 + lane]);
  const int j = lane & 31, h = lane >> 5;
  v2u* out = (v2u*)(KB + (size_t)d * L + 8 * j + 4 * h);
  if (w == 0) {
    out[0] = v2u{pack2(v.x, v.y), pack2(v.z, v.w)};
  } else if (w == 1) {
    out[64] = v2u{pack2(v.x, v.x), pack2(v.y, v.z)};
  } else if (w == 2) {
    out[128] = v2u{pack2(v.x, v.x), pack2(h ? v.y : v.x, v.y)};
    out[192] = v2u{pack2(v.z, v.z), pack2(h ? v.w : v.z, v.w)};
  } else {
#pragma unroll
    for (int b = 0; b < 4; ++b) {
      const float vb = v[b];
      const unsigned u = __builtin_bit_cast(unsigned, vb);
      auto r = __builtin_amdgcn_permlane32_swap(u, u, false, false);
      const float o = __builtin_bit_cast(float, (unsigned)r[0]);
      out[256 + 64 * b] = v2u{pack2((j & 1) ? vb : o, vb), pack2(vb, vb)};
    }
  }
}

__device__ __forceinline__ void role_ln(int tile, const u16* x, unsigned* XP, f32x2* ST, LAS char* smem) {
  const int tid = threadIdx.x, lane = tid & 63, w = __builtin_amdgcn_readfirstlane(tid >> 6);
  const int tq = lane & 7, cc = lane >> 3;
  const int nrd = (w < 3) ? 2 : 1;
  v4u v[2][4];
#pragma unroll
  for (int rd = 0; rd < 2; ++rd)
#pragma unroll
    for (int i = 0; i < 4; ++i) {
      v[rd][i] = v4u{0u, 0u, 0u, 0u};
      if (rd < nrd) v[rd][i] = *(const v4u*)(x + (size_t)(tile * 32 + 4 * tq + i) * D + (8 * (w + 4 * rd) + cc) * 8);
    }
#pragma unroll
  for (int rd = 0; rd < 2; ++rd)
    if (rd < nrd) {
#pragma unroll
      for (int c = 0; c < 4; ++c)
        *(v4u*)(XP + (size_t)tile * TILE_DW + (4 * (8 * (w + 4 * rd) + cc) + c) * 32 + 4 * tq) =
            v4u{v[rd][0][c], v[rd][1][c], v[rd][2][c], v[rd][3][c]};
    }
  LAS f32x4* red = (LAS f32x4*)smem;
  f32x4 s;
#pragma unroll
  for (int i = 0; i < 4; ++i) {
    float a = 0.f;
#pragma unroll
    for (int rd = 0; rd < 2; ++rd)
#pragma unroll
      for (int c = 0; c < 4; ++c) a += lo2f(v[rd][i][c]) + hi2f(v[rd][i][c]);
    s[i] = sum_hi3(a);
  }
  if (cc == 0) red[w * 8 + tq] = s;
  __syncthreads();
  const f32x4 mean = ((red[tq] + red[8 + tq]) + (red[16 + tq] + red[24 + tq])) * (1.f / D);
#pragma unroll
  for (int i = 0; i < 4; ++i) {
    float a = 0.f;
#pragma unroll
    for (int rd = 0; rd < 2; ++rd)
      if (rd < nrd) {
#pragma unroll
        for (int c = 0; c < 4; ++c) {
          const float d0 = lo2f(v[rd][i][c]) - mean[i], d1 = hi2f(v[rd][i][c]) - mean[i];
          a += d0 * d0 + d1 * d1;
        }
      }
    s[i] = sum_hi3(a);
  }
  if (cc == 0) red[32 + w * 8 + tq] = s;
  __syncthreads();
  if (tid < 8) {
    const f32x4 var = ((red[32 + tq] + red[40 + tq]) + (red[48 + tq] + red[56 + tq])) * (1.f / D);
#pragma unroll
    for (int i = 0; i < 4; ++i) {
      const float sd = rbf(sqrtf(rbf(rbf(var[i]) + 1e-5f)));
      ST[tile * 32 + 4 * tq + i] = f32x2{rbf(mean[i]), 1.f / sd};
    }
  }
}

__device__ __forceinline__ void role_mod(int blk, const u16* c, const u16* wmod, float* MOD, LAS char* smem) {
  const int tid = threadIdx.x, lane = tid & 63, w = __builtin_amdgcn_readfirstlane(tid >> 6);
  const int cg = tid & 7, rr = tid >> 3;
  v4u v[14]; float sc[14];
#pragma unroll
  for (int i = 0; i < 14; ++i) {
    v[i] = *(const v4u*)(wmod + (size_t)(32 * i + rr) * (6 * D) + blk * 64 + cg * 8);
    sc[i] = bf2f(c[32 * i + rr]);
  }
  float acc[8] = {};
#pragma unroll
  for (int i = 0; i < 14; ++i) {
    const float s = rbf(sc[i] / (1.f + __expf(-sc[i])));
#pragma unroll
    for (int k = 0; k < 4; ++k) { acc[2 * k] += s * lo2f(v[i][k]); acc[2 * k + 1] += s * hi2f(v[i][k]); }
  }
#pragma unroll
  for (int k = 0; k < 8; ++k) acc[k] = sum_hi3(acc[k]);
  LAS float* red = (LAS float*)smem;
  if (lane < 8) {
    *(LAS f32x4*)(red + w * 64 + cg * 8) = f32x4{acc[0], acc[1], acc[2], acc[3]};
    *(LAS f32x4*)(red + w * 64 + cg * 8 + 4) = f32x4{acc[4], acc[5], acc[6], acc[7]};
  }
  __syncthreads();
  if (tid < 64) {
    float r = rbf((red[tid] + red[64 + tid]) + (red[128 + tid] + red[192 + tid]));
    const int col = blk * 64 + tid, sec = col / D;
    if (sec == 1 || sec == 4) r = rbf(1.f + r);
    MOD[col] = r;
  }
}

__device__ __forceinline__ void role_w2(int ft, const u16* w2, v4u* W2T, LAS char* smem) {
  const int tid = threadIdx.x, lane = tid & 63, w = __builtin_amdgcn_readfirstlane(tid >> 6);
  v4u v[7];
#pragma unroll
  for (int i = 0; i < 7; ++i) v[i] = *(const v4u*)(w2 + (size_t)ft * 32 * D + (tid + 256 * i) * 8);
#pragma unroll
  for (int i = 0; i < 7; ++i) { const int c = tid + 256 * i, row = c / 56, col = c - row * 56; *(LAS v4u*)(smem + row * 960 + col * 16) = v[i]; }
  __syncthreads();
  const int g = lane >> 4, q = (lane & 15) >> 2, p = lane & 3;
  LAS char* rb = smem + (4 * (g >> 1) + q) * 960 + (16 * (g & 1) + 4 * p) * 2;
#pragma unroll
  for (int i = 0; i < 7; ++i) {
    const int t = w + 4 * i, dt = t >> 1, s = t & 1;
    const v2u a = trread(rb + (16 * s) * 960 + dt * 64), b = trread(rb + (16 * s + 8) * 960 + dt * 64);
    W2T[((((dt >> 1) * 56 + ft) * 2 + (dt & 1)) * 2 + s) * 64 + lane] = v4u{a.x, a.y, b.x, b.y};
  }
}

struct K1Args {
  const u16 *x, *Are, *Aim, *Bre, *Bim, *Cre, *Cim, *logdt, *c, *wmod, *w2;
  char* ws;
};
__global__ void __launch_bounds__(256) k1_prep(K1Args a) {
  __shared__ __attribute__((aligned(16))) char smem_[K1_LDS];
  LAS char* smem = (LAS char*)smem_;
  int b = blockIdx.x;
  STAMP(a.ws, 0, 0);
  if (b < K1_NKG) role_kgen(b, a.Are, a.Aim, a.Bre, a.Bim, a.Cre, a.Cim, a.logdt, (u16*)(a.ws + WS_KB), smem);
  else if (b < K1_NKG + K1_NLN) role_ln(b - K1_NKG, a.x, (unsigned*)(a.ws + WS_XP), (f32x2*)(a.ws + WS_ST), smem);
  else if (b < K1_NKG + K1_NLN + K1_NMOD) role_mod(b - K1_NKG - K1_NLN, a.c, a.wmod, (float*)(a.ws + WS_MOD), smem);
  else role_w2(b - K1_NKG - K1_NLN - K1_NMOD, a.w2, (v4u*)(a.ws + WS_W2T), smem);
  STAMP(a.ws, 0, 1);
}

constexpr int K2_KB = 0, K2_KBP = 4608;
constexpr int K2_HC = 2 * K2_KBP, K2_HCP = 4160;
constexpr int K2_EX = K2_HC + 8 * K2_HCP;
constexpr int K2_LDS = K2_EX + 32768;

__device__ __forceinline__ void role_conv(int p, const char* ws, const u16* Dskip, LAS char* smem) {
  const int tid = threadIdx.x, lane = tid & 63, w = __builtin_amdgcn_readfirstlane(tid >> 6);
  const unsigned* XP = (const unsigned*)(ws + WS_XP);
  const float* MOD = (const float*)(ws + WS_MOD);
  {
    const char* src = ws + WS_KB + (size_t)(2 * p + (w >> 1)) * (L * 2) + (w & 1) * 2048 + lane * 16;
    LAS char* dst = smem + K2_KB + (w >> 1) * K2_KBP + 480 + (w & 1) * 2048;
    __builtin_amdgcn_global_load_lds((const GAS void*)src, (LAS void*)dst, 16, 0, 0);
    __builtin_amdgcn_global_load_lds((const GAS void*)(src + 1024), (LAS void*)(dst + 1024), 16, 0, 0);
  }
  const int t0 = 8 * tid, tm = (tid ? t0 - 4 : 0);
  const v4u xa = *(const v4u*)(XP + (tm >> 5) * TILE_DW + p * 32 + (tm & 31));
  const v4u xb = *(const v4u*)(XP + (t0 >> 5) * TILE_DW + p * 32 + (t0 & 31));
  const v4u xc = *(const v4u*)(XP + (t0 >> 5) * TILE_DW + p * 32 + (t0 & 31) + 4);
  const f32x4* ST4 = (const f32x4*)(ws + WS_ST);
  f32x4 st[6];
#pragma unroll
  for (int i = 0; i < 2; ++i) st[i] = ST4[(tm >> 1) + i];
#pragma unroll
  for (int i = 0; i < 4; ++i) st[2 + i] = ST4[(t0 >> 1) + i];
  const int aq = lane & 15, q = lane >> 4;
  v4u xe[2];
#pragma unroll
  for (int i = 0; i < 2; ++i) { const int l0 = 256 * (2 * w + i) + 16 * aq + 4 * q; xe[i] = *(const v4u*)(XP + (l0 >> 5) * TILE_DW + p * 32 + (l0 & 31)); }
  const f32x2 sa = *(const f32x2*)(MOD + 2 * p), ca = *(const f32x2*)(MOD + D + 2 * p), ga = *(const f32x2*)(MOD + 2 * D + 2 * p);
  const unsigned dku = *(const unsigned*)(Dskip + 2 * p);
  if (tid < 60) *(LAS v4u*)(smem + K2_KB + (tid / 30) * K2_KBP + (tid % 30) * 16) = v4u{0u, 0u, 0u, 0u};
  if (tid >= 64 && tid < 80) *(LAS v4u*)(smem + K2_HC + ((tid - 64) >> 1) * K2_HCP + 4096 + (tid & 1) * 16) = v4u{0u, 0u, 0u, 0u};
  float h0[12], h1[12];
#pragma unroll
  for (int i = 1; i < 12; ++i) {
    const unsigned u = i < 4 ? xa[i] : (i < 8 ? xb[i - 4] : xc[i - 8]);
    const float m = st[i >> 1][(i & 1) * 2], r = st[i >> 1][(i & 1) * 2 + 1];
    h0[i] = (lo2f(u) - m) * r * ca.x + sa.x;
    h1[i] = (hi2f(u) - m) * r * ca.y + sa.y;
    if (i < 4 && tid == 0) { h0[i] = 0.f; h1[i] = 0.f; }
  }
  {
    LAS char* dst = smem + K2_HC + (2040 - t0) * 2;
#pragma unroll
    for (int sg = 0; sg < 4; ++sg) {
      *(LAS v4u*)(dst + sg * K2_HCP) = v4u{pack2(h0[11 - sg], h0[10 - sg]), pack2(h0[9 - sg], h0[8 - sg]), pack2(h0[7 - sg], h0[6 - sg]), pack2(h0[5 - sg], h0[4 - sg])};
      *(LAS v4u*)(dst + (4 + sg) * K2_HCP) = v4u{pack2(h1[11 - sg], h1[10 - sg]), pack2(h1[9 - sg], h1[8 - sg]), pack2(h1[7 - sg], h1[6 - sg]), pack2(h1[5 - sg], h1[4 - sg])};
    }
  }
  __syncthreads();
  const int ch = w >> 1, hf = w & 1;
  const int sg = (3 - aq) & 3;
  LAS char* fb = smem + K2_HC + (ch * 4 + sg) * K2_HCP + 2 * (15 - aq + 8 * q - sg) + 256 * (1 - hf);
  LAS char* gb = smem + K2_KB + ch * K2_KBP + 32 * aq + 16 * q + 256 * (1 - hf);
  f32x4 acc[8];
#pragma unroll
  for (int c = 0; c < 8; ++c) acc[c] = f32x4{0.f, 0.f, 0.f, 0.f};
#pragma unroll
  for (int t = 0; t < 4; ++t) {
    v4u fr[8], gr[8];
#pragma unroll
    for (int m = 0; m < 8; ++m) {
      const v2u a = *(const LAS v2u*)(fb + 64 * (59 - t - 8 * m)), b = *(const LAS v2u*)(fb + 64 * (59 - t - 8 * m) + 8);
      fr[m] = v4u{a.x, a.y, b.x, b.y};
      gr[m] = *(const LAS v4u*)(gb + 192 + 512 * m - 64 * t);
    }
#pragma unroll
    for (int s = 0; s < 8; ++s)
#pragma unroll
      for (int o = 0; o <= s; ++o) {
        if (s + o < 8) acc[s + o] = mfma16(fr[s], gr[o], acc[s + o]);
        if (o < s && s + o < 8) acc[s + o] = mfma16(fr[o], gr[s], acc[s + o]);
      }
  }
  LAS f32x4* ex = (LAS f32x4*)(smem + K2_EX);
#pragma unroll
  for (int c = 0; c < 8; ++c) ex[(w * 8 + c) * 64 + lane] = acc[c];
  __syncthreads();
  unsigned* X1P = (unsigned*)(ws + WS_X1P);
  const float dk0 = lo2f(dku), dk1 = hi2f(dku);
#pragma unroll
  for (int i = 0; i < 2; ++i) {
    const int c = 2 * w + i, l0 = 256 * c + 16 * aq + 4 * q;
    const f32x4 y0 = ex[(0 * 8 + c) * 64 + lane] + ex[(1 * 8 + c) * 64 + lane];
    const f32x4 y1 = ex[(2 * 8 + c) * 64 + lane] + ex[(3 * 8 + c) * 64 + lane];
    const v2u ha = *(const LAS v2u*)(smem + K2_HC + (2044 - l0) * 2), hb = *(const LAS v2u*)(smem + K2_HC + 4 * K2_HCP + (2044 - l0) * 2);
    const float hh0[4] = {hi2f(ha.y), lo2f(ha.y), hi2f(ha.x), lo2f(ha.x)}, hh1[4] = {hi2f(hb.y), lo2f(hb.y), hi2f(hb.x), lo2f(hb.x)};
    v4u o;
#pragma unroll
    for (int k = 0; k < 4; ++k)
      o[k] = pack2(lo2f(xe[i][k]) + ga.x * (y0[k] + hh0[k] * dk0), hi2f(xe[i][k]) + ga.y * (y1[k] + hh1[k] * dk1));
    *(v4u*)(X1P + (l0 >> 5) * TILE_DW + p * 32 + (l0 & 31)) = o;
  }
}

__device__ __forceinline__ void role_w1(int blk, const u16* w1, char* ws, LAS char* smem) {
  const int tid = threadIdx.x, lane = tid & 63, w = __builtin_amdgcn_readfirstlane(tid >> 6);
  const int cg = tid & 7, rr = tid >> 3;
  const float* MOD = (const float*)(ws + WS_MOD);
  v4u v[14]; float sb[14], cb[14];
#pragma unroll
  for (int i = 0; i < 14; ++i) {
    v[i] = *(const v4u*)(w1 + (size_t)(32 * i + rr) * F + blk * 64 + cg * 8);
    sb[i] = MOD[3 * D + 32 * i + rr]; cb[i] = MOD[4 * D + 32 * i + rr];
  }
  float cs[8] = {}, bi[8] = {};
#pragma unroll
  for (int i = 0; i < 14; ++i) {
    v4u o;
#pragma unroll
    for (int k = 0; k < 4; ++k) {
      const float a = lo2f(v[i][k]), b = hi2f(v[i][k]);
      o[k] = pack2(a * cb[i], b * cb[i]);
      cs[2 * k] += lo2f(o[k]); cs[2 * k + 1] += hi2f(o[k]);
      bi[2 * k] += sb[i] * a; bi[2 * k + 1] += sb[i] * b;
    }
    *(LAS v4u*)(smem + (32 * i + rr) * 144 + cg * 16) = o;
  }
#pragma unroll
  for (int k = 0; k < 8; ++k) { cs[k] = sum_hi3(cs[k]); bi[k] = sum_hi3(bi[k]); }
  LAS float* red = (LAS float*)(smem + 448 * 144);
  if (lane < 8) {
    *(LAS f32x4*)(red + w * 64 + cg * 8) = f32x4{cs[0], cs[1], cs[2], cs[3]};
    *(LAS f32x4*)(red + w * 64 + cg * 8 + 4) = f32x4{cs[4], cs[5], cs[6], cs[7]};
    *(LAS f32x4*)(red + 256 + w * 64 + cg * 8) = f32x4{bi[0], bi[1], bi[2], bi[3]};
    *(LAS f32x4*)(red + 256 + w * 64 + cg * 8 + 4) = f32x4{bi[4], bi[5], bi[6], bi[7]};
  }
  __syncthreads();
  if (tid < 128) {
    const int a = tid >> 6, t = tid & 63;
    const float r = (red[a * 256 + t] + red[a * 256 + 64 + t]) + (red[a * 256 + 128 + t] + red[a * 256 + 192 + t]);
    ((float*)(ws + (a ? WS_BI : WS_CS)))[blk * 64 + t] = r;
  }
  const int g = lane >> 4, q = (lane & 15) >> 2, p = lane & 3;
  LAS char* rb = smem + (2 * (g >> 1) + (q & 1) + 4 * (q >> 1)) * 144 + (16 * (g & 1) + 4 * p) * 2;
  v4u* W1T = (v4u*)(ws + WS_W1T);
#pragma unroll
  for (int i = 0; i < 14; ++i) {
    const int t = w + 4 * i, f2 = t & 1, s = t >> 1;
    const v2u a = trread(rb + (16 * s) * 144 + f2 * 64), b = trread(rb + (16 * s + 8) * 144 + f2 * 64);
    W1T[((blk * 2 + f2) * 28 + s) * 64 + lane] = v4u{a.x, a.y, b.x, b.y};
  }
}

__global__ void __launch_bounds__(256) k2_conv(char* ws, const u16* Dskip, const u16* w1) {
  __shared__ __attribute__((aligned(16))) char smem_[K2_LDS];
  LAS char* smem = (LAS char*)smem_;
  STAMP(ws, 1, 0);
  if (blockIdx.x < NPAIR) role_conv(blockIdx.x, ws, Dskip, smem);
  else role_w1(blockIdx.x - NPAIR, w1, ws, smem);
  STAMP(ws, 1, 1);
}

constexpr int K3_LDS = 112 * 1024 + 1024;
__global__ void __launch_bounds__(256) k3_gemm1(char* ws) {
  __shared__ __attribute__((aligned(16))) char smem_[K3_LDS];
  LAS char* smem = (LAS char*)smem_;
  const int tid = threadIdx.x, lane = tid & 63, w = __builtin_amdgcn_readfirstlane(tid >> 6);
  const int xcd = blockIdx.x & 7, r = blockIdx.x >> 3, tb = 2 * xcd + (r >= 14), fb = r >= 14 ? r - 14 : r;
  STAMP(ws, 2, 0);
  const unsigned* xsrc = (const unsigned*)(ws + WS_X1P) + (size_t)(tb * 4 + w) * TILE_DW + lane;
  v4u xf[28];
#pragma unroll
  for (int s = 0; s < 28; ++s)
#pragma unroll
    for (int i = 0; i < 4; ++i) xf[s][i] = xsrc[(4 * s + i) * 64];
  {
    const char* src = ws + WS_W1T + (size_t)fb * (112 * 1024) + w * 1024 + lane * 16;
#pragma unroll
    for (int i = 0; i < 28; ++i)
      __builtin_amdgcn_global_load_lds((const GAS void*)(src + i * 4096), (LAS void*)(smem + w * 1024 + i * 4096), 16, 0, 0);
  }
  {
    const float v = ((const float*)(ws + (tid >> 7 ? WS_BI : WS_CS)))[fb * 128 + (tid & 127)];
    ((LAS float*)(smem + 112 * 1024))[tid] = v;
  }
  float s1 = 0.f, s2 = 0.f;
#pragma unroll
  for (int s = 0; s < 28; ++s)
#pragma unroll
    for (int i = 0; i < 4; ++i) { s1 = dot2(xf[s][i], 0x3f803f80u, s1); s2 = dot2(xf[s][i], xf[s][i], s2); }
  s1 = sum_x32(s1) * (1.f / D); s2 = sum_x32(s2) * (1.f / D);
  const float var = fmaxf(s2 - s1 * s1, 0.f);
  const float rs = 1.f / rbf(sqrtf(rbf(rbf(var) + 1e-5f))), nmr = -rbf(s1) * rs;
  __syncthreads();
  const int h = lane >> 5;
  v4u* HT = (v4u*)(ws + WS_HT) + (size_t)(tb * 2 + (w >> 1)) * (56 * 256) + (w & 1) * 128 + lane;
#pragma unroll
  for (int ft = 0; ft < 4; ++ft) {
    f32x16 acc = {};
#pragma unroll
    for (int s = 0; s < 28; ++s) acc = mfma32(*(const LAS v4u*)(smem + (ft * 28 + s) * 1024 + lane * 16), xf[s], acc);
    unsigned o[8];
#pragma unroll
    for (int g = 0; g < 4; ++g) {
      const f32x4 cs = *(const LAS f32x4*)(smem + 112 * 1024 + (ft * 32 + 8 * g + 4 * h) * 4);
      const f32x4 bi = *(const LAS f32x4*)(smem + 112 * 1024 + 512 + (ft * 32 + 8 * g + 4 * h) * 4);
      float e[4];
#pragma unroll
      for (int i = 0; i < 4; ++i) {
        const float pre = acc[4 * g + i] * rs + (cs[i] * nmr + bi[i]);
        e[i] = pre * __builtin_amdgcn_rcpf(1.f + __builtin_amdgcn_exp2f(pre * -1.4426950408889634f));
      }
      o[2 * g] = pack2(e[0], e[1]); o[2 * g + 1] = pack2(e[2], e[3]);
    }
    HT[(fb * 4 + ft) * 256] = v4u{o[0], o[1], o[2], o[3]};
    HT[(fb * 4 + ft) * 256 + 64] = v4u{o[4], o[5], o[6], o[7]};
  }
  STAMP(ws, 2, 1);
}

constexpr int K4_LDS = 65536;
__global__ void __launch_bounds__(256) k4_gemm2(const char* ws, u16* out) {
  __shared__ __attribute__((aligned(16))) char smem_[K4_LDS];
  LAS char* smem = (LAS char*)smem_;
  const int tid = threadIdx.x, lane = tid & 63, w = __builtin_amdgcn_readfirstlane(tid >> 6);
  const int xcd = blockIdx.x & 7, r = blockIdx.x >> 3, tb = 4 * xcd + r / 7, cb = r % 7;
  STAMP(ws, 3, 0);
  const v4u* hp = (const v4u*)(ws + WS_HT) + ((size_t)tb * 56 + 14 * w) * 256 + lane;
  const v4u* wp = (const v4u*)(ws + WS_W2T) + ((size_t)cb * 56 + 14 * w) * 256 + lane;
  constexpr int WIN = 6;
  v4u hb[WIN][4], wb[WIN][4];
#pragma unroll
  for (int i = 0; i < WIN; ++i)
#pragma unroll
    for (int k = 0; k < 4; ++k) { hb[i][k] = hp[i * 256 + k * 64]; wb[i][k] = wp[i * 256 + k * 64]; }
  const int h = lane >> 5, tok = tb * 64 + (w & 1) * 32 + (lane & 31), d0 = cb * 64 + (w >> 1) * 32;
  unsigned xr[8]; f32x4 gbv[4];
#pragma unroll
  for (int g = 0; g < 4; ++g) {
    const unsigned* xs = (const unsigned*)(ws + WS_X1P) + (size_t)(tok >> 5) * TILE_DW + ((d0 + 8 * g + 4 * h) >> 1) * 32 + (tok & 31);
    xr[2 * g] = xs[0]; xr[2 * g + 1] = xs[32];
    gbv[g] = *(const f32x4*)((const float*)(ws + WS_MOD) + 5 * D + d0 + 8 * g + 4 * h);
  }
  __builtin_amdgcn_sched_barrier(0);
  f32x16 acc[2][2] = {};
#pragma unroll
  for (int i = 0; i < 14; ++i) {
    v4u hc[4], wc[4];
#pragma unroll
    for (int k = 0; k < 4; ++k) { hc[k] = hb[i % WIN][k]; wc[k] = wb[i % WIN][k]; }
#pragma unroll
    for (int s = 0; s < 2; ++s)
#pragma unroll
      for (int a = 0; a < 2; ++a)
#pragma unroll
        for (int b = 0; b < 2; ++b) acc[a][b] = mfma32(wc[a * 2 + s], hc[b * 2 + s], acc[a][b]);
    if (i + WIN < 14) {
#pragma unroll
      for (int k = 0; k < 4; ++k) { hb[i % WIN][k] = hp[(i + WIN) * 256 + k * 64]; wb[i % WIN][k] = wp[(i + WIN) * 256 + k * 64]; }
    }
    __builtin_amdgcn_sched_barrier(0);
  }
  LAS f32x4* ex = (LAS f32x4*)smem;
#pragma unroll
  for (int a = 0; a < 2; ++a)
#pragma unroll
    for (int b = 0; b < 2; ++b)
#pragma unroll
      for (int g = 0; g < 4; ++g)
        ex[((w * 4 + a * 2 + b) * 4 + g) * 64 + lane] = f32x4{acc[a][b][4 * g], acc[a][b][4 * g + 1], acc[a][b][4 * g + 2], acc[a][b][4 * g + 3]};
  __syncthreads();
  unsigned o[8];
#pragma unroll
  for (int g = 0; g < 4; ++g) {
    const f32x4 v = (ex[((0 * 4 + w) * 4 + g) * 64 + lane] + ex[((1 * 4 + w) * 4 + g) * 64 + lane]) +
                    (ex[((2 * 4 + w) * 4 + g) * 64 + lane] + ex[((3 * 4 + w) * 4 + g) * 64 + lane]);
    o[2 * g] = pack2(lo2f(xr[2 * g]) + gbv[g].x * v.x, hi2f(xr[2 * g]) + gbv[g].y * v.y);
    o[2 * g + 1] = pack2(lo2f(xr[2 * g + 1]) + gbv[g].z * v.z, hi2f(xr[2 * g + 1]) + gbv[g].w * v.w);
  }
#pragma unroll
  for (int g = 0; g < 4; g += 2) {
    auto r0 = __builtin_amdgcn_permlane32_swap(o[2 * g], o[2 * g + 2], false, false);
    auto r1 = __builtin_amdgcn_permlane32_swap(o[2 * g + 1], o[2 * g + 3], false, false);
    *(v4u*)(out + (size_t)tok * D + d0 + 8 * g + 8 * h) = v4u{(unsigned)r0[0], (unsigned)r1[0], (unsigned)r0[1], (unsigned)r1[1]};
  }
  STAMP(ws, 3, 1);
}

__global__ void k_report(const char* ws, int which) {
  const unsigned long long* st = (const unsigned long long*)(ws + WS_STAMP);
  const int nb[4] = {610, 252, 224, 224};
  unsigned long long t0 = ~0ull;
  for (int i = threadIdx.x; i < 610; i += 64) t0 = st[i] < t0 ? st[i] : t0;
  for (int o = 32; o; o >>= 1) { unsigned long long u = __shfl_xor(t0, o); t0 = u < t0 ? u : t0; }
  unsigned long long v = which & 1 ? 0ull : ~0ull;
  for (int i = threadIdx.x; i < nb[which >> 1]; i += 64) { unsigned long long u = st[which * 1024 + i]; v = (which & 1) ? (u > v ? u : v) : (u < v ? u : v); }
  for (int o = 32; o; o >>= 1) { unsigned long long u = __shfl_xor(v, o); v = (which & 1) ? (u > v ? u : v) : (u < v ? u : v); }
  unsigned long long dlt = v - t0; if (dlt > 10000) dlt = 10000;
  const unsigned long long tgt = t0 + 6000 + 2 * dlt;
  while (__builtin_amdgcn_s_memrealtime() < tgt) __builtin_amdgcn_s_sleep(1);
}
extern "C" void kernel_launch(void* const* d_in, const int* in_sizes, int n_in, void* d_out, int out_size, void* d_ws,
                              size_t ws_size, hipStream_t stream) {
  K1Args a;
  a.x = (const u16*)d_in[0]; a.Are = (const u16*)d_in[1]; a.Aim = (const u16*)d_in[2]; a.Bre = (const u16*)d_in[3];
  a.Bim = (const u16*)d_in[4]; a.Cre = (const u16*)d_in[5]; a.Cim = (const u16*)d_in[6]; a.logdt = (const u16*)d_in[7];
  a.c = (const u16*)d_in[12]; a.wmod = (const u16*)d_in[13]; a.w2 = (const u16*)d_in[11]; a.ws = (char*)d_ws;
  k1_prep<<<K1_NKG + K1_NLN + K1_NMOD + K1_NW2, 256, 0, stream>>>(a);
  k2_conv<<<NPAIR + 28, 256, 0, stream>>>((char*)d_ws, (const u16*)d_in[8], (const u16*)d_in[10]);
  k3_gemm1<<<224, 256, 0, stream>>>((char*)d_ws);
  k4_gemm2<<<224, 256, 0, stream>>>((const char*)d_ws, (u16*)d_out);
  k_report<<<1, 64, 0, stream>>>((const char*)d_ws, 0);
}
```

```cpp
#include <hip/hip_runtime.h>
#include <hip/hip_bf16.h>
#include <stdint.h>

#define GAS __attribute__((address_space(1)))
#define LAS __attribute__((address_space(3)))
typedef unsigned v4u __attribute__((ext_vector_type(4)));
typedef unsigned v2u __attribute__((ext_vector_type(2)));
typedef float f32x2 __attribute__((ext_vector_type(2)));
typedef float f32x4 __attribute__((ext_vector_type(4)));
typedef float f32x16 __attribute__((ext_vector_type(16)));
typedef __bf16 bf16x8 __attribute__((ext_vector_type(8)));
typedef __bf16 bf16x2 __attribute__((ext_vector_type(2)));
typedef short s16x4 __attribute__((ext_vector_type(4)));
typedef unsigned short u16;

constexpr int L = 2048, D = 448, NM = 256, F = 1792;
constexpr int NPAIR = D / 2;
constexpr int TILE_DW = NPAIR * 32;

constexpr size_t WS_XP = 0;
constexpr size_t WS_X1P = WS_XP + (size_t)L * D * 2;
constexpr size_t WS_KB = WS_X1P + (size_t)L * D * 2;
constexpr size_t WS_ST = WS_KB + (size_t)L * D * 2;
constexpr size_t WS_MOD = WS_ST + (size_t)L * 8;
constexpr size_t WS_CS = WS_MOD + 16384;
constexpr size_t WS_BI = WS_CS + 8192;
constexpr size_t WS_W1T = WS_BI + 8192;
constexpr size_t WS_W2T = WS_W1T + (size_t)D * F * 2;
constexpr size_t WS_HT = WS_W2T + (size_t)D * F * 2;

__device__ __forceinline__ float bf2f(u16 b) { return __builtin_bit_cast(float, (unsigned)b << 16); }
__device__ __forceinline__ float lo2f(unsigned u) { return __builtin_bit_cast(float, u << 16); }
__device__ __forceinline__ float hi2f(unsigned u) { return __builtin_bit_cast(float, u & 0xffff0000u); }
__device__ __forceinline__ unsigned pack2(float lo, float hi) {
  bf16x2 v; v.x = (__bf16)lo; v.y = (__bf16)hi; return __builtin_bit_cast(unsigned, v);
}
__device__ __forceinline__ float rbf(float f) { return lo2f(pack2(f, 0.f)); }
__device__ __forceinline__ f32x16 mfma32(v4u a, v4u b, f32x16 c) {
  return __builtin_amdgcn_mfma_f32_32x32x16_bf16(__builtin_bit_cast(bf16x8, a), __builtin_bit_cast(bf16x8, b), c, 0, 0, 0);
}
__device__ __forceinline__ f32x4 mfma16(v4u a, v4u b, f32x4 c) {
  return __builtin_amdgcn_mfma_f32_16x16x32_bf16(__builtin_bit_cast(bf16x8, a), __builtin_bit_cast(bf16x8, b), c, 0, 0, 0);
}
__device__ __forceinline__ float dot2(unsigned a, unsigned b, float c) {
  return __builtin_amdgcn_fdot2_f32_bf16(__builtin_bit_cast(bf16x2, a), __builtin_bit_cast(bf16x2, b), c, false);
}
__device__ __forceinline__ v2u trread(LAS char* p) {
  return __builtin_bit_cast(v2u, __builtin_amdgcn_ds_read_tr16_b64_v4i16((LAS s16x4*)p));
}
__device__ __forceinline__ float sum_x8(float v) {
  return v + __builtin_bit_cast(float, __builtin_amdgcn_update_dpp(0, __builtin_bit_cast(int, v), 0x128, 0xf, 0xf, false));
}
__device__ __forceinline__ float sum_x16(float v) {
  const unsigned u = __builtin_bit_cast(unsigned, v);
  auto r = __builtin_amdgcn_permlane16_swap(u, u, false, false);
  return __builtin_bit_cast(float, (unsigned)r[0]) + __builtin_bit_cast(float, (unsigned)r[1]);
}
__device__ __forceinline__ float sum_x32(float v) {
  const unsigned u = __builtin_bit_cast(unsigned, v);
  auto r = __builtin_amdgcn_permlane32_swap(u, u, false, false);
  return __builtin_bit_cast(float, (unsigned)r[0]) + __builtin_bit_cast(float, (unsigned)r[1]);
}
__device__ __forceinline__ float sum_hi3(float v) { return sum_x32(sum_x16(sum_x8(v))); }

struct cplx { float re, im; };
__device__ __forceinline__ cplx cmul(cplx a, cplx b) {
  cplx r; r.re = a.re * b.re - a.im * b.im; r.im = a.re * b.im + a.im * b.re; return r;
}
__device__ __forceinline__ cplx cexp_l(float al, float th, float l) {
  const float mag = __builtin_amdgcn_exp2f(al * l);
  const float ph = __builtin_amdgcn_fractf(th * l);
  cplx r; r.re = mag * __builtin_amdgcn_cosf(ph); r.im = mag * __builtin_amdgcn_sinf(ph); return r;
}

constexpr int K1_NKG = D, K1_NLN = 64, K1_NMOD = 42, K1_NW2 = 56;
constexpr int K1_LDS = 4 * 32 * 272;

__device__ __forceinline__ void role_kgen(int d, const u16* Are, const u16* Aim, const u16* Bre, const u16* Bim,
                                          const u16* Cre, const u16* Cim, const u16* logdt, u16* KB, LAS char* smem) {
  const int tid = threadIdx.x, lane = tid & 63, w = __builtin_amdgcn_readfirstlane(tid >> 6);
  const int idx = d * NM + tid;
  const float are = bf2f(Are[idx]), aim = bf2f(Aim[idx]), bre = bf2f(Bre[idx]), bim = bf2f(Bim[idx]);
  const float cre = bf2f(Cre[idx]), cim = bf2f(Cim[idx]);
  const float dt = rbf(expf(bf2f(logdt[d])));
  const float al = (dt * are) * 1.4426950408889634f, th = (dt * aim) * 0.15915494309189535f;
  const cplx W = cmul(cplx{cre, cim}, cplx{dt * bre, dt * bim});
  const cplx E1 = cexp_l(al, th, 1.f), E8 = cexp_l(al, th, 8.f), E16 = cexp_l(al, th, 16.f), E256 = cexp_l(al, th, 256.f);
  const cplx E2 = cmul(E1, E1), E4 = cmul(E2, E2);
  LAS char* base = smem + w * (32 * 272);
  LAS unsigned* wr = (LAS unsigned*)(base + lane * 4);
  const LAS v4u* rd = (const LAS v4u*)(base + (lane & 31) * 272 + (lane >> 5) * 16);
  {
    cplx p0{1.f, 0.f}, p1 = E8;
#pragma unroll
    for (int j = 0; j < 32; j += 2) {
      wr[j * 68] = pack2(p0.re, p0.im);
      wr[(j + 1) * 68] = pack2(p1.re, p1.im);
      if (j < 30) { p0 = cmul(p0, E16); p1 = cmul(p1, E16); }
    }
  }
  v4u bf[8];
#pragma unroll
  for (int s = 0; s < 8; ++s) bf[s] = rd[2 * s];
  {
    auto put = [&](int row, cplx v) { wr[row * 68] = pack2(v.re, -v.im); };
    cplx v = W;
#pragma unroll
    for (int a = 0; a < 8; ++a) { put(a, v); if (a < 7) v = cmul(v, E1); }
    cplx q = cmul(W, E256);
    put(8, q); put(9, cmul(q, E2));
    { const cplx t = cmul(q, E4); put(10, t); put(12, t); put(13, cmul(t, E2)); }
    put(14, cmul(q, E8));
    q = cmul(q, E256);
    put(16, q); { const cplx t = cmul(q, E4); put(17, t); put(20, t); } put(21, cmul(q, E8));
    q = cmul(q, E256);
    put(18, q); { const cplx t = cmul(q, E4); put(19, t); put(22, t); } put(23, cmul(q, E8));
#pragma unroll
    for (int b = 0; b < 4; ++b) { q = cmul(q, E256); put(24 + b, q); put(28 + b, cmul(q, E8)); }
  }
  f32x16 acc = {};
#pragma unroll
  for (int s = 0; s < 8; ++s) acc = mfma32(rd[2 * s], bf[s], acc);
  __syncthreads();
  LAS f32x4* ex = (LAS f32x4*)smem;
#pragma unroll
  for (int g = 0; g < 4; ++g) ex[(w * 4 + g) * 64 + lane] = f32x4{acc[4 * g], acc[4 * g + 1], acc[4 * g + 2], acc[4 * g + 3]};
  __syncthreads();
  const f32x4 v = (ex[(0 * 4 + w) * 64 + lane] + ex[(1 * 4 + w) * 64 + lane]) + (ex[(2 * 4 + w) * 64 + lane] + ex[(3 * 4 + w) * 64 + lane]);
  const int j = lane & 31, h = lane >> 5;
  v2u* out = (v2u*)(KB + (size_t)d * L + 8 * j + 4 * h);
  if (w == 0) {
    out[0] = v2u{pack2(v.x, v.y), pack2(v.z, v.w)};
  } else if (w == 1) {
    out[64] = v2u{pack2(v.x, v.x), pack2(v.y, v.z)};
  } else if (w == 2) {
    out[128] = v2u{pack2(v.x, v.x), pack2(h ? v.y : v.x, v.y)};
    out[192] = v2u{pack2(v.z, v.z), pack2(h ? v.w : v.z, v.w)};
  } else {
#pragma unroll
    for (int b = 0; b < 4; ++b) {
      const float vb = v[b];
      const unsigned u = __builtin_bit_cast(unsigned, vb);
      auto r = __builtin_amdgcn_permlane32_swap(u, u, false, false);
      const float o = __builtin_bit_cast(float, (unsigned)r[0]);
      out[256 + 64 * b] = v2u{pack2((j & 1) ? vb : o, vb), pack2(vb, vb)};
    }
  }
}

__device__ __forceinline__ void role_ln(int tile, const u16* x, unsigned* XP, f32x2* ST, LAS char* smem) {
  const int tid = threadIdx.x, lane = tid & 63, w = __builtin_amdgcn_readfirstlane(tid >> 6);
  const int tq = lane & 7, cc = lane >> 3;
  const int nrd = (w < 3) ? 2 : 1;
  v4u v[2][4];
#pragma unroll
  for (int rd = 0; rd < 2; ++rd)
#pragma unroll
    for (int i = 0; i < 4; ++i) {
      v[rd][i] = v4u{0u, 0u, 0u, 0u};
      if (rd < nrd) v[rd][i] = *(const v4u*)(x + (size_t)(tile * 32 + 4 * tq + i) * D + (8 * (w + 4 * rd) + cc) * 8);
    }
#pragma unroll
  for (int rd = 0; rd < 2; ++rd)
    if (rd < nrd) {
#pragma unroll
      for (int c = 0; c < 4; ++c)
        *(v4u*)(XP + (size_t)tile * TILE_DW + (4 * (8 * (w + 4 * rd) + cc) + c) * 32 + 4 * tq) =
            v4u{v[rd][0][c], v[rd][1][c], v[rd][2][c], v[rd][3][c]};
    }
  LAS f32x4* red = (LAS f32x4*)smem;
  f32x4 s;
#pragma unroll
  for (int i = 0; i < 4; ++i) {
    float a = 0.f;
#pragma unroll
    for (int rd = 0; rd < 2; ++rd)
#pragma unroll
      for (int c = 0; c < 4; ++c) a += lo2f(v[rd][i][c]) + hi2f(v[rd][i][c]);
    s[i] = sum_hi3(a);
  }
  if (cc == 0) red[w * 8 + tq] = s;
  __syncthreads();
  const f32x4 mean = ((red[tq] + red[8 + tq]) + (red[16 + tq] + red[24 + tq])) * (1.f / D);
#pragma unroll
  for (int i = 0; i < 4; ++i) {
    float a = 0.f;
#pragma unroll
    for (int rd = 0; rd < 2; ++rd)
      if (rd < nrd) {
#pragma unroll
        for (int c = 0; c < 4; ++c) {
          const float d0 = lo2f(v[rd][i][c]) - mean[i], d1 = hi2f(v[rd][i][c]) - mean[i];
          a += d0 * d0 + d1 * d1;
        }
      }
    s[i] = sum_hi3(a);
  }
  if (cc == 0) red[32 + w * 8 + tq] = s;
  __syncthreads();
  if (tid < 8) {
    const f32x4 var = ((red[32 + tq] + red[40 + tq]) + (red[48 + tq] + red[56 + tq])) * (1.f / D);
#pragma unroll
    for (int i = 0; i < 4; ++i) {
      const float sd = rbf(sqrtf(rbf(rbf(var[i]) + 1e-5f)));
      ST[tile * 32 + 4 * tq + i] = f32x2{rbf(mean[i]), 1.f / sd};
    }
  }
}

__device__ __forceinline__ void role_mod(int blk, const u16* c, const u16* wmod, float* MOD, LAS char* smem) {
  const int tid = threadIdx.x, lane = tid & 63, w = __builtin_amdgcn_readfirstlane(tid >> 6);
  const int cg = tid & 7, rr = tid >> 3;
  v4u v[14]; float sc[14];
#pragma unroll
  for (int i = 0; i < 14; ++i) {
    v[i] = *(const v4u*)(wmod + (size_t)(32 * i + rr) * (6 * D) + blk * 64 + cg * 8);
    sc[i] = bf2f(c[32 * i + rr]);
  }
  float acc[8] = {};
#pragma unroll
  for (int i = 0; i < 14; ++i) {
    const float s = rbf(sc[i] / (1.f + __expf(-sc[i])));
#pragma unroll
    for (int k = 0; k < 4; ++k) { acc[2 * k] += s * lo2f(v[i][k]); acc[2 * k + 1] += s * hi2f(v[i][k]); }
  }
#pragma unroll
  for (int k = 0; k < 8; ++k) acc[k] = sum_hi3(acc[k]);
  LAS float* red = (LAS float*)smem;
  if (lane < 8) {
    *(LAS f32x4*)(red + w * 64 + cg * 8) = f32x4{acc[0], acc[1], acc[2], acc[3]};
    *(LAS f32x4*)(red + w * 64 + cg * 8 + 4) = f32x4{acc[4], acc[5], acc[6], acc[7]};
  }
  __syncthreads();
  if (tid < 64) {
    float r = rbf((red[tid] + red[64 + tid]) + (red[128 + tid] + red[192 + tid]));
    const int col = blk * 64 + tid, sec = col / D;
    if (sec == 1 || sec == 4) r = rbf(1.f + r);
    MOD[col] = r;
  }
}

__device__ __forceinline__ void role_w2(int ft, const u16* w2, v4u* W2T, LAS char* smem) {
  const int tid = threadIdx.x, lane = tid & 63, w = __builtin_amdgcn_readfirstlane(tid >> 6);
  v4u v[7];
#pragma unroll
  for (int i = 0; i < 7; ++i) v[i] = *(const v4u*)(w2 + (size_t)ft * 32 * D + (tid + 256 * i) * 8);
#pragma unroll
  for (int i = 0; i < 7; ++i) { const int c = tid + 256 * i, row = c / 56, col = c - row * 56; *(LAS v4u*)(smem + row * 960 + col * 16) = v[i]; }
  __syncthreads();
  const int g = lane >> 4, q = (lane & 15) >> 2, p = lane & 3;
  LAS char* rb = smem + (4 * (g >> 1) + q) * 960 + (16 * (g & 1) + 4 * p) * 2;
#pragma unroll
  for (int i = 0; i < 7; ++i) {
    const int t = w + 4 * i, dt = t >> 1, s = t & 1;
    const v2u a = trread(rb + (16 * s) * 960 + dt * 64), b = trread(rb + (16 * s + 8) * 960 + dt * 64);
    W2T[((((dt >> 1) * 56 + ft) * 2 + (dt & 1)) * 2 + s) * 64 + lane] = v4u{a.x, a.y, b.x, b.y};
  }
}

struct K1Args {
  const u16 *x, *Are, *Aim, *Bre, *Bim, *Cre, *Cim, *logdt, *c, *wmod, *w2;
  char* ws;
};
__global__ void __launch_bounds__(256) k1_prep(K1Args a) {
  __shared__ __attribute__((aligned(16))) char smem_[K1_LDS];
  LAS char* smem = (LAS char*)smem_;
  int b = blockIdx.x;
  if (b < K1_NKG) { role_kgen(b, a.Are, a.Aim, a.Bre, a.Bim, a.Cre, a.Cim, a.logdt, (u16*)(a.ws + WS_KB), smem); return; }
  b -= K1_NKG;
  if (b < K1_NLN) { role_ln(b, a.x, (unsigned*)(a.ws + WS_XP), (f32x2*)(a.ws + WS_ST), smem); return; }
  b -= K1_NLN;
  if (b < K1_NMOD) { role_mod(b, a.c, a.wmod, (float*)(a.ws + WS_MOD), smem); return; }
  b -= K1_NMOD;
  role_w2(b, a.w2, (v4u*)(a.ws + WS_W2T), smem);
}

constexpr int K2_KB = 0, K2_KBP = 4608;
constexpr int K2_HC = 2 * K2_KBP, K2_HCP = 4160;
constexpr int K2_EX = K2_HC + 8 * K2_HCP;
constexpr int K2_LDS = K2_EX + 32768;

__device__ __forceinline__ void role_conv(int p, const char* ws, const u16* Dskip, LAS char* smem) {
  const int tid = threadIdx.x, lane = tid & 63, w = __builtin_amdgcn_readfirstlane(tid >> 6);
  const unsigned* XP = (const unsigned*)(ws + WS_XP);
  const float* MOD = (const float*)(ws + WS_MOD);
  {
    const char* src = ws + WS_KB + (size_t)(2 * p + (w >> 1)) * (L * 2) + (w & 1) * 2048 + lane * 16;
    LAS char* dst = smem + K2_KB + (w >> 1) * K2_KBP + 480 + (w & 1) * 2048;
    __builtin_amdgcn_global_load_lds((const GAS void*)src, (LAS void*)dst, 16, 0, 0);
    __builtin_amdgcn_global_load_lds((const GAS void*)(src + 1024), (LAS void*)(dst + 1024), 16, 0, 0);
  }
  const int t0 = 8 * tid, tm = (tid ? t0 - 4 : 0);
  const v4u xa = *(const v4u*)(XP + (tm >> 5) * TILE_DW + p * 32 + (tm & 31));
  const v4u xb = *(const v4u*)(XP + (t0 >> 5) * TILE_DW + p * 32 + (t0 & 31));
  const v4u xc = *(const v4u*)(XP + (t0 >> 5) * TILE_DW + p * 32 + (t0 & 31) + 4);
  const f32x4* ST4 = (const f32x4*)(ws + WS_ST);
  f32x4 st[6];
#pragma unroll
  for (int i = 0; i < 2; ++i) st[i] = ST4[(tm >> 1) + i];
#pragma unroll
  for (int i = 0; i < 4; ++i) st[2 + i] = ST4[(t0 >> 1) + i];
  const int aq = lane & 15, q = lane >> 4;
  v4u xe[2];
#pragma unroll
  for (int i = 0; i < 2; ++i) { const int l0 = 256 * (2 * w + i) + 16 * aq + 4 * q; xe[i] = *(const v4u*)(XP + (l0 >> 5) * TILE_DW + p * 32 + (l0 & 31)); }
  const f32x2 sa = *(const f32x2*)(MOD + 2 * p), ca = *(const f32x2*)(MOD + D + 2 * p), ga = *(const f32x2*)(MOD + 2 * D + 2 * p);
  const unsigned dku = *(const unsigned*)(Dskip + 2 * p);
  if (tid < 60) *(LAS v4u*)(smem + K2_KB + (tid / 30) * K2_KBP + (tid % 30) * 16) = v4u{0u, 0u, 0u, 0u};
  if (tid >= 64 && tid < 80) *(LAS v4u*)(smem + K2_HC + ((tid - 64) >> 1) * K2_HCP + 4096 + (tid & 1) * 16) = v4u{0u, 0u, 0u, 0u};
  float h0[12], h1[12];
#pragma unroll
  for (int i = 1; i < 12; ++i) {
    const unsigned u = i < 4 ? xa[i] : (i < 8 ? xb[i - 4] : xc[i - 8]);
    const float m = st[i >> 1][(i & 1) * 2], r = st[i >> 1][(i & 1) * 2 + 1];
    h0[i] = (lo2f(u) - m) * r * ca.x + sa.x;
    h1[i] = (hi2f(u) - m) * r * ca.y + sa.y;
    if (i < 4 && tid == 0) { h0[i] = 0.f; h1[i] = 0.f; }
  }
  {
    LAS char* dst = smem + K2_HC + (2040 - t0) * 2;
#pragma unroll
    for (int sg = 0; sg < 4; ++sg) {
      *(LAS v4u*)(dst + sg * K2_HCP) = v4u{pack2(h0[11 - sg], h0[10 - sg]), pack2(h0[9 - sg], h0[8 - sg]), pack2(h0[7 - sg], h0[6 - sg]), pack2(h0[5 - sg], h0[4 - sg])};
      *(LAS v4u*)(dst + (4 + sg) * K2_HCP) = v4u{pack2(h1[11 - sg], h1[10 - sg]), pack2(h1[9 - sg], h1[8 - sg]), pack2(h1[7 - sg], h1[6 - sg]), pack2(h1[5 - sg], h1[4 - sg])};
    }
  }
  __syncthreads();
  const int ch = w >> 1, hf = w & 1;
  const int sg = (3 - aq) & 3;
  LAS char* fb = smem + K2_HC + (ch * 4 + sg) * K2_HCP + 2 * (15 - aq + 8 * q - sg) + 256 * (1 - hf);
  LAS char* gb = smem + K2_KB + ch * K2_KBP + 32 * aq + 16 * q + 256 * (1 - hf);
  f32x4 acc[8];
#pragma unroll
  for (int c = 0; c < 8; ++c) acc[c] = f32x4{0.f, 0.f, 0.f, 0.f};
#pragma unroll
  for (int t = 0; t < 4; ++t) {
    v4u fr[8], gr[8];
#pragma unroll
    for (int m = 0; m < 8; ++m) {
      const v2u a = *(const LAS v2u*)(fb + 64 * (59 - t - 8 * m)), b = *(const LAS v2u*)(fb + 64 * (59 - t - 8 * m) + 8);
      fr[m] = v4u{a.x, a.y, b.x, b.y};
      gr[m] = *(const LAS v4u*)(gb + 192 + 512 * m - 64 * t);
    }
#pragma unroll
    for (int s = 0; s < 8; ++s)
#pragma unroll
      for (int o = 0; o <= s; ++o) {
        if (s + o < 8) acc[s + o] = mfma16(fr[s], gr[o], acc[s + o]);
        if (o < s && s + o < 8) acc[s + o] = mfma16(fr[o], gr[s], acc[s + o]);
      }
  }
  LAS f32x4* ex = (LAS f32x4*)(smem + K2_EX);
#pragma unroll
  for (int c = 0; c < 8; ++c) ex[(w * 8 + c) * 64 + lane] = acc[c];
  __syncthreads();
  unsigned* X1P = (unsigned*)(ws + WS_X1P);
  const float dk0 = lo2f(dku), dk1 = hi2f(dku);
#pragma unroll
  for (int i = 0; i < 2; ++i) {
    const int c = 2 * w + i, l0 = 256 * c + 16 * aq + 4 * q;
    const f32x4 y0 = ex[(0 * 8 + c) * 64 + lane] + ex[(1 * 8 + c) * 64 + lane];
    const f32x4 y1 = ex[(2 * 8 + c) * 64 + lane] + ex[(3 * 8 + c) * 64 + lane];
    const v2u ha = *(const LAS v2u*)(smem + K2_HC + (2044 - l0) * 2), hb = *(const LAS v2u*)(smem + K2_HC + 4 * K2_HCP + (2044 - l0) * 2);
    const float hh0[4] = {hi2f(ha.y), lo2f(ha.y), hi2f(ha.x), lo2f(ha.x)}, hh1[4] = {hi2f(hb.y), lo2f(hb.y), hi2f(hb.x), lo2f(hb.x)};
    v4u o;
#pragma unroll
    for (int k = 0; k < 4; ++k)
      o[k] = pack2(lo2f(xe[i][k]) + ga.x * (y0[k] + hh0[k] * dk0), hi2f(xe[i][k]) + ga.y * (y1[k] + hh1[k] * dk1));
    *(v4u*)(X1P + (l0 >> 5) * TILE_DW + p * 32 + (l0 & 31)) = o;
  }
}

__device__ __forceinline__ void role_w1(int blk, const u16* w1, char* ws, LAS char* smem) {
  const int tid = threadIdx.x, lane = tid & 63, w = __builtin_amdgcn_readfirstlane(tid >> 6);
  const int cg = tid & 7, rr = tid >> 3;
  const float* MOD = (const float*)(ws + WS_MOD);
  v4u v[14]; float sb[14], cb[14];
#pragma unroll
  for (int i = 0; i < 14; ++i) {
    v[i] = *(const v4u*)(w1 + (size_t)(32 * i + rr) * F + blk * 64 + cg * 8);
    sb[i] = MOD[3 * D + 32 * i + rr]; cb[i] = MOD[4 * D + 32 * i + rr];
  }
  float cs[8] = {}, bi[8] = {};
#pragma unroll
  for (int i = 0; i < 14; ++i) {
    v4u o;
#pragma unroll
    for (int k = 0; k < 4; ++k) {
      const float a = lo2f(v[i][k]), b = hi2f(v[i][k]);
      o[k] = pack2(a * cb[i], b * cb[i]);
      cs[2 * k] += lo2f(o[k]); cs[2 * k + 1] += hi2f(o[k]);
      bi[2 * k] += sb[i] * a; bi[2 * k + 1] += sb[i] * b;
    }
    *(LAS v4u*)(smem + (32 * i + rr) * 144 + cg * 16) = o;
  }
#pragma unroll
  for (int k = 0; k < 8; ++k) { cs[k] = sum_hi3(cs[k]); bi[k] = sum_hi3(bi[k]); }
  LAS float* red = (LAS float*)(smem + 448 * 144);
  if (lane < 8) {
    *(LAS f32x4*)(red + w * 64 + cg * 8) = f32x4{cs[0], cs[1], cs[2], cs[3]};
    *(LAS f32x4*)(red + w * 64 + cg * 8 + 4) = f32x4{cs[4], cs[5], cs[6], cs[7]};
    *(LAS f32x4*)(red + 256 + w * 64 + cg * 8) = f32x4{bi[0], bi[1], bi[2], bi[3]};
    *(LAS f32x4*)(red + 256 + w * 64 + cg * 8 + 4) = f32x4{bi[4], bi[5], bi[6], bi[7]};
  }
  __syncthreads();
  if (tid < 128) {
    const int a = tid >> 6, t = tid & 63;
    const float r = (red[a * 256 + t] + red[a * 256 + 64 + t]) + (red[a * 256 + 128 + t] + red[a * 256 + 192 + t]);
    ((float*)(ws + (a ? WS_BI : WS_CS)))[blk * 64 + t] = r;
  }
  const int g = lane >> 4, q = (lane & 15) >> 2, p = lane & 3;
  LAS char* rb = smem + (2 * (g >> 1) + (q & 1) + 4 * (q >> 1)) * 144 + (16 * (g & 1) + 4 * p) * 2;
  v4u* W1T = (v4u*)(ws + WS_W1T);
#pragma unroll
  for (int i = 0; i < 14; ++i) {
    const int t = w + 4 * i, f2 = t & 1, s = t >> 1;
    const v2u a = trread(rb + (16 * s) * 144 + f2 * 64), b = trread(rb + (16 * s + 8) * 144 + f2 * 64);
    W1T[((blk * 2 + f2) * 28 + s) * 64 + lane] = v4u{a.x, a.y, b.x, b.y};
  }
}

__global__ void __launch_bounds__(256) k2_conv(char* ws, const u16* Dskip, const u16* w1) {
  __shared__ __attribute__((aligned(16))) char smem_[K2_LDS];
  LAS char* smem = (LAS char*)smem_;
  if (blockIdx.x < NPAIR) role_conv(blockIdx.x, ws, Dskip, smem);
  else role_w1(blockIdx.x - NPAIR, w1, ws, smem);
}

constexpr int K3_LDS = 112 * 1024 + 1024;
__device__ __forceinline__ void k3_body(char* ws, LAS char* smem) {
  const int tid = threadIdx.x, lane = tid & 63, w = __builtin_amdgcn_readfirstlane(tid >> 6);
  const int xcd = blockIdx.x & 7, r = blockIdx.x >> 3, tb = 2 * xcd + (r >= 14), fb = r >= 14 ? r - 14 : r;
  const unsigned* xsrc = (const unsigned*)(ws + WS_X1P) + (size_t)(tb * 4 + w) * TILE_DW + lane;
  v4u xf[28];
#pragma unroll
  for (int s = 0; s < 28; ++s)
#pragma unroll
    for (int i = 0; i < 4; ++i) xf[s][i] = xsrc[(4 * s + i) * 64];
  {
    const char* src = ws + WS_W1T + (size_t)fb * (112 * 1024) + w * 1024 + lane * 16;
#pragma unroll
    for (int i = 0; i < 28; ++i)
      __builtin_amdgcn_global_load_lds((const GAS void*)(src + i * 4096), (LAS void*)(smem + w * 1024 + i * 4096), 16, 0, 0);
  }
  {
    const float v = ((const float*)(ws + (tid >> 7 ? WS_BI : WS_CS)))[fb * 128 + (tid & 127)];
    ((LAS float*)(smem + 112 * 1024))[tid] = v;
  }
  float s1 = 0.f, s2 = 0.f;
#pragma unroll
  for (int s = 0; s < 28; ++s)
#pragma unroll
    for (int i = 0; i < 4; ++i) { s1 = dot2(xf[s][i], 0x3f803f80u, s1); s2 = dot2(xf[s][i], xf[s][i], s2); }
  s1 = sum_x32(s1) * (1.f / D); s2 = sum_x32(s2) * (1.f / D);
  const float var = fmaxf(s2 - s1 * s1, 0.f);
  const float rs = 1.f / rbf(sqrtf(rbf(rbf(var) + 1e-5f))), nmr = -rbf(s1) * rs;
  __syncthreads();
  const int h = lane >> 5;
  v4u* HT = (v4u*)(ws + WS_HT) + (size_t)(tb * 2 + (w >> 1)) * (56 * 256) + (w & 1) * 128 + lane;
#pragma unroll
  for (int ft = 0; ft < 4; ++ft) {
    f32x16 acc = {};
#pragma unroll
    for (int s = 0; s < 28; ++s) acc = mfma32(*(const LAS v4u*)(smem + (ft * 28 + s) * 1024 + lane * 16), xf[s], acc);
    unsigned o[8];
#pragma unroll
    for (int g = 0; g < 4; ++g) {
      const f32x4 cs = *(const LAS f32x4*)(smem + 112 * 1024 + (ft * 32 + 8 * g + 4 * h) * 4);
      const f32x4 bi = *(const LAS f32x4*)(smem + 112 * 1024 + 512 + (ft * 32 + 8 * g + 4 * h) * 4);
      float e[4];
#pragma unroll
      for (int i = 0; i < 4; ++i) {
        const float pre = acc[4 * g + i] * rs + (cs[i] * nmr + bi[i]);
        e[i] = pre * __builtin_amdgcn_rcpf(1.f + __builtin_amdgcn_exp2f(pre * -1.4426950408889634f));
      }
      o[2 * g] = pack2(e[0], e[1]); o[2 * g + 1] = pack2(e[2], e[3]);
    }
    HT[(fb * 4 + ft) * 256] = v4u{o[0], o[1], o[2], o[3]};
    HT[(fb * 4 + ft) * 256 + 64] = v4u{o[4], o[5], o[6], o[7]};
  }
}

constexpr int K4_LDS = 65536;
__device__ __forceinline__ void k4_body(const char* ws, u16* out, LAS char* smem) {
  const int tid = threadIdx.x, lane = tid & 63, w = __builtin_amdgcn_readfirstlane(tid >> 6);
  const int xcd = blockIdx.x & 7, r = blockIdx.x >> 3, tb = 4 * xcd + r / 7, cb = r % 7;
  const v4u* hp = (const v4u*)(ws + WS_HT) + ((size_t)tb * 56 + 14 * w) * 256 + lane;
  const v4u* wp = (const v4u*)(ws + WS_W2T) + ((size_t)cb * 56 + 14 * w) * 256 + lane;
  constexpr int WIN = 6;
  v4u hb[WIN][4], wb[WIN][4];
#pragma unroll
  for (int i = 0; i < WIN; ++i)
#pragma unroll
    for (int k = 0; k < 4; ++k) { hb[i][k] = hp[i * 256 + k * 64]; wb[i][k] = wp[i * 256 + k * 64]; }
  const int h = lane >> 5, tok = tb * 64 + (w & 1) * 32 + (lane & 31), d0 = cb * 64 + (w >> 1) * 32;
  unsigned xr[8]; f32x4 gbv[4];
#pragma unroll
  for (int g = 0; g < 4; ++g) {
    const unsigned* xs = (const unsigned*)(ws + WS_X1P) + (size_t)(tok >> 5) * TILE_DW + ((d0 + 8 * g + 4 * h) >> 1) * 32 + (tok & 31);
    xr[2 * g] = xs[0]; xr[2 * g + 1] = xs[32];
    gbv[g] = *(const f32x4*)((const float*)(ws + WS_MOD) + 5 * D + d0 + 8 * g + 4 * h);
  }
  __builtin_amdgcn_sched_barrier(0);
  f32x16 acc[2][2] = {};
#pragma unroll
  for (int i = 0; i < 14; ++i) {
    v4u hc[4], wc[4];
#pragma unroll
    for (int k = 0; k < 4; ++k) { hc[k] = hb[i % WIN][k]; wc[k] = wb[i % WIN][k]; }
#pragma unroll
    for (int s = 0; s < 2; ++s)
#pragma unroll
      for (int a = 0; a < 2; ++a)
#pragma unroll
        for (int b = 0; b < 2; ++b) acc[a][b] = mfma32(wc[a * 2 + s], hc[b * 2 + s], acc[a][b]);
    if (i + WIN < 14) {
#pragma unroll
      for (int k = 0; k < 4; ++k) { hb[i % WIN][k] = hp[(i + WIN) * 256 + k * 64]; wb[i % WIN][k] = wp[(i + WIN) * 256 + k * 64]; }
    }
    __builtin_amdgcn_sched_barrier(0);
  }
  LAS f32x4* ex = (LAS f32x4*)smem;
#pragma unroll
  for (int a = 0; a < 2; ++a)
#pragma unroll
    for (int b = 0; b < 2; ++b)
#pragma unroll
      for (int g = 0; g < 4; ++g)
        ex[((w * 4 + a * 2 + b) * 4 + g) * 64 + lane] = f32x4{acc[a][b][4 * g], acc[a][b][4 * g + 1], acc[a][b][4 * g + 2], acc[a][b][4 * g + 3]};
  __syncthreads();
  unsigned o[8];
#pragma unroll
  for (int g = 0; g < 4; ++g) {
    const f32x4 v = (ex[((0 * 4 + w) * 4 + g) * 64 + lane] + ex[((1 * 4 + w) * 4 + g) * 64 + lane]) +
                    (ex[((2 * 4 + w) * 4 + g) * 64 + lane] + ex[((3 * 4 + w) * 4 + g) * 64 + lane]);
    o[2 * g] = pack2(lo2f(xr[2 * g]) + gbv[g].x * v.x, hi2f(xr[2 * g]) + gbv[g].y * v.y);
    o[2 * g + 1] = pack2(lo2f(xr[2 * g + 1]) + gbv[g].z * v.z, hi2f(xr[2 * g + 1]) + gbv[g].w * v.w);
  }
#pragma unroll
  for (int g = 0; g < 4; g += 2) {
    auto r0 = __builtin_amdgcn_permlane32_swap(o[2 * g], o[2 * g + 2], false, false);
    auto r1 = __builtin_amdgcn_permlane32_swap(o[2 * g + 1], o[2 * g + 3], false, false);
    *(v4u*)(out + (size_t)tok * D + d0 + 8 * g + 8 * h) = v4u{(unsigned)r0[0], (unsigned)r1[0], (unsigned)r0[1], (unsigned)r1[1]};
  }
}


struct AllArgs { K1Args a; const u16* Dskip; const u16* w1; u16* out; };
__device__ __forceinline__ void phase_wait(unsigned long long tgt) {
  asm volatile("s_waitcnt vmcnt(0)" ::: "memory");
  __syncthreads();
  __builtin_amdgcn_fence(__ATOMIC_RELEASE, "agent");
  asm volatile("s_waitcnt vmcnt(0)" ::: "memory");
  while (__builtin_amdgcn_s_memrealtime() < tgt) __builtin_amdgcn_s_sleep(1);
  __builtin_amdgcn_fence(__ATOMIC_ACQUIRE, "agent");
  asm volatile("s_waitcnt vmcnt(0)" ::: "memory");
  __syncthreads();
}
__global__ void __launch_bounds__(256) k_all(AllArgs p) {
  __shared__ __attribute__((aligned(16))) char smem_[K3_LDS];
  LAS char* smem = (LAS char*)smem_;
  const unsigned long long t0 = __builtin_amdgcn_s_memrealtime();
  const K1Args& a = p.a;
  for (int b = blockIdx.x; b < 610; b += 256) {
    if (b < K1_NKG) role_kgen(b, a.Are, a.Aim, a.Bre, a.Bim, a.Cre, a.Cim, a.logdt, (u16*)(a.ws + WS_KB), smem);
    else if (b < K1_NKG + K1_NLN) role_ln(b - K1_NKG, a.x, (unsigned*)(a.ws + WS_XP), (f32x2*)(a.ws + WS_ST), smem);
    else if (b < K1_NKG + K1_NLN + K1_NMOD) role_mod(b - K1_NKG - K1_NLN, a.c, a.wmod, (float*)(a.ws + WS_MOD), smem);
    else role_w2(b - K1_NKG - K1_NLN - K1_NMOD, a.w2, (v4u*)(a.ws + WS_W2T), smem);
    __syncthreads();
  }
  phase_wait(t0 + 20000);
  if (blockIdx.x < NPAIR) role_conv(blockIdx.x, a.ws, p.Dskip, smem);
  else if (blockIdx.x < NPAIR + 28) role_w1(blockIdx.x - NPAIR, p.w1, a.ws, smem);
  phase_wait(t0 + 40000);
  if (blockIdx.x < 224) k3_body(a.ws, smem);
  phase_wait(t0 + 60000);
  if (blockIdx.x < 224) k4_body(a.ws, p.out, smem);
  while (__builtin_amdgcn_s_memrealtime() < t0 + 70000) __builtin_amdgcn_s_sleep(1);
}
__global__ void k_spin(int n) { unsigned long long t0 = __builtin_amdgcn_s_memrealtime(); while (__builtin_amdgcn_s_memrealtime() - t0 < (unsigned long long)n) __builtin_amdgcn_s_sleep(1); }
extern "C" void kernel_launch(void* const* d_in, const int* in_sizes, int n_in, void* d_out, int out_size, void* d_ws,
                              size_t ws_size, hipStream_t stream) {
  AllArgs p; K1Args& a = p.a;
  a.x = (const u16*)d_in[0]; a.Are = (const u16*)d_in[1]; a.Aim = (const u16*)d_in[2]; a.Bre = (const u16*)d_in[3];
  a.Bim = (const u16*)d_in[4]; a.Cre = (const u16*)d_in[5]; a.Cim = (const u16*)d_in[6]; a.logdt = (const u16*)d_in[7];
  a.c = (const u16*)d_in[12]; a.wmod = (const u16*)d_in[13]; a.w2 = (const u16*)d_in[11]; a.ws = (char*)d_ws;
  p.Dskip = (const u16*)d_in[8]; p.w1 = (const u16*)d_in[10]; p.out = (u16*)d_out;
  k_all<<<256, 256, 0, stream>>>(p);
}
```

```cpp
#include <hip/hip_runtime.h>
#include <hip/hip_bf16.h>
#include <stdint.h>

#define WSHIFT 0
#define LAS __attribute__((address_space(3)))
typedef unsigned short u16;

struct Args {
  const u16 *x, *Are, *Aim, *Bre, *Bim, *Cre, *Cim, *logdt, *Dskip, *w2;
  char* ws; u16* out;
  const u16 *c, *wmod, *w1;
};
__global__ void __launch_bounds__(256) k_fused(Args a) {
  __shared__ __attribute__((aligned(16))) char smem_[163840];
  unsigned tid = threadIdx.x, blk = blockIdx.x;
  unsigned long long ka = (unsigned long long)__builtin_amdgcn_kernarg_segment_ptr();
  asm volatile(
"s_load_dwordx16 s[4:19], s[36:37], 0x0\n"
"s_load_dwordx8 s[20:27], s[36:37], 0x40\n"
"s_load_dwordx4 s[44:47], s[36:37], 0x68\n"
"s_getpc_b64 s[40:41]\n"
"Lpc_%=:\n"
"s_lshr_b32 s0, s38, 3\n"
"s_mul_i32 s0, s0, 13\n"
"v_min_u32 v219, 12, v0\n"
"v_add_lshl_u32 v219, v219, s0, 7\n"
"v_min_u32 v219, Lcodeend_%=-Lpc_%=-4, v219\n"
"v_mov_b32 v220, v219\n"
"s_cmp_ge_u32 s38, 224\n"
"s_cbranch_scc1 Lhelper_%=\n"
"v_and_b32 v1, 63, v0\n"
"v_lshrrev_b32 v2, 6, v0\n"
"v_lshrrev_b32 v3, 5, v1\n"
"v_readfirstlane_b32 s43, v2\n"
"v_and_b32 v4, 31, v1\n"
"v_mul_u32_u24 v5, 896, v3\n"
"v_lshl_add_u32 v5, v4, 2, v5\n"
"v_mov_b32 v8, 0\n"
"s_mul_i32 s28, s38, 7168\n"
"s_mul_i32 s29, s43, 1792\n"
"s_add_u32 s99, s28, s29\n"
"v_lshlrev_b32 v232, 4, v1\n"
"v_lshlrev_b32 v233, 2, v1\n"
"s_lshl_b32 s28, s38, 10\n"
"v_lshl_add_u32 v6, v0, 1, s28\n"
"s_lshl_b32 s28, s38, 2\n"
"v_mov_b32 v7, s28\n"
"s_mov_b64 s[32:33], s[40:41]\n"
"v_mov_b32 v24, 0\n"
"v_mov_b32 v25, 0\n"
"v_mov_b32 v26, 0\n"
"v_mov_b32 v27, 0\n"
"v_mov_b32 v28, 0\n"
"v_mov_b32 v29, 0\n"
"v_mov_b32 v30, 0\n"
"v_mov_b32 v31, 0\n"
"v_mov_b32 v32, 0\n"
"v_mov_b32 v33, 0\n"
"v_mov_b32 v34, 0\n"
"v_mov_b32 v35, 0\n"
"s_lshr_b32 s28, s38, 3\n"
"s_mul_i32 s29, s28, 37\n"
"s_lshr_b32 s29, s29, 8\n"
"s_mul_i32 s60, s29, 7\n"
"s_sub_u32 s61, s28, s60\n"
"s_and_b32 s63, s38, 7\n"
"s_lshl_b32 s63, s63, 2\n"
"s_add_u32 s63, s63, s29\n"
"s_lshr_b32 s60, s63, 1\n"
"s_and_b32 s29, s29, 1\n"
"s_mul_i32 s62, s29, 7\n"
"s_add_u32 s62, s62, s61\n"
"v_lshrrev_b32 v9, 3, v1\n"
"s_lshl_b32 s28, s43, 4\n"
"v_add_u32 v11, s28, v9\n"
"v_mul_u32_u24 v11, 28672, v11\n"
"v_and_b32 v10, 7, v1\n"
"v_lshl_add_u32 v11, v10, 4, v11\n"
"s_lshl_b32 s28, s38, 7\n"
"v_add_u32 v242, s28, v11\n"
"s_mov_b32 s84, 3751936\n"
"s_mov_b32 s85, 3981312\n"
"v_mov_b32 v224, -1\n"
"v_mov_b32 v225, -1\n"
"v_mov_b32 v226, -1\n"
"v_mov_b32 v227, -1\n"
"s_lshl_b32 s0, s38, 2\n"
"s_add_u32 s0, s0, s43\n"
"s_lshr_b32 s1, s0, 1\n"
"s_mul_i32 s1, s1, 5376\n"
"s_and_b32 s40, s0, 1\n"
"s_mul_i32 s40, s40, 1408\n"
"s_add_u32 s1, s1, s40\n"
"s_add_u32 s1, s1, 1792\n"
"v_min_u32 v217, 10, v1\n"
"v_lshl_add_u32 v217, v217, 7, s1\n"
"s_mul_i32 s1, s0, 1792\n"
"v_min_u32 v218, 13, v1\n"
"v_lshl_add_u32 v218, v218, 7, s1\n"
"s_mul_i32 s0, s43, 1824\n"
"v_mul_u32_u24 v9, 912, v3\n"
"v_lshl_add_u32 v9, v4, 2, v9\n"
"v_add_u32 v38, s0, v9\n"
"v_and_b32 v9, 3, v0\n"
"v_mul_u32_u24 v9, 1824, v9\n"
"v_lshrrev_b32 v14, 2, v0\n"
"v_lshl_add_u32 v39, v14, 2, v9\n"
"v_lshlrev_b32 v40, 8, v14\n"
"v_and_b32 v9, 3, v0\n"
"v_lshl_add_u32 v40, v9, 4, v40\n"
"s_lshr_b32 s0, s38, 2\n"
"s_mul_i32 s94, s0, 57344\n"
"s_and_b32 s0, s38, 3\n"
"s_lshl_b32 s0, s0, 6\n"
"s_add_u32 s94, s94, s0\n"
"s_add_u32 s94, s94, 81920\n"
"s_lshl_b32 s2, s38, 5\n"
"s_lshr_b32 s0, s43, 1\n"
"s_lshl_b32 s0, s0, 4\n"
"s_add_u32 s2, s2, s0\n"
"s_and_b32 s0, s43, 1\n"
"s_lshl_b32 s0, s0, 13\n"
"s_add_u32 s2, s2, s0\n"
"s_add_u32 s2, s2, 8192\n"
"v_lshlrev_b32 v41, 3, v3\n"
"v_mov_b32 v42, 0\n"
"v_mov_b32 v43, 0\n"
"v_mov_b32 v44, 0x3f803f80\n"
"s_lshr_b32 s28, s43, 1\n"
"s_and_b32 s29, s43, 1\n"
"s_waitcnt lgkmcnt(0)\n"
"s_add_u32 s4, s4, s99\n"
"s_addc_u32 s5, s5, 0\n"
"global_load_dword v23, v8, s[24:25] sc1\n"
"global_load_dword v16, v5, s[4:5] offset:0\n"
"global_load_dword v17, v5, s[4:5] offset:128\n"
"global_load_dword v18, v5, s[4:5] offset:256\n"
"global_load_dword v19, v5, s[4:5] offset:384\n"
"global_load_dword v20, v5, s[4:5] offset:512\n"
"global_load_dword v21, v5, s[4:5] offset:640\n"
"global_load_dword v22, v5, s[4:5] offset:768\n"
"global_load_dword v36, v7, s[18:19]\n"
"global_load_short_d16_hi v24, v6, s[6:7] offset:0\n"
"global_load_short_d16_hi v25, v6, s[8:9] offset:0\n"
"global_load_short_d16_hi v26, v6, s[10:11] offset:0\n"
"global_load_short_d16_hi v27, v6, s[12:13] offset:0\n"
"global_load_short_d16_hi v28, v6, s[14:15] offset:0\n"
"global_load_short_d16_hi v29, v6, s[16:17] offset:0\n"
"global_load_short_d16_hi v30, v6, s[6:7] offset:512\n"
"global_load_short_d16_hi v31, v6, s[8:9] offset:512\n"
"global_load_short_d16_hi v32, v6, s[10:11] offset:512\n"
"global_load_short_d16_hi v33, v6, s[12:13] offset:512\n"
"global_load_short_d16_hi v34, v6, s[14:15] offset:512\n"
"global_load_short_d16_hi v35, v6, s[16:17] offset:512\n"
"global_load_dword v37, v7, s[20:21]\n"
"global_load_dword v255, v220, s[32:33]\n"
"s_mov_b32 s48, s24\n"
"s_mov_b32 s49, s25\n"
"s_mov_b32 s50, 14532608\n"
"s_mov_b32 s51, 0x20000\n"
"buffer_store_dwordx4 v[224:227], v242, s[48:51], s84 offen sc1\n"
"buffer_store_dwordx4 v[224:227], v242, s[48:51], s85 offen sc1\n"
"s_mov_b32 s56, s22\n"
"s_mov_b32 s57, s23\n"
"s_mov_b32 s58, 1605632\n"
"s_mov_b32 s59, 0x20000\n"
"s_mov_b32 s52, s26\n"
"s_mov_b32 s53, s27\n"
"s_mov_b32 s54, 1835008\n"
"s_mov_b32 s55, 0x20000\n"
"s_lshl_b32 s0, s60, 1\n"
"s_add_u32 s0, s0, s28\n"
"s_mul_i32 s82, s0, 229376\n"
"s_lshl_b32 s0, s29, 11\n"
"s_add_u32 s82, s82, s0\n"
"s_lshl_b32 s0, s62, 14\n"
"s_add_u32 s82, s82, s0\n"
"s_add_u32 s82, s82, 7192576\n"
"s_mul_i32 s42, s43, 8704\n"
"s_add_u32 s42, s42, 9216\n"
"v_mul_u32_u24 v9, 272, v4\n"
"v_lshl_add_u32 v9, v3, 4, v9\n"
"v_add_u32 v210, s42, v9\n"
"s_lshl_b32 s0, s43, 3\n"
"v_xor_b32 v9, s0, v4\n"
"v_mul_u32_u24 v9, 272, v9\n"
"v_lshl_add_u32 v9, v3, 4, v9\n"
"v_add_u32 v211, s42, v9\n"
"v_lshrrev_b32 v9, 3, v1\n"
"v_bfe_u32 v11, v1, 4, 1\n"
"v_lshlrev_b32 v11, 2, v11\n"
"v_xor_b32 v11, v11, v10\n"
"v_mul_u32_u24 v9, 896, v9\n"
"v_lshl_add_u32 v236, v11, 4, v9\n"
"v_add_u32 v237, 6144, v236\n"
"v_add_u32 v238, 28672, v236\n"
"v_add_u32 v239, 34816, v236\n"
"s_mul_i32 s83, s43, 28672\n"
"s_add_u32 s83, s83, 49152\n"
"s_mul_i32 s98, s43, 7\n"
"s_lshl_b32 s87, s61, 7\n"
"s_mul_i32 s88, s63, 229376\n"
"s_add_u32 s88, s88, 7192576\n"
"s_add_u32 s0, s98, 0\n"
"s_lshr_b32 s1, s0, 1\n"
"s_and_b32 s0, s0, 1\n"
"s_mul_i32 s65, s1, 114688\n"
"s_mul_i32 s72, s0, 14336\n"
"s_add_u32 s65, s65, s72\n"
"s_add_u32 s65, s65, s87\n"
"s_lshl_b32 s1, s1, 14\n"
"s_lshl_b32 s0, s0, 10\n"
"s_add_u32 s72, s1, s0\n"
"s_add_u32 s72, s72, s88\n"
"s_add_u32 s0, s98, 1\n"
"s_lshr_b32 s1, s0, 1\n"
"s_and_b32 s0, s0, 1\n"
"s_mul_i32 s66, s1, 114688\n"
"s_mul_i32 s73, s0, 14336\n"
"s_add_u32 s66, s66, s73\n"
"s_add_u32 s66, s66, s87\n"
"s_lshl_b32 s1, s1, 14\n"
"s_lshl_b32 s0, s0, 10\n"
"s_add_u32 s73, s1, s0\n"
"s_add_u32 s73, s73, s88\n"
"s_add_u32 s0, s98, 2\n"
"s_lshr_b32 s1, s0, 1\n"
"s_and_b32 s0, s0, 1\n"
"s_mul_i32 s67, s1, 114688\n"
"s_mul_i32 s74, s0, 14336\n"
"s_add_u32 s67, s67, s74\n"
"s_add_u32 s67, s67, s87\n"
"s_lshl_b32 s1, s1, 14\n"
"s_lshl_b32 s0, s0, 10\n"
"s_add_u32 s74, s1, s0\n"
"s_add_u32 s74, s74, s88\n"
"s_add_u32 s0, s98, 3\n"
"s_lshr_b32 s1, s0, 1\n"
"s_and_b32 s0, s0, 1\n"
"s_mul_i32 s68, s1, 114688\n"
"s_mul_i32 s75, s0, 14336\n"
"s_add_u32 s68, s68, s75\n"
"s_add_u32 s68, s68, s87\n"
"s_lshl_b32 s1, s1, 14\n"
"s_lshl_b32 s0, s0, 10\n"
"s_add_u32 s75, s1, s0\n"
"s_add_u32 s75, s75, s88\n"
"s_add_u32 s0, s98, 4\n"
"s_lshr_b32 s1, s0, 1\n"
"s_and_b32 s0, s0, 1\n"
"s_mul_i32 s69, s1, 114688\n"
"s_mul_i32 s76, s0, 14336\n"
"s_add_u32 s69, s69, s76\n"
"s_add_u32 s69, s69, s87\n"
"s_lshl_b32 s1, s1, 14\n"
"s_lshl_b32 s0, s0, 10\n"
"s_add_u32 s76, s1, s0\n"
"s_add_u32 s76, s76, s88\n"
"s_add_u32 s0, s98, 5\n"
"s_lshr_b32 s1, s0, 1\n"
"s_and_b32 s0, s0, 1\n"
"s_mul_i32 s70, s1, 114688\n"
"s_mul_i32 s77, s0, 14336\n"
"s_add_u32 s70, s70, s77\n"
"s_add_u32 s70, s70, s87\n"
"s_lshl_b32 s1, s1, 14\n"
"s_lshl_b32 s0, s0, 10\n"
"s_add_u32 s77, s1, s0\n"
"s_add_u32 s77, s77, s88\n"
"s_add_u32 s0, s98, 6\n"
"s_lshr_b32 s1, s0, 1\n"
"s_and_b32 s0, s0, 1\n"
"s_mul_i32 s71, s1, 114688\n"
"s_mul_i32 s78, s0, 14336\n"
"s_add_u32 s71, s71, s78\n"
"s_add_u32 s71, s71, s87\n"
"s_lshl_b32 s1, s1, 14\n"
"s_lshl_b32 s0, s0, 10\n"
"s_add_u32 s78, s1, s0\n"
"s_add_u32 s78, s78, s88\n"
"s_and_b32 s0, s63, 3\n"
"s_lshl_b32 s0, s0, 2\n"
"s_add_u32 s0, s0, s43\n"
"s_mul_i32 s0, s0, 100352\n"
"s_lshl_b32 s1, s61, 7\n"
"s_add_u32 s0, s0, s1\n"
"v_min_u32 v219, 55, v1\n"
"v_mul_u32_u24 v219, 896, v219\n"
"v_add_u32 v219, s0, v219\n"
"v_add_u32 v228, 50176, v219\n"
"v_lshrrev_b32 v9, 3, v1\n"
"s_lshl_b32 s0, s43, 4\n"
"v_add_u32 v9, s0, v9\n"
"v_mul_u32_u24 v9, 57344, v9\n"
"v_lshl_add_u32 v9, v10, 5, v9\n"
"s_lshl_b32 s1, s38, 8\n"
"v_add_u32 v208, s1, v9\n"
"s_lshl_b32 s0, s43, 11\n"
"v_add_u32 v209, s0, v232\n"
"s_mov_b32 s30, 81920\n"
"s_mov_b32 s31, 540672\n"
"s_mov_b32 s34, 8192\n"
"s_mov_b32 s35, 16384\n"
"s_max_u32 s0, s43, 1\n"
"s_mul_i32 s87, s0, 917504\n"
"s_add_u32 s87, s87, s1\n"
"s_add_u32 s87, s87, 24800\n"
"s_lshl_b32 s0, s0, 11\n"
"s_add_u32 s0, s0, 8176\n"
"s_lshl_b32 s1, s38, 4\n"
"s_add_u32 s1, s1, 32768\n"
"v_mov_b32 v216, s87\n"
"s_add_u32 s87, s87, 16\n"
"v_writelane_b32 v216, s87, 1\n"
"v_writelane_b32 v216, s0, 2\n"
"s_add_u32 s0, s0, 8192\n"
"v_writelane_b32 v216, s0, 3\n"
"v_writelane_b32 v216, s1, 4\n"
"s_add_u32 s0, s1, 24576\n"
"v_writelane_b32 v216, s0, 5\n"
"s_add_u32 s0, s1, 3584\n"
"v_writelane_b32 v216, s0, 6\n"
"s_add_u32 s0, s0, 24576\n"
"v_writelane_b32 v216, s0, 7\n"
"s_add_u32 s41, s1, 7168\n"
"v_lshlrev_b32 v9, 4, v4\n"
"v_lshl_add_u32 v9, v3, 3, v9\n"
"v_add_u32 v215, 480, v9\n"
"s_xor_b32 s0, s43, 1\n"
"s_mul_i32 s0, s0, 8704\n"
"s_add_u32 s0, s0, 9216\n"
"v_add_u32 v212, s0, v232\n"
"s_xor_b32 s0, s43, 2\n"
"s_mul_i32 s0, s0, 8704\n"
"s_add_u32 s0, s0, 10240\n"
"v_add_u32 v213, s0, v232\n"
"s_xor_b32 s0, s43, 3\n"
"s_mul_i32 s0, s0, 8704\n"
"s_add_u32 s0, s0, 11264\n"
"v_add_u32 v214, s0, v232\n"
"v_and_b32 v12, 15, v1\n"
"v_lshrrev_b32 v13, 4, v1\n"
"v_sub_u32 v9, 3, v12\n"
"v_and_b32 v9, 3, v9\n"
"s_lshl_b32 s0, s28, 2\n"
"v_add_u32 v14, s0, v9\n"
"v_mul_u32_u24 v14, 4160, v14\n"
"v_lshlrev_b32 v15, 3, v13\n"
"v_sub_u32 v15, v15, v12\n"
"v_sub_u32 v15, v15, v9\n"
"v_add_u32 v15, 15, v15\n"
"v_lshl_add_u32 v14, v15, 1, v14\n"
"s_lshl_b32 s1, s29, 8\n"
"s_sub_u32 s0, 9472, s1\n"
"v_add_u32 v234, s0, v14\n"
"v_lshlrev_b32 v14, 5, v12\n"
"v_lshl_add_u32 v14, v13, 4, v14\n"
"s_mul_i32 s0, s28, 4608\n"
"s_add_u32 s0, s0, 256\n"
"s_sub_u32 s0, s0, s1\n"
"v_add_u32 v235, s0, v14\n"
"v_bfe_u32 v9, v1, 2, 2\n"
"v_lshl_add_u32 v14, v3, 2, v9\n"
"v_lshlrev_b32 v14, 7, v14\n"
"v_bfe_u32 v15, v1, 4, 1\n"
"v_lshl_add_u32 v14, v15, 5, v14\n"
"v_and_b32 v15, 3, v1\n"
"v_lshl_add_u32 v14, v15, 3, v14\n"
"v_add_u32 v14, s83, v14\n"
"v_lshrrev_b32 v9, 1, v9\n"
"v_xor_b32 v9, s28, v9\n"
"v_lshl_add_u32 v240, v9, 6, v14\n"
"v_xor_b32 v9, 1, v9\n"
"v_lshl_add_u32 v241, v9, 6, v14\n"
"s_lshl_b32 s0, s43, 10\n"
"s_sub_u32 s0, 12792, s0\n"
"v_lshlrev_b32 v9, 3, v1\n"
"v_sub_u32 v243, s0, v9\n"
"v_lshrrev_b32 v9, 2, v1\n"
"v_lshlrev_b32 v14, 2, v15\n"
"v_xor_b32 v9, v9, v14\n"
"v_lshl_add_u32 v9, v15, 4, v9\n"
"v_lshlrev_b32 v9, 4, v9\n"
"s_lshl_b32 s0, s43, 11\n"
"v_add_u32 v244, s0, v9\n"
"v_lshlrev_b32 v9, 2, v13\n"
"v_xor_b32 v9, v9, v12\n"
"v_lshl_add_u32 v9, v13, 4, v9\n"
"v_lshlrev_b32 v9, 4, v9\n"
"s_lshl_b32 s0, s43, 13\n"
"v_add_u32 v245, s0, v9\n"
"s_lshl_b32 s0, s29, 11\n"
"v_add_u32 v246, s0, v232\n"
"v_xor_b32 v247, 0x800, v246\n"
"v_lshlrev_b32 v9, 4, v3\n"
"v_add_u32 v248, 42496, v9\n"
"s_lshl_b32 s0, s63, 1\n"
"s_add_u32 s0, s0, s29\n"
"s_mul_i32 s1, s0, 28672\n"
"s_lshl_b32 s87, s61, 6\n"
"s_lshl_b32 s88, s28, 5\n"
"s_add_u32 s87, s87, s88\n"
"s_lshl_b32 s88, s87, 6\n"
"s_add_u32 s88, s88, s1\n"
"v_lshlrev_b32 v14, 8, v3\n"
"v_lshl_add_u32 v14, v4, 2, v14\n"
"v_add_u32 v249, s88, v14\n"
"s_lshl_b32 s88, s61, 9\n"
"v_lshl_add_u32 v250, v1, 3, s88\n"
"s_lshl_b32 s88, s87, 1\n"
"s_add_u32 s1, s1, s88\n"
"v_mul_u32_u24 v14, 896, v4\n"
"v_add_u32 v14, v14, v9\n"
"v_add_u32 v251, s1, v14\n"
"s_lshl_b32 s0, s43, 14\n"
"v_add_u32 v252, s0, v232\n"
"v_add_u32 v253, 49152, v252\n"
"s_lshl_b32 s0, s60, 2\n"
"s_add_u32 s0, s0, s43\n"
"s_mul_i32 s79, s0, 28672\n"
"s_add_u32 s79, s79, 3751936\n"
"s_lshl_b32 s0, s62, 2\n"
"s_add_u32 s0, s0, s43\n"
"s_mul_i32 s81, s0, 28672\n"
"s_add_u32 s81, s81, 5586944\n"
"s_mov_b32 s89, 50688\n"
"s_mov_b32 s86, 0x40000\n"
"s_lshl_b32 s64, s62, 9\n"
"s_lshl_b32 s80, s29, 8\n"
"s_add_u32 s64, s64, s80\n"
"s_add_u32 s64, s64, 24576\n"
"s_add_u32 s80, s80, 42496\n"
"s_mov_b32 s3, 4096\n"
"v_mov_b32 v3, 0\n"
"s_waitcnt vmcnt(24)\n"
"v_add_u32 v23, 1, v23\n"
"s_waitcnt vmcnt(23)\n"
"ds_write_b32 v38, v16 offset:0\n"
"s_waitcnt vmcnt(22)\n"
"ds_write_b32 v38, v17 offset:128\n"
"s_waitcnt vmcnt(21)\n"
"ds_write_b32 v38, v18 offset:256\n"
"s_waitcnt vmcnt(20)\n"
"ds_write_b32 v38, v19 offset:384\n"
"s_waitcnt vmcnt(19)\n"
"ds_write_b32 v38, v20 offset:512\n"
"s_waitcnt vmcnt(18)\n"
"ds_write_b32 v38, v21 offset:640\n"
"s_waitcnt vmcnt(17)\n"
"ds_write_b32 v38, v22 offset:768\n"
"v_readfirstlane_b32 s39, v23\n"
"v_dot2c_f32_bf16 v42, v16, v44\n"
"v_dot2c_f32_bf16 v43, v16, v16\n"
"v_dot2c_f32_bf16 v42, v17, v44\n"
"v_dot2c_f32_bf16 v43, v17, v17\n"
"v_dot2c_f32_bf16 v42, v18, v44\n"
"v_dot2c_f32_bf16 v43, v18, v18\n"
"v_dot2c_f32_bf16 v42, v19, v44\n"
"v_dot2c_f32_bf16 v43, v19, v19\n"
"v_dot2c_f32_bf16 v42, v20, v44\n"
"v_dot2c_f32_bf16 v43, v20, v20\n"
"v_dot2c_f32_bf16 v42, v21, v44\n"
"v_dot2c_f32_bf16 v43, v21, v21\n"
"v_dot2c_f32_bf16 v42, v22, v44\n"
"v_dot2c_f32_bf16 v43, v22, v22\n"
"s_waitcnt lgkmcnt(0)\n"
"s_barrier\n"
"ds_read_b32 v96, v39 offset:0\n"
"ds_read_b32 v98, v39 offset:912\n"
"ds_read_b32 v100, v39 offset:256\n"
"ds_read_b32 v102, v39 offset:1168\n"
"ds_read_b32 v104, v39 offset:512\n"
"ds_read_b32 v106, v39 offset:1424\n"
"ds_read_b32 v108, v39 offset:768\n"
"ds_read_b32 v110, v39 offset:1680\n"
"v_add_f32_dpp v42, v42, v42 quad_perm:[1,0,3,2] row_mask:0xf bank_mask:0xf bound_ctrl:1\n"
"v_add_f32_dpp v43, v43, v43 quad_perm:[1,0,3,2] row_mask:0xf bank_mask:0xf bound_ctrl:1\n"
"v_mov_b32 v97, v23\n"
"v_mov_b32 v99, v23\n"
"v_add_f32_dpp v42, v42, v42 quad_perm:[2,3,0,1] row_mask:0xf bank_mask:0xf bound_ctrl:1\n"
"v_add_f32_dpp v43, v43, v43 quad_perm:[2,3,0,1] row_mask:0xf bank_mask:0xf bound_ctrl:1\n"
"v_mov_b32 v101, v23\n"
"v_mov_b32 v103, v23\n"
"v_add_f32_dpp v42, v42, v42 row_half_mirror row_mask:0xf bank_mask:0xf bound_ctrl:1\n"
"v_add_f32_dpp v43, v43, v43 row_half_mirror row_mask:0xf bank_mask:0xf bound_ctrl:1\n"
"v_mov_b32 v105, v23\n"
"v_mov_b32 v107, v23\n"
"v_add_f32_dpp v42, v42, v42 row_mirror row_mask:0xf bank_mask:0xf bound_ctrl:1\n"
"v_add_f32_dpp v43, v43, v43 row_mirror row_mask:0xf bank_mask:0xf bound_ctrl:1\n"
"v_mov_b32 v109, v23\n"
"v_mov_b32 v111, v23\n"
"v_mov_b32 v45, v42\n"
"v_mov_b32 v44, v43\n"
"s_add_u32 s0, s94, 0\n"
"s_waitcnt lgkmcnt(6)\n"
"buffer_store_dwordx4 v[96:99], v40, s[48:51], s0 offen sc1\n"
"v_permlane16_swap_b32 v42, v45\n"
"v_permlane16_swap_b32 v43, v44\n"
"v_add_f32 v42, v42, v45\n"
"v_add_f32 v43, v43, v44\n"
"s_add_u32 s0, s94, 16384\n"
"s_waitcnt lgkmcnt(4)\n"
"buffer_store_dwordx4 v[100:103], v40, s[48:51], s0 offen sc1\n"
"s_add_u32 s0, s94, 32768\n"
"s_waitcnt lgkmcnt(2)\n"
"buffer_store_dwordx4 v[104:107], v40, s[48:51], s0 offen sc1\n"
"s_cmp_gt_u32 s43, 1\n"
"s_cbranch_scc1 Lxg_%=\n"
"s_add_u32 s0, s94, 49152\n"
"s_waitcnt lgkmcnt(0)\n"
"buffer_store_dwordx4 v[108:111], v40, s[48:51], s0 offen sc1\n"
"Lxg_%=:\n"
"v_mul_f32 v42, 0x3b124925, v42\n"
"s_mov_b32 s98, 0x3fb8aa3b\n"
"s_waitcnt vmcnt(19)\n"
"v_lshlrev_b32 v46, 16, v36\n"
"v_mul_f32 v43, 0x3b124925, v43\n"
"v_and_b32 v47, 0xffff0000, v36\n"
"v_mul_f32 v48, s98, v46\n"
"v_fma_f32 v43, -v42, v42, v43\n"
"v_mul_f32 v50, s98, v47\n"
"v_fma_f32 v49, v46, s98, -v48\n"
"v_max_f32 v43, 0, v43\n"
"v_fma_f32 v51, v47, s98, -v50\n"
"v_fmac_f32 v49, 0x32a57060, v46\n"
"v_cvt_pk_bf16_f32 v43, v43, v43\n"
"v_fmac_f32 v51, 0x32a57060, v47\n"
"v_exp_f32 v48, v48\n"
"v_lshlrev_b32 v43, 16, v43\n"
"v_exp_f32 v50, v50\n"
"v_mul_f32 v49, 0x3f317218, v49\n"
"v_add_f32 v43, 0x3727c5ac, v43\n"
"v_mul_f32 v51, 0x3f317218, v51\n"
"v_fmac_f32 v48, v48, v49\n"
"v_cvt_pk_bf16_f32 v43, v43, v43\n"
"v_fmac_f32 v50, v50, v51\n"
"v_cvt_pk_bf16_f32 v46, v48, v48\n"
"v_lshlrev_b32 v43, 16, v43\n"
"v_cvt_pk_bf16_f32 v47, v50, v50\n"
"v_lshlrev_b32 v46, 16, v46\n"
"v_sqrt_f32 v43, v43\n"
"v_lshlrev_b32 v47, 16, v47\n"
"v_cvt_pk_bf16_f32 v22, v42, v43\n"
"v_mov_b32 v221, v23\n"
"v_mov_b32 v223, v23\n"
"s_mov_b32 m0, s42\n"
"s_waitcnt vmcnt(18)\n"
"v_mul_f32 v24, v46, v24\n"
"s_waitcnt vmcnt(17)\n"
"v_mul_f32 v25, v46, v25\n"
"s_waitcnt vmcnt(16)\n"
"v_mul_f32 v26, v46, v26\n"
"s_waitcnt vmcnt(15)\n"
"v_mul_f32 v27, v46, v27\n"
"v_mul_f32 v50, 0x3fb8aa3b, v24\n"
"v_mul_f32 v51, 0x3e22f983, v25\n"
"v_mul_f32 v56, 0x41000000, v50\n"
"v_mul_f32 v57, 0x41000000, v51\n"
"v_mul_f32 v58, 0x41800000, v50\n"
"v_mul_f32 v59, 0x41800000, v51\n"
"v_mul_f32 v60, 0x43800000, v50\n"
"v_mul_f32 v61, 0x43800000, v51\n"
"v_fract_f32 v57, v57\n"
"v_fract_f32 v59, v59\n"
"v_fract_f32 v61, v61\n"
"v_fract_f32 v51, v51\n"
"v_exp_f32 v74, v56\n"
"v_cos_f32 v56, v57\n"
"v_sin_f32 v57, v57\n"
"v_exp_f32 v75, v58\n"
"v_cos_f32 v58, v59\n"
"v_sin_f32 v59, v59\n"
"v_exp_f32 v76, v60\n"
"v_cos_f32 v60, v61\n"
"v_sin_f32 v61, v61\n"
"v_exp_f32 v77, v50\n"
"v_cos_f32 v50, v51\n"
"v_sin_f32 v51, v51\n"
"v_mul_f32 v56, v56, v74\n"
"v_mul_f32 v57, v57, v74\n"
"v_mul_f32 v58, v58, v75\n"
"v_mul_f32 v59, v59, v75\n"
"v_mul_f32 v60, v60, v76\n"
"v_mul_f32 v61, v61, v76\n"
"v_mul_f32 v50, v50, v77\n"
"v_mul_f32 v51, v51, v77\n"
"v_mov_b32 v77, 0x3f80\n"
"s_waitcnt vmcnt(13)\n"
"v_pk_mul_f32 v[66:67], v[28:29], v[26:27] op_sel_hi:[1,0]\n"
"v_pk_mul_f32 v[68:69], v[56:57], v[58:59] op_sel_hi:[1,0]\n"
"v_pk_fma_f32 v[48:49], v[28:29], v[26:27], v[66:67] op_sel:[1,1,0] op_sel_hi:[0,1,1] neg_lo:[1,0,0]\n"
"v_pk_fma_f32 v[64:65], v[56:57], v[58:59], v[68:69] op_sel:[1,1,0] op_sel_hi:[0,1,1] neg_lo:[1,0,0]\n"
"v_cvt_pk_bf16_f32 v74, v56, v57\n"
"ds_write_addtid_b32 v77 offset:0\n"
"ds_write_addtid_b32 v74 offset:272\n"
"v_cvt_pk_bf16_f32 v75, v58, v59\n"
"v_cvt_pk_bf16_f32 v76, v64, v65\n"
"v_pk_mul_f32 v[66:67], v[58:59], v[58:59] op_sel_hi:[1,0]\n"
"v_pk_mul_f32 v[68:69], v[64:65], v[58:59] op_sel_hi:[1,0]\n"
"ds_write_addtid_b32 v75 offset:544\n"
"v_pk_fma_f32 v[62:63], v[58:59], v[58:59], v[66:67] op_sel:[1,1,0] op_sel_hi:[0,1,1] neg_lo:[1,0,0]\n"
"ds_write_addtid_b32 v76 offset:816\n"
"v_pk_fma_f32 v[64:65], v[64:65], v[58:59], v[68:69] op_sel:[1,1,0] op_sel_hi:[0,1,1] neg_lo:[1,0,0]\n"
"v_cvt_pk_bf16_f32 v77, v62, v63\n"
"v_cvt_pk_bf16_f32 v74, v64, v65\n"
"v_pk_mul_f32 v[66:67], v[62:63], v[58:59] op_sel_hi:[1,0]\n"
"v_pk_mul_f32 v[68:69], v[64:65], v[58:59] op_sel_hi:[1,0]\n"
"ds_write_addtid_b32 v77 offset:1088\n"
"v_pk_fma_f32 v[62:63], v[62:63], v[58:59], v[66:67] op_sel:[1,1,0] op_sel_hi:[0,1,1] neg_lo:[1,0,0]\n"
"ds_write_addtid_b32 v74 offset:1360\n"
"v_pk_fma_f32 v[64:65], v[64:65], v[58:59], v[68:69] op_sel:[1,1,0] op_sel_hi:[0,1,1] neg_lo:[1,0,0]\n"
"v_cvt_pk_bf16_f32 v75, v62, v63\n"
"v_cvt_pk_bf16_f32 v76, v64, v65\n"
"v_pk_mul_f32 v[66:67], v[62:63], v[58:59] op_sel_hi:[1,0]\n"
"v_pk_mul_f32 v[68:69], v[64:65], v[58:59] op_sel_hi:[1,0]\n"
"ds_write_addtid_b32 v75 offset:1632\n"
"v_pk_fma_f32 v[62:63], v[62:63], v[58:59], v[66:67] op_sel:[1,1,0] op_sel_hi:[0,1,1] neg_lo:[1,0,0]\n"
"ds_write_addtid_b32 v76 offset:1904\n"
"v_pk_fma_f32 v[64:65], v[64:65], v[58:59], v[68:69] op_sel:[1,1,0] op_sel_hi:[0,1,1] neg_lo:[1,0,0]\n"
"v_cvt_pk_bf16_f32 v77, v62, v63\n"
"v_cvt_pk_bf16_f32 v74, v64, v65\n"
"v_pk_mul_f32 v[66:67], v[62:63], v[58:59] op_sel_hi:[1,0]\n"
"v_pk_mul_f32 v[68:69], v[64:65], v[58:59] op_sel_hi:[1,0]\n"
"ds_write_addtid_b32 v77 offset:2176\n"
"v_pk_fma_f32 v[62:63], v[62:63], v[58:59], v[66:67] op_sel:[1,1,0] op_sel_hi:[0,1,1] neg_lo:[1,0,0]\n"
"ds_write_addtid_b32 v74 offset:2448\n"
"v_pk_fma_f32 v[64:65], v[64:65], v[58:59], v[68:69] op_sel:[1,1,0] op_sel_hi:[0,1,1] neg_lo:[1,0,0]\n"
"s_waitcnt vmcnt(3)\n"
"s_mov_b32 exec_lo, 1\n"
"s_mov_b32 exec_hi, 1\n"
"buffer_store_dwordx2 v[22:23], v41, s[48:51], s2 offen sc1\n"
"s_mov_b64 exec, -1\n"
"global_load_dword v255, v217, s[44:45]\n"
"v_cvt_pk_bf16_f32 v75, v62, v63\n"
"v_cvt_pk_bf16_f32 v76, v64, v65\n"
"v_pk_mul_f32 v[66:67], v[62:63], v[58:59] op_sel_hi:[1,0]\n"
"v_pk_mul_f32 v[68:69], v[64:65], v[58:59] op_sel_hi:[1,0]\n"
"ds_write_addtid_b32 v75 offset:2720\n"
"v_pk_fma_f32 v[62:63], v[62:63], v[58:59], v[66:67] op_sel:[1,1,0] op_sel_hi:[0,1,1] neg_lo:[1,0,0]\n"
"ds_write_addtid_b32 v76 offset:2992\n"
"v_pk_fma_f32 v[64:65], v[64:65], v[58:59], v[68:69] op_sel:[1,1,0] op_sel_hi:[0,1,1] neg_lo:[1,0,0]\n"
"v_cvt_pk_bf16_f32 v77, v62, v63\n"
"v_cvt_pk_bf16_f32 v74, v64, v65\n"
"v_pk_mul_f32 v[66:67], v[62:63], v[58:59] op_sel_hi:[1,0]\n"
"v_pk_mul_f32 v[68:69], v[64:65], v[58:59] op_sel_hi:[1,0]\n"
"ds_write_addtid_b32 v77 offset:3264\n"
"v_pk_fma_f32 v[62:63], v[62:63], v[58:59], v[66:67] op_sel:[1,1,0] op_sel_hi:[0,1,1] neg_lo:[1,0,0]\n"
"ds_write_addtid_b32 v74 offset:3536\n"
"v_pk_fma_f32 v[64:65], v[64:65], v[58:59], v[68:69] op_sel:[1,1,0] op_sel_hi:[0,1,1] neg_lo:[1,0,0]\n"
"v_cvt_pk_bf16_f32 v75, v62, v63\n"
"v_cvt_pk_bf16_f32 v76, v64, v65\n"
"v_pk_mul_f32 v[66:67], v[62:63], v[58:59] op_sel_hi:[1,0]\n"
"v_pk_mul_f32 v[68:69], v[64:65], v[58:59] op_sel_hi:[1,0]\n"
"ds_write_addtid_b32 v75 offset:3808\n"
"v_pk_fma_f32 v[62:63], v[62:63], v[58:59], v[66:67] op_sel:[1,1,0] op_sel_hi:[0,1,1] neg_lo:[1,0,0]\n"
"ds_write_addtid_b32 v76 offset:4080\n"
"v_pk_fma_f32 v[64:65], v[64:65], v[58:59], v[68:69] op_sel:[1,1,0] op_sel_hi:[0,1,1] neg_lo:[1,0,0]\n"
"v_cvt_pk_bf16_f32 v77, v62, v63\n"
"v_cvt_pk_bf16_f32 v74, v64, v65\n"
"v_pk_mul_f32 v[66:67], v[62:63], v[58:59] op_sel_hi:[1,0]\n"
"v_pk_mul_f32 v[68:69], v[64:65], v[58:59] op_sel_hi:[1,0]\n"
"ds_write_addtid_b32 v77 offset:4352\n"
"v_pk_fma_f32 v[62:63], v[62:63], v[58:59], v[66:67] op_sel:[1,1,0] op_sel_hi:[0,1,1] neg_lo:[1,0,0]\n"
"ds_write_addtid_b32 v74 offset:4624\n"
"v_pk_fma_f32 v[64:65], v[64:65], v[58:59], v[68:69] op_sel:[1,1,0] op_sel_hi:[0,1,1] neg_lo:[1,0,0]\n"
"v_cvt_pk_bf16_f32 v75, v62, v63\n"
"v_cvt_pk_bf16_f32 v76, v64, v65\n"
"v_pk_mul_f32 v[66:67], v[62:63], v[58:59] op_sel_hi:[1,0]\n"
"v_pk_mul_f32 v[68:69], v[64:65], v[58:59] op_sel_hi:[1,0]\n"
"ds_write_addtid_b32 v75 offset:4896\n"
"v_pk_fma_f32 v[62:63], v[62:63], v[58:59], v[66:67] op_sel:[1,1,0] op_sel_hi:[0,1,1] neg_lo:[1,0,0]\n"
"ds_write_addtid_b32 v76 offset:5168\n"
"v_pk_fma_f32 v[64:65], v[64:65], v[58:59], v[68:69] op_sel:[1,1,0] op_sel_hi:[0,1,1] neg_lo:[1,0,0]\n"
"v_cvt_pk_bf16_f32 v77, v62, v63\n"
"v_cvt_pk_bf16_f32 v74, v64, v65\n"
"v_pk_mul_f32 v[66:67], v[62:63], v[58:59] op_sel_hi:[1,0]\n"
"v_pk_mul_f32 v[68:69], v[64:65], v[58:59] op_sel_hi:[1,0]\n"
"ds_write_addtid_b32 v77 offset:5440\n"
"v_pk_fma_f32 v[62:63], v[62:63], v[58:59], v[66:67] op_sel:[1,1,0] op_sel_hi:[0,1,1] neg_lo:[1,0,0]\n"
"ds_write_addtid_b32 v74 offset:5712\n"
"v_pk_fma_f32 v[64:65], v[64:65], v[58:59], v[68:69] op_sel:[1,1,0] op_sel_hi:[0,1,1] neg_lo:[1,0,0]\n"
"global_load_dword v255, v218, s[46:47]\n"
"v_cvt_pk_bf16_f32 v75, v62, v63\n"
"v_cvt_pk_bf16_f32 v76, v64, v65\n"
"v_pk_mul_f32 v[66:67], v[62:63], v[58:59] op_sel_hi:[1,0]\n"
"v_pk_mul_f32 v[68:69], v[64:65], v[58:59] op_sel_hi:[1,0]\n"
"ds_write_addtid_b32 v75 offset:5984\n"
"v_pk_fma_f32 v[62:63], v[62:63], v[58:59], v[66:67] op_sel:[1,1,0] op_sel_hi:[0,1,1] neg_lo:[1,0,0]\n"
"ds_write_addtid_b32 v76 offset:6256\n"
"v_pk_fma_f32 v[64:65], v[64:65], v[58:59], v[68:69] op_sel:[1,1,0] op_sel_hi:[0,1,1] neg_lo:[1,0,0]\n"
"v_cvt_pk_bf16_f32 v77, v62, v63\n"
"v_cvt_pk_bf16_f32 v74, v64, v65\n"
"v_pk_mul_f32 v[66:67], v[62:63], v[58:59] op_sel_hi:[1,0]\n"
"v_pk_mul_f32 v[68:69], v[64:65], v[58:59] op_sel_hi:[1,0]\n"
"ds_write_addtid_b32 v77 offset:6528\n"
"v_pk_fma_f32 v[62:63], v[62:63], v[58:59], v[66:67] op_sel:[1,1,0] op_sel_hi:[0,1,1] neg_lo:[1,0,0]\n"
"ds_write_addtid_b32 v74 offset:6800\n"
"v_pk_fma_f32 v[64:65], v[64:65], v[58:59], v[68:69] op_sel:[1,1,0] op_sel_hi:[0,1,1] neg_lo:[1,0,0]\n"
"v_cvt_pk_bf16_f32 v75, v62, v63\n"
"v_cvt_pk_bf16_f32 v76, v64, v65\n"
"v_pk_mul_f32 v[66:67], v[62:63], v[58:59] op_sel_hi:[1,0]\n"
"v_pk_mul_f32 v[68:69], v[64:65], v[58:59] op_sel_hi:[1,0]\n"
"ds_write_addtid_b32 v75 offset:7072\n"
"v_pk_fma_f32 v[62:63], v[62:63], v[58:59], v[66:67] op_sel:[1,1,0] op_sel_hi:[0,1,1] neg_lo:[1,0,0]\n"
"ds_write_addtid_b32 v76 offset:7344\n"
"v_pk_fma_f32 v[64:65], v[64:65], v[58:59], v[68:69] op_sel:[1,1,0] op_sel_hi:[0,1,1] neg_lo:[1,0,0]\n"
"v_cvt_pk_bf16_f32 v77, v62, v63\n"
"v_cvt_pk_bf16_f32 v74, v64, v65\n"
"v_pk_mul_f32 v[66:67], v[62:63], v[58:59] op_sel_hi:[1,0]\n"
"v_pk_mul_f32 v[68:69], v[64:65], v[58:59] op_sel_hi:[1,0]\n"
"ds_write_addtid_b32 v77 offset:7616\n"
"v_pk_fma_f32 v[62:63], v[62:63], v[58:59], v[66:67] op_sel:[1,1,0] op_sel_hi:[0,1,1] neg_lo:[1,0,0]\n"
"ds_write_addtid_b32 v74 offset:7888\n"
"v_pk_fma_f32 v[64:65], v[64:65], v[58:59], v[68:69] op_sel:[1,1,0] op_sel_hi:[0,1,1] neg_lo:[1,0,0]\n"
"v_cvt_pk_bf16_f32 v75, v62, v63\n"
"v_cvt_pk_bf16_f32 v76, v64, v65\n"
"v_pk_mul_f32 v[66:67], v[50:51], v[50:51] op_sel_hi:[1,0]\n"
"v_pk_mul_f32 v[68:69], v[48:49], v[60:61] op_sel_hi:[1,0]\n"
"ds_write_addtid_b32 v75 offset:8160\n"
"v_pk_fma_f32 v[52:53], v[50:51], v[50:51], v[66:67] op_sel:[1,1,0] op_sel_hi:[0,1,1] neg_lo:[1,0,0]\n"
"ds_write_addtid_b32 v76 offset:8432\n"
"v_pk_fma_f32 v[70:71], v[48:49], v[60:61], v[68:69] op_sel:[1,1,0] op_sel_hi:[0,1,1] neg_lo:[1,0,0]\n"
"ds_read_b128 v[80:83], v210 offset:0\n"
"ds_read_b128 v[84:87], v210 offset:32\n"
"ds_read_b128 v[88:91], v210 offset:64\n"
"ds_read_b128 v[92:95], v210 offset:96\n"
"ds_read_b128 v[96:99], v210 offset:128\n"
"ds_read_b128 v[100:103], v210 offset:160\n"
"ds_read_b128 v[104:107], v210 offset:192\n"
"ds_read_b128 v[108:111], v210 offset:224\n"
"v_pk_mul_f32 v[66:67], v[52:53], v[52:53] op_sel_hi:[1,0]\n"
"v_pk_mul_f32 v[68:69], v[48:49], v[50:51] op_sel_hi:[1,0]\n"
"v_pk_fma_f32 v[54:55], v[52:53], v[52:53], v[66:67] op_sel:[1,1,0] op_sel_hi:[0,1,1] neg_lo:[1,0,0]\n"
"v_pk_fma_f32 v[62:63], v[48:49], v[50:51], v[68:69] op_sel:[1,1,0] op_sel_hi:[0,1,1] neg_lo:[1,0,0]\n"
"v_cvt_pk_bf16_f32 v77, v48, -v49\n"
"v_cvt_pk_bf16_f32 v74, v62, -v63\n"
"v_pk_mul_f32 v[66:67], v[62:63], v[50:51] op_sel_hi:[1,0]\n"
"v_pk_mul_f32 v[68:69], v[70:71], v[52:53] op_sel_hi:[1,0]\n"
"ds_write_addtid_b32 v77 offset:0\n"
"v_pk_fma_f32 v[62:63], v[62:63], v[50:51], v[66:67] op_sel:[1,1,0] op_sel_hi:[0,1,1] neg_lo:[1,0,0]\n"
"ds_write_addtid_b32 v74 offset:272\n"
"v_pk_fma_f32 v[64:65], v[70:71], v[52:53], v[68:69] op_sel:[1,1,0] op_sel_hi:[0,1,1] neg_lo:[1,0,0]\n"
"v_cvt_pk_bf16_f32 v75, v62, -v63\n"
"v_cvt_pk_bf16_f32 v76, v64, -v65\n"
"v_pk_mul_f32 v[66:67], v[62:63], v[50:51] op_sel_hi:[1,0]\n"
"v_pk_mul_f32 v[68:69], v[70:71], v[54:55] op_sel_hi:[1,0]\n"
"ds_write_addtid_b32 v75 offset:544\n"
"v_pk_fma_f32 v[62:63], v[62:63], v[50:51], v[66:67] op_sel:[1,1,0] op_sel_hi:[0,1,1] neg_lo:[1,0,0]\n"
"ds_write_addtid_b32 v76 offset:2448\n"
"v_pk_fma_f32 v[64:65], v[70:71], v[54:55], v[68:69] op_sel:[1,1,0] op_sel_hi:[0,1,1] neg_lo:[1,0,0]\n"
"global_load_dword v255, v218, s[22:23]\n"
"v_cvt_pk_bf16_f32 v77, v62, -v63\n"
"v_cvt_pk_bf16_f32 v74, v64, -v65\n"
"v_pk_mul_f32 v[66:67], v[62:63], v[50:51] op_sel_hi:[1,0]\n"
"v_pk_mul_f32 v[68:69], v[64:65], v[52:53] op_sel_hi:[1,0]\n"
"ds_write_addtid_b32 v77 offset:816\n"
"v_pk_fma_f32 v[62:63], v[62:63], v[50:51], v[66:67] op_sel:[1,1,0] op_sel_hi:[0,1,1] neg_lo:[1,0,0]\n"
"ds_write_addtid_b32 v74 offset:2720\n"
"ds_write_addtid_b32 v74 offset:3264\n"
"v_pk_fma_f32 v[64:65], v[64:65], v[52:53], v[68:69] op_sel:[1,1,0] op_sel_hi:[0,1,1] neg_lo:[1,0,0]\n"
"v_cvt_pk_bf16_f32 v75, v62, -v63\n"
"v_cvt_pk_bf16_f32 v76, v64, -v65\n"
"v_pk_mul_f32 v[66:67], v[62:63], v[50:51] op_sel_hi:[1,0]\n"
"v_pk_mul_f32 v[68:69], v[70:71], v[56:57] op_sel_hi:[1,0]\n"
"ds_write_addtid_b32 v75 offset:1088\n"
"v_pk_fma_f32 v[62:63], v[62:63], v[50:51], v[66:67] op_sel:[1,1,0] op_sel_hi:[0,1,1] neg_lo:[1,0,0]\n"
"ds_write_addtid_b32 v76 offset:3536\n"
"v_pk_fma_f32 v[64:65], v[70:71], v[56:57], v[68:69] op_sel:[1,1,0] op_sel_hi:[0,1,1] neg_lo:[1,0,0]\n"
"v_cvt_pk_bf16_f32 v77, v62, -v63\n"
"v_cvt_pk_bf16_f32 v74, v64, -v65\n"
"v_pk_mul_f32 v[66:67], v[62:63], v[50:51] op_sel_hi:[1,0]\n"
"v_pk_mul_f32 v[68:69], v[70:71], v[60:61] op_sel_hi:[1,0]\n"
"ds_write_addtid_b32 v77 offset:1360\n"
"v_pk_fma_f32 v[62:63], v[62:63], v[50:51], v[66:67] op_sel:[1,1,0] op_sel_hi:[0,1,1] neg_lo:[1,0,0]\n"
"ds_write_addtid_b32 v74 offset:3808\n"
"v_pk_fma_f32 v[72:73], v[70:71], v[60:61], v[68:69] op_sel:[1,1,0] op_sel_hi:[0,1,1] neg_lo:[1,0,0]\n"
"v_cvt_pk_bf16_f32 v75, v62, -v63\n"
"v_cvt_pk_bf16_f32 v76, v70, -v71\n"
"v_pk_mul_f32 v[66:67], v[62:63], v[50:51] op_sel_hi:[1,0]\n"
"v_pk_mul_f32 v[68:69], v[72:73], v[54:55] op_sel_hi:[1,0]\n"
"ds_write_addtid_b32 v75 offset:1632\n"
"v_pk_fma_f32 v[62:63], v[62:63], v[50:51], v[66:67] op_sel:[1,1,0] op_sel_hi:[0,1,1] neg_lo:[1,0,0]\n"
"ds_write_addtid_b32 v76 offset:2176\n"
"v_pk_fma_f32 v[64:65], v[72:73], v[54:55], v[68:69] op_sel:[1,1,0] op_sel_hi:[0,1,1] neg_lo:[1,0,0]\n"
"v_cvt_pk_bf16_f32 v77, v62, -v63\n"
"v_cvt_pk_bf16_f32 v74, v64, -v65\n"
"v_pk_mul_f32 v[66:67], v[72:73], v[56:57] op_sel_hi:[1,0]\n"
"v_pk_mul_f32 v[68:69], v[72:73], v[60:61] op_sel_hi:[1,0]\n"
"ds_write_addtid_b32 v77 offset:1904\n"
"v_pk_fma_f32 v[62:63], v[72:73], v[56:57], v[66:67] op_sel:[1,1,0] op_sel_hi:[0,1,1] neg_lo:[1,0,0]\n"
"ds_write_addtid_b32 v74 offset:4624\n"
"ds_write_addtid_b32 v74 offset:5440\n"
"v_pk_fma_f32 v[70:71], v[72:73], v[60:61], v[68:69] op_sel:[1,1,0] op_sel_hi:[0,1,1] neg_lo:[1,0,0]\n"
"v_cvt_pk_bf16_f32 v75, v62, -v63\n"
"v_cvt_pk_bf16_f32 v76, v72, -v73\n"
"v_pk_mul_f32 v[66:67], v[70:71], v[54:55] op_sel_hi:[1,0]\n"
"v_pk_mul_f32 v[68:69], v[70:71], v[56:57] op_sel_hi:[1,0]\n"
"ds_write_addtid_b32 v75 offset:5712\n"
"v_pk_fma_f32 v[62:63], v[70:71], v[54:55], v[66:67] op_sel:[1,1,0] op_sel_hi:[0,1,1] neg_lo:[1,0,0]\n"
"ds_write_addtid_b32 v76 offset:4352\n"
"v_pk_fma_f32 v[64:65], v[70:71], v[56:57], v[68:69] op_sel:[1,1,0] op_sel_hi:[0,1,1] neg_lo:[1,0,0]\n"
"v_cvt_pk_bf16_f32 v77, v62, -v63\n"
"v_cvt_pk_bf16_f32 v74, v64, -v65\n"
"v_pk_mul_f32 v[66:67], v[70:71], v[60:61] op_sel_hi:[1,0]\n"
"ds_write_addtid_b32 v77 offset:5168\n"
"ds_write_addtid_b32 v77 offset:5984\n"
"v_pk_fma_f32 v[72:73], v[70:71], v[60:61], v[66:67] op_sel:[1,1,0] op_sel_hi:[0,1,1] neg_lo:[1,0,0]\n"
"ds_write_addtid_b32 v74 offset:6256\n"
"v_cvt_pk_bf16_f32 v75, v70, -v71\n"
"v_cvt_pk_bf16_f32 v76, v72, -v73\n"
"v_pk_mul_f32 v[66:67], v[72:73], v[56:57] op_sel_hi:[1,0]\n"
"v_pk_mul_f32 v[68:69], v[72:73], v[60:61] op_sel_hi:[1,0]\n"
"ds_write_addtid_b32 v75 offset:4896\n"
"v_pk_fma_f32 v[62:63], v[72:73], v[56:57], v[66:67] op_sel:[1,1,0] op_sel_hi:[0,1,1] neg_lo:[1,0,0]\n"
"ds_write_addtid_b32 v76 offset:6528\n"
"v_pk_fma_f32 v[70:71], v[72:73], v[60:61], v[68:69] op_sel:[1,1,0] op_sel_hi:[0,1,1] neg_lo:[1,0,0]\n"
"v_cvt_pk_bf16_f32 v77, v62, -v63\n"
"v_cvt_pk_bf16_f32 v74, v70, -v71\n"
"v_pk_mul_f32 v[66:67], v[70:71], v[56:57] op_sel_hi:[1,0]\n"
"v_pk_mul_f32 v[68:69], v[70:71], v[60:61] op_sel_hi:[1,0]\n"
"ds_write_addtid_b32 v77 offset:7616\n"
"v_pk_fma_f32 v[62:63], v[70:71], v[56:57], v[66:67] op_sel:[1,1,0] op_sel_hi:[0,1,1] neg_lo:[1,0,0]\n"
"ds_write_addtid_b32 v74 offset:6800\n"
"v_pk_fma_f32 v[72:73], v[70:71], v[60:61], v[68:69] op_sel:[1,1,0] op_sel_hi:[0,1,1] neg_lo:[1,0,0]\n"
"v_cvt_pk_bf16_f32 v75, v62, -v63\n"
"v_cvt_pk_bf16_f32 v76, v72, -v73\n"
"v_pk_mul_f32 v[66:67], v[72:73], v[56:57] op_sel_hi:[1,0]\n"
"v_pk_mul_f32 v[68:69], v[72:73], v[60:61] op_sel_hi:[1,0]\n"
"ds_write_addtid_b32 v75 offset:7888\n"
"v_pk_fma_f32 v[62:63], v[72:73], v[56:57], v[66:67] op_sel:[1,1,0] op_sel_hi:[0,1,1] neg_lo:[1,0,0]\n"
"ds_write_addtid_b32 v76 offset:7072\n"
"v_pk_fma_f32 v[70:71], v[72:73], v[60:61], v[68:69] op_sel:[1,1,0] op_sel_hi:[0,1,1] neg_lo:[1,0,0]\n"
"v_cvt_pk_bf16_f32 v77, v62, -v63\n"
"v_cvt_pk_bf16_f32 v74, v70, -v71\n"
"v_pk_mul_f32 v[66:67], v[70:71], v[56:57] op_sel_hi:[1,0]\n"
"ds_write_addtid_b32 v77 offset:8160\n"
"v_pk_fma_f32 v[62:63], v[70:71], v[56:57], v[66:67] op_sel:[1,1,0] op_sel_hi:[0,1,1] neg_lo:[1,0,0]\n"
"ds_write_addtid_b32 v74 offset:7344\n"
"v_cvt_pk_bf16_f32 v75, v62, -v63\n"
"ds_write_addtid_b32 v75 offset:8432\n"
"ds_read_b128 v[112:115], v211 offset:0\n"
"ds_read_b128 v[116:119], v211 offset:32\n"
"ds_read_b128 v[120:123], v211 offset:64\n"
"ds_read_b128 v[124:127], v211 offset:96\n"
"ds_read_b128 v[128:131], v211 offset:128\n"
"ds_read_b128 v[132:135], v211 offset:160\n"
"ds_read_b128 v[136:139], v211 offset:192\n"
"ds_read_b128 v[140:143], v211 offset:224\n"
"s_waitcnt lgkmcnt(7)\n"
"v_mfma_f32_32x32x16_bf16 v[144:159], v[112:115], v[80:83], 0\n"
"v_mul_f32 v30, v47, v30\n"
"v_mul_f32 v31, v47, v31\n"
"v_mul_f32 v32, v47, v32\n"
"v_mul_f32 v33, v47, v33\n"
"v_mul_f32 v50, 0x3fb8aa3b, v30\n"
"s_waitcnt lgkmcnt(6)\n"
"v_mfma_f32_32x32x16_bf16 v[144:159], v[116:119], v[84:87], v[144:159]\n"
"v_mul_f32 v51, 0x3e22f983, v31\n"
"v_mul_f32 v56, 0x41000000, v50\n"
"v_mul_f32 v57, 0x41000000, v51\n"
"v_mul_f32 v58, 0x41800000, v50\n"
"v_mul_f32 v59, 0x41800000, v51\n"
"s_waitcnt lgkmcnt(5)\n"
"v_mfma_f32_32x32x16_bf16 v[144:159], v[120:123], v[88:91], v[144:159]\n"
"v_mul_f32 v60, 0x43800000, v50\n"
"v_mul_f32 v61, 0x43800000, v51\n"
"v_fract_f32 v57, v57\n"
"v_fract_f32 v59, v59\n"
"v_fract_f32 v61, v61\n"
"s_waitcnt lgkmcnt(4)\n"
"v_mfma_f32_32x32x16_bf16 v[144:159], v[124:127], v[92:95], v[144:159]\n"
"v_fract_f32 v51, v51\n"
"v_exp_f32 v74, v56\n"
"v_cos_f32 v56, v57\n"
"v_sin_f32 v57, v57\n"
"v_exp_f32 v75, v58\n"
"s_waitcnt lgkmcnt(3)\n"
"v_mfma_f32_32x32x16_bf16 v[144:159], v[128:131], v[96:99], v[144:159]\n"
"v_cos_f32 v58, v59\n"
"v_sin_f32 v59, v59\n"
"v_exp_f32 v76, v60\n"
"v_cos_f32 v60, v61\n"
"v_sin_f32 v61, v61\n"
"s_waitcnt lgkmcnt(2)\n"
"v_mfma_f32_32x32x16_bf16 v[144:159], v[132:135], v[100:103], v[144:159]\n"
"v_exp_f32 v77, v50\n"
"v_cos_f32 v50, v51\n"
"v_sin_f32 v51, v51\n"
"v_mul_f32 v56, v56, v74\n"
"v_mul_f32 v57, v57, v74\n"
"s_waitcnt lgkmcnt(1)\n"
"v_mfma_f32_32x32x16_bf16 v[144:159], v[136:139], v[104:107], v[144:159]\n"
"v_mul_f32 v58, v58, v75\n"
"v_mul_f32 v59, v59, v75\n"
"v_mul_f32 v60, v60, v76\n"
"v_mul_f32 v61, v61, v76\n"
"v_mul_f32 v50, v50, v77\n"
"s_waitcnt lgkmcnt(0)\n"
"v_mfma_f32_32x32x16_bf16 v[144:159], v[140:143], v[108:111], v[144:159]\n"
"v_mul_f32 v51, v51, v77\n"
"v_mov_b32 v77, 0x3f80\n"
"v_pk_mul_f32 v[66:67], v[34:35], v[32:33] op_sel_hi:[1,0]\n"
"v_pk_mul_f32 v[68:69], v[56:57], v[58:59] op_sel_hi:[1,0]\n"
"v_pk_fma_f32 v[48:49], v[34:35], v[32:33], v[66:67] op_sel:[1,1,0] op_sel_hi:[0,1,1] neg_lo:[1,0,0]\n"
"v_pk_fma_f32 v[64:65], v[56:57], v[58:59], v[68:69] op_sel:[1,1,0] op_sel_hi:[0,1,1] neg_lo:[1,0,0]\n"
"v_cvt_pk_bf16_f32 v74, v56, v57\n"
"ds_write_addtid_b32 v77 offset:0\n"
"ds_write_addtid_b32 v74 offset:272\n"
"v_cvt_pk_bf16_f32 v75, v58, v59\n"
"v_cvt_pk_bf16_f32 v76, v64, v65\n"
"v_pk_mul_f32 v[66:67], v[58:59], v[58:59] op_sel_hi:[1,0]\n"
"v_pk_mul_f32 v[68:69], v[64:65], v[58:59] op_sel_hi:[1,0]\n"
"ds_write_addtid_b32 v75 offset:544\n"
"v_pk_fma_f32 v[62:63], v[58:59], v[58:59], v[66:67] op_sel:[1,1,0] op_sel_hi:[0,1,1] neg_lo:[1,0,0]\n"
"ds_write_addtid_b32 v76 offset:816\n"
"v_pk_fma_f32 v[64:65], v[64:65], v[58:59], v[68:69] op_sel:[1,1,0] op_sel_hi:[0,1,1] neg_lo:[1,0,0]\n"
"v_cvt_pk_bf16_f32 v77, v62, v63\n"
"v_cvt_pk_bf16_f32 v74, v64, v65\n"
"v_pk_mul_f32 v[66:67], v[62:63], v[58:59] op_sel_hi:[1,0]\n"
"v_pk_mul_f32 v[68:69], v[64:65], v[58:59] op_sel_hi:[1,0]\n"
"ds_write_addtid_b32 v77 offset:1088\n"
"v_pk_fma_f32 v[62:63], v[62:63], v[58:59], v[66:67] op_sel:[1,1,0] op_sel_hi:[0,1,1] neg_lo:[1,0,0]\n"
"ds_write_addtid_b32 v74 offset:1360\n"
"v_pk_fma_f32 v[64:65], v[64:65], v[58:59], v[68:69] op_sel:[1,1,0] op_sel_hi:[0,1,1] neg_lo:[1,0,0]\n"
"v_cvt_pk_bf16_f32 v75, v62, v63\n"
"v_cvt_pk_bf16_f32 v76, v64, v65\n"
"v_pk_mul_f32 v[66:67], v[62:63], v[58:59] op_sel_hi:[1,0]\n"
"v_pk_mul_f32 v[68:69], v[64:65], v[58:59] op_sel_hi:[1,0]\n"
"ds_write_addtid_b32 v75 offset:1632\n"
"v_pk_fma_f32 v[62:63], v[62:63], v[58:59], v[66:67] op_sel:[1,1,0] op_sel_hi:[0,1,1] neg_lo:[1,0,0]\n"
"ds_write_addtid_b32 v76 offset:1904\n"
"v_pk_fma_f32 v[64:65], v[64:65], v[58:59], v[68:69] op_sel:[1,1,0] op_sel_hi:[0,1,1] neg_lo:[1,0,0]\n"
"v_cvt_pk_bf16_f32 v77, v62, v63\n"
"v_cvt_pk_bf16_f32 v74, v64, v65\n"
"v_pk_mul_f32 v[66:67], v[62:63], v[58:59] op_sel_hi:[1,0]\n"
"v_pk_mul_f32 v[68:69], v[64:65], v[58:59] op_sel_hi:[1,0]\n"
"ds_write_addtid_b32 v77 offset:2176\n"
"v_pk_fma_f32 v[62:63], v[62:63], v[58:59], v[66:67] op_sel:[1,1,0] op_sel_hi:[0,1,1] neg_lo:[1,0,0]\n"
"ds_write_addtid_b32 v74 offset:2448\n"
"v_pk_fma_f32 v[64:65], v[64:65], v[58:59], v[68:69] op_sel:[1,1,0] op_sel_hi:[0,1,1] neg_lo:[1,0,0]\n"
"v_cvt_pk_bf16_f32 v75, v62, v63\n"
"v_cvt_pk_bf16_f32 v76, v64, v65\n"
"v_pk_mul_f32 v[66:67], v[62:63], v[58:59] op_sel_hi:[1,0]\n"
"v_pk_mul_f32 v[68:69], v[64:65], v[58:59] op_sel_hi:[1,0]\n"
"ds_write_addtid_b32 v75 offset:2720\n"
"v_pk_fma_f32 v[62:63], v[62:63], v[58:59], v[66:67] op_sel:[1,1,0] op_sel_hi:[0,1,1] neg_lo:[1,0,0]\n"
"ds_write_addtid_b32 v76 offset:2992\n"
"v_pk_fma_f32 v[64:65], v[64:65], v[58:59], v[68:69] op_sel:[1,1,0] op_sel_hi:[0,1,1] neg_lo:[1,0,0]\n"
"v_cvt_pk_bf16_f32 v77, v62, v63\n"
"v_cvt_pk_bf16_f32 v74, v64, v65\n"
"v_pk_mul_f32 v[66:67], v[62:63], v[58:59] op_sel_hi:[1,0]\n"
"v_pk_mul_f32 v[68:69], v[64:65], v[58:59] op_sel_hi:[1,0]\n"
"ds_write_addtid_b32 v77 offset:3264\n"
"v_pk_fma_f32 v[62:63], v[62:63], v[58:59], v[66:67] op_sel:[1,1,0] op_sel_hi:[0,1,1] neg_lo:[1,0,0]\n"
"ds_write_addtid_b32 v74 offset:3536\n"
"v_pk_fma_f32 v[64:65], v[64:65], v[58:59], v[68:69] op_sel:[1,1,0] op_sel_hi:[0,1,1] neg_lo:[1,0,0]\n"
"v_cvt_pk_bf16_f32 v75, v62, v63\n"
"v_cvt_pk_bf16_f32 v76, v64, v65\n"
"v_pk_mul_f32 v[66:67], v[62:63], v[58:59] op_sel_hi:[1,0]\n"
"v_pk_mul_f32 v[68:69], v[64:65], v[58:59] op_sel_hi:[1,0]\n"
"ds_write_addtid_b32 v75 offset:3808\n"
"v_pk_fma_f32 v[62:63], v[62:63], v[58:59], v[66:67] op_sel:[1,1,0] op_sel_hi:[0,1,1] neg_lo:[1,0,0]\n"
"ds_write_addtid_b32 v76 offset:4080\n"
"v_pk_fma_f32 v[64:65], v[64:65], v[58:59], v[68:69] op_sel:[1,1,0] op_sel_hi:[0,1,1] neg_lo:[1,0,0]\n"
"v_cvt_pk_bf16_f32 v77, v62, v63\n"
"v_cvt_pk_bf16_f32 v74, v64, v65\n"
"v_pk_mul_f32 v[66:67], v[62:63], v[58:59] op_sel_hi:[1,0]\n"
"v_pk_mul_f32 v[68:69], v[64:65], v[58:59] op_sel_hi:[1,0]\n"
"ds_write_addtid_b32 v77 offset:4352\n"
"v_pk_fma_f32 v[62:63], v[62:63], v[58:59], v[66:67] op_sel:[1,1,0] op_sel_hi:[0,1,1] neg_lo:[1,0,0]\n"
"ds_write_addtid_b32 v74 offset:4624\n"
"v_pk_fma_f32 v[64:65], v[64:65], v[58:59], v[68:69] op_sel:[1,1,0] op_sel_hi:[0,1,1] neg_lo:[1,0,0]\n"
"v_cvt_pk_bf16_f32 v75, v62, v63\n"
"v_cvt_pk_bf16_f32 v76, v64, v65\n"
"v_pk_mul_f32 v[66:67], v[62:63], v[58:59] op_sel_hi:[1,0]\n"
"v_pk_mul_f32 v[68:69], v[64:65], v[58:59] op_sel_hi:[1,0]\n"
"ds_write_addtid_b32 v75 offset:4896\n"
"v_pk_fma_f32 v[62:63], v[62:63], v[58:59], v[66:67] op_sel:[1,1,0] op_sel_hi:[0,1,1] neg_lo:[1,0,0]\n"
"ds_write_addtid_b32 v76 offset:5168\n"
"v_pk_fma_f32 v[64:65], v[64:65], v[58:59], v[68:69] op_sel:[1,1,0] op_sel_hi:[0,1,1] neg_lo:[1,0,0]\n"
"v_cvt_pk_bf16_f32 v77, v62, v63\n"
"v_cvt_pk_bf16_f32 v74, v64, v65\n"
"v_pk_mul_f32 v[66:67], v[62:63], v[58:59] op_sel_hi:[1,0]\n"
"v_pk_mul_f32 v[68:69], v[64:65], v[58:59] op_sel_hi:[1,0]\n"
"ds_write_addtid_b32 v77 offset:5440\n"
"v_pk_fma_f32 v[62:63], v[62:63], v[58:59], v[66:67] op_sel:[1,1,0] op_sel_hi:[0,1,1] neg_lo:[1,0,0]\n"
"ds_write_addtid_b32 v74 offset:5712\n"
"v_pk_fma_f32 v[64:65], v[64:65], v[58:59], v[68:69] op_sel:[1,1,0] op_sel_hi:[0,1,1] neg_lo:[1,0,0]\n"
"buffer_load_dwordx4 v[176:179], v208, s[48:51], s30 offen offset:0 sc1\n"
"v_cvt_pk_bf16_f32 v75, v62, v63\n"
"v_cvt_pk_bf16_f32 v76, v64, v65\n"
"v_pk_mul_f32 v[66:67], v[62:63], v[58:59] op_sel_hi:[1,0]\n"
"v_pk_mul_f32 v[68:69], v[64:65], v[58:59] op_sel_hi:[1,0]\n"
"ds_write_addtid_b32 v75 offset:5984\n"
"v_pk_fma_f32 v[62:63], v[62:63], v[58:59], v[66:67] op_sel:[1,1,0] op_sel_hi:[0,1,1] neg_lo:[1,0,0]\n"
"ds_write_addtid_b32 v76 offset:6256\n"
"v_pk_fma_f32 v[64:65], v[64:65], v[58:59], v[68:69] op_sel:[1,1,0] op_sel_hi:[0,1,1] neg_lo:[1,0,0]\n"
"v_cvt_pk_bf16_f32 v77, v62, v63\n"
"v_cvt_pk_bf16_f32 v74, v64, v65\n"
"v_pk_mul_f32 v[66:67], v[62:63], v[58:59] op_sel_hi:[1,0]\n"
"v_pk_mul_f32 v[68:69], v[64:65], v[58:59] op_sel_hi:[1,0]\n"
"ds_write_addtid_b32 v77 offset:6528\n"
"v_pk_fma_f32 v[62:63], v[62:63], v[58:59], v[66:67] op_sel:[1,1,0] op_sel_hi:[0,1,1] neg_lo:[1,0,0]\n"
"ds_write_addtid_b32 v74 offset:6800\n"
"v_pk_fma_f32 v[64:65], v[64:65], v[58:59], v[68:69] op_sel:[1,1,0] op_sel_hi:[0,1,1] neg_lo:[1,0,0]\n"
"buffer_load_dwordx4 v[192:195], v209, s[48:51], s34 offen offset:0 sc1\n"
"v_cvt_pk_bf16_f32 v75, v62, v63\n"
"v_cvt_pk_bf16_f32 v76, v64, v65\n"
"v_pk_mul_f32 v[66:67], v[62:63], v[58:59] op_sel_hi:[1,0]\n"
"v_pk_mul_f32 v[68:69], v[64:65], v[58:59] op_sel_hi:[1,0]\n"
"ds_write_addtid_b32 v75 offset:7072\n"
"v_pk_fma_f32 v[62:63], v[62:63], v[58:59], v[66:67] op_sel:[1,1,0] op_sel_hi:[0,1,1] neg_lo:[1,0,0]\n"
"ds_write_addtid_b32 v76 offset:7344\n"
"v_pk_fma_f32 v[64:65], v[64:65], v[58:59], v[68:69] op_sel:[1,1,0] op_sel_hi:[0,1,1] neg_lo:[1,0,0]\n"
"v_cvt_pk_bf16_f32 v77, v62, v63\n"
"v_cvt_pk_bf16_f32 v74, v64, v65\n"
"v_pk_mul_f32 v[66:67], v[62:63], v[58:59] op_sel_hi:[1,0]\n"
"v_pk_mul_f32 v[68:69], v[64:65], v[58:59] op_sel_hi:[1,0]\n"
"ds_write_addtid_b32 v77 offset:7616\n"
"v_pk_fma_f32 v[62:63], v[62:63], v[58:59], v[66:67] op_sel:[1,1,0] op_sel_hi:[0,1,1] neg_lo:[1,0,0]\n"
"ds_write_addtid_b32 v74 offset:7888\n"
"v_pk_fma_f32 v[64:65], v[64:65], v[58:59], v[68:69] op_sel:[1,1,0] op_sel_hi:[0,1,1] neg_lo:[1,0,0]\n"
"buffer_load_dwordx4 v[180:183], v208, s[48:51], s30 offen offset:16 sc1\n"
"v_cvt_pk_bf16_f32 v75, v62, v63\n"
"v_cvt_pk_bf16_f32 v76, v64, v65\n"
"v_pk_mul_f32 v[66:67], v[50:51], v[50:51] op_sel_hi:[1,0]\n"
"v_pk_mul_f32 v[68:69], v[48:49], v[60:61] op_sel_hi:[1,0]\n"
"ds_write_addtid_b32 v75 offset:8160\n"
"v_pk_fma_f32 v[52:53], v[50:51], v[50:51], v[66:67] op_sel:[1,1,0] op_sel_hi:[0,1,1] neg_lo:[1,0,0]\n"
"ds_write_addtid_b32 v76 offset:8432\n"
"v_pk_fma_f32 v[70:71], v[48:49], v[60:61], v[68:69] op_sel:[1,1,0] op_sel_hi:[0,1,1] neg_lo:[1,0,0]\n"
"ds_read_b128 v[80:83], v210 offset:0\n"
"ds_read_b128 v[84:87], v210 offset:32\n"
"ds_read_b128 v[88:91], v210 offset:64\n"
"ds_read_b128 v[92:95], v210 offset:96\n"
"ds_read_b128 v[96:99], v210 offset:128\n"
"ds_read_b128 v[100:103], v210 offset:160\n"
"ds_read_b128 v[104:107], v210 offset:192\n"
"ds_read_b128 v[108:111], v210 offset:224\n"
"v_pk_mul_f32 v[66:67], v[52:53], v[52:53] op_sel_hi:[1,0]\n"
"v_pk_mul_f32 v[68:69], v[48:49], v[50:51] op_sel_hi:[1,0]\n"
"v_pk_fma_f32 v[54:55], v[52:53], v[52:53], v[66:67] op_sel:[1,1,0] op_sel_hi:[0,1,1] neg_lo:[1,0,0]\n"
"v_pk_fma_f32 v[62:63], v[48:49], v[50:51], v[68:69] op_sel:[1,1,0] op_sel_hi:[0,1,1] neg_lo:[1,0,0]\n"
"buffer_load_dwordx4 v[196:199], v209, s[48:51], s35 offen offset:0 sc1\n"
"v_cvt_pk_bf16_f32 v77, v48, -v49\n"
"v_cvt_pk_bf16_f32 v74, v62, -v63\n"
"v_pk_mul_f32 v[66:67], v[62:63], v[50:51] op_sel_hi:[1,0]\n"
"v_pk_mul_f32 v[68:69], v[70:71], v[52:53] op_sel_hi:[1,0]\n"
"ds_write_addtid_b32 v77 offset:0\n"
"v_pk_fma_f32 v[62:63], v[62:63], v[50:51], v[66:67] op_sel:[1,1,0] op_sel_hi:[0,1,1] neg_lo:[1,0,0]\n"
"ds_write_addtid_b32 v74 offset:272\n"
"v_pk_fma_f32 v[64:65], v[70:71], v[52:53], v[68:69] op_sel:[1,1,0] op_sel_hi:[0,1,1] neg_lo:[1,0,0]\n"
"v_cvt_pk_bf16_f32 v75, v62, -v63\n"
"v_cvt_pk_bf16_f32 v76, v64, -v65\n"
"v_pk_mul_f32 v[66:67], v[62:63], v[50:51] op_sel_hi:[1,0]\n"
"v_pk_mul_f32 v[68:69], v[70:71], v[54:55] op_sel_hi:[1,0]\n"
"ds_write_addtid_b32 v75 offset:544\n"
"v_pk_fma_f32 v[62:63], v[62:63], v[50:51], v[66:67] op_sel:[1,1,0] op_sel_hi:[0,1,1] neg_lo:[1,0,0]\n"
"ds_write_addtid_b32 v76 offset:2448\n"
"v_pk_fma_f32 v[64:65], v[70:71], v[54:55], v[68:69] op_sel:[1,1,0] op_sel_hi:[0,1,1] neg_lo:[1,0,0]\n"
"buffer_load_dwordx4 v[184:187], v208, s[48:51], s31 offen offset:0 sc1\n"
"v_cvt_pk_bf16_f32 v77, v62, -v63\n"
"v_cvt_pk_bf16_f32 v74, v64, -v65\n"
"v_pk_mul_f32 v[66:67], v[62:63], v[50:51] op_sel_hi:[1,0]\n"
"v_pk_mul_f32 v[68:69], v[64:65], v[52:53] op_sel_hi:[1,0]\n"
"ds_write_addtid_b32 v77 offset:816\n"
"v_pk_fma_f32 v[62:63], v[62:63], v[50:51], v[66:67] op_sel:[1,1,0] op_sel_hi:[0,1,1] neg_lo:[1,0,0]\n"
"ds_write_addtid_b32 v74 offset:2720\n"
"ds_write_addtid_b32 v74 offset:3264\n"
"v_pk_fma_f32 v[64:65], v[64:65], v[52:53], v[68:69] op_sel:[1,1,0] op_sel_hi:[0,1,1] neg_lo:[1,0,0]\n"
"v_cvt_pk_bf16_f32 v75, v62, -v63\n"
"v_cvt_pk_bf16_f32 v76, v64, -v65\n"
"v_pk_mul_f32 v[66:67], v[62:63], v[50:51] op_sel_hi:[1,0]\n"
"v_pk_mul_f32 v[68:69], v[70:71], v[56:57] op_sel_hi:[1,0]\n"
"ds_write_addtid_b32 v75 offset:1088\n"
"v_pk_fma_f32 v[62:63], v[62:63], v[50:51], v[66:67] op_sel:[1,1,0] op_sel_hi:[0,1,1] neg_lo:[1,0,0]\n"
"ds_write_addtid_b32 v76 offset:3536\n"
"v_pk_fma_f32 v[64:65], v[70:71], v[56:57], v[68:69] op_sel:[1,1,0] op_sel_hi:[0,1,1] neg_lo:[1,0,0]\n"
"buffer_load_dwordx4 v[200:203], v209, s[48:51], s34 offen offset:1024 sc1\n"
"v_cvt_pk_bf16_f32 v77, v62, -v63\n"
"v_cvt_pk_bf16_f32 v74, v64, -v65\n"
"v_pk_mul_f32 v[66:67], v[62:63], v[50:51] op_sel_hi:[1,0]\n"
"v_pk_mul_f32 v[68:69], v[70:71], v[60:61] op_sel_hi:[1,0]\n"
"ds_write_addtid_b32 v77 offset:1360\n"
"v_pk_fma_f32 v[62:63], v[62:63], v[50:51], v[66:67] op_sel:[1,1,0] op_sel_hi:[0,1,1] neg_lo:[1,0,0]\n"
"ds_write_addtid_b32 v74 offset:3808\n"
"v_pk_fma_f32 v[72:73], v[70:71], v[60:61], v[68:69] op_sel:[1,1,0] op_sel_hi:[0,1,1] neg_lo:[1,0,0]\n"
"v_cvt_pk_bf16_f32 v75, v62, -v63\n"
"v_cvt_pk_bf16_f32 v76, v70, -v71\n"
"v_pk_mul_f32 v[66:67], v[62:63], v[50:51] op_sel_hi:[1,0]\n"
"v_pk_mul_f32 v[68:69], v[72:73], v[54:55] op_sel_hi:[1,0]\n"
"ds_write_addtid_b32 v75 offset:1632\n"
"v_pk_fma_f32 v[62:63], v[62:63], v[50:51], v[66:67] op_sel:[1,1,0] op_sel_hi:[0,1,1] neg_lo:[1,0,0]\n"
"ds_write_addtid_b32 v76 offset:2176\n"
"v_pk_fma_f32 v[64:65], v[72:73], v[54:55], v[68:69] op_sel:[1,1,0] op_sel_hi:[0,1,1] neg_lo:[1,0,0]\n"
"buffer_load_dwordx4 v[188:191], v208, s[48:51], s31 offen offset:16 sc1\n"
"v_cvt_pk_bf16_f32 v77, v62, -v63\n"
"v_cvt_pk_bf16_f32 v74, v64, -v65\n"
"v_pk_mul_f32 v[66:67], v[72:73], v[56:57] op_sel_hi:[1,0]\n"
"v_pk_mul_f32 v[68:69], v[72:73], v[60:61] op_sel_hi:[1,0]\n"
"ds_write_addtid_b32 v77 offset:1904\n"
"v_pk_fma_f32 v[62:63], v[72:73], v[56:57], v[66:67] op_sel:[1,1,0] op_sel_hi:[0,1,1] neg_lo:[1,0,0]\n"
"ds_write_addtid_b32 v74 offset:4624\n"
"ds_write_addtid_b32 v74 offset:5440\n"
"v_pk_fma_f32 v[70:71], v[72:73], v[60:61], v[68:69] op_sel:[1,1,0] op_sel_hi:[0,1,1] neg_lo:[1,0,0]\n"
"v_cvt_pk_bf16_f32 v75, v62, -v63\n"
"v_cvt_pk_bf16_f32 v76, v72, -v73\n"
"v_pk_mul_f32 v[66:67], v[70:71], v[54:55] op_sel_hi:[1,0]\n"
"v_pk_mul_f32 v[68:69], v[70:71], v[56:57] op_sel_hi:[1,0]\n"
"ds_write_addtid_b32 v75 offset:5712\n"
"v_pk_fma_f32 v[62:63], v[70:71], v[54:55], v[66:67] op_sel:[1,1,0] op_sel_hi:[0,1,1] neg_lo:[1,0,0]\n"
"ds_write_addtid_b32 v76 offset:4352\n"
"v_pk_fma_f32 v[64:65], v[70:71], v[56:57], v[68:69] op_sel:[1,1,0] op_sel_hi:[0,1,1] neg_lo:[1,0,0]\n"
"buffer_load_dwordx4 v[204:207], v209, s[48:51], s35 offen offset:1024 sc1\n"
"v_cvt_pk_bf16_f32 v77, v62, -v63\n"
"v_cvt_pk_bf16_f32 v74, v64, -v65\n"
"v_pk_mul_f32 v[66:67], v[70:71], v[60:61] op_sel_hi:[1,0]\n"
"ds_write_addtid_b32 v77 offset:5168\n"
"ds_write_addtid_b32 v77 offset:5984\n"
"v_pk_fma_f32 v[72:73], v[70:71], v[60:61], v[66:67] op_sel:[1,1,0] op_sel_hi:[0,1,1] neg_lo:[1,0,0]\n"
"ds_write_addtid_b32 v74 offset:6256\n"
"v_cvt_pk_bf16_f32 v75, v70, -v71\n"
"v_cvt_pk_bf16_f32 v76, v72, -v73\n"
"v_pk_mul_f32 v[66:67], v[72:73], v[56:57] op_sel_hi:[1,0]\n"
"v_pk_mul_f32 v[68:69], v[72:73], v[60:61] op_sel_hi:[1,0]\n"
"ds_write_addtid_b32 v75 offset:4896\n"
"v_pk_fma_f32 v[62:63], v[72:73], v[56:57], v[66:67] op_sel:[1,1,0] op_sel_hi:[0,1,1] neg_lo:[1,0,0]\n"
"ds_write_addtid_b32 v76 offset:6528\n"
"v_pk_fma_f32 v[70:71], v[72:73], v[60:61], v[68:69] op_sel:[1,1,0] op_sel_hi:[0,1,1] neg_lo:[1,0,0]\n"
"v_cvt_pk_bf16_f32 v77, v62, -v63\n"
"v_cvt_pk_bf16_f32 v74, v70, -v71\n"
"v_pk_mul_f32 v[66:67], v[70:71], v[56:57] op_sel_hi:[1,0]\n"
"v_pk_mul_f32 v[68:69], v[70:71], v[60:61] op_sel_hi:[1,0]\n"
"ds_write_addtid_b32 v77 offset:7616\n"
"v_pk_fma_f32 v[62:63], v[70:71], v[56:57], v[66:67] op_sel:[1,1,0] op_sel_hi:[0,1,1] neg_lo:[1,0,0]\n"
"ds_write_addtid_b32 v74 offset:6800\n"
"v_pk_fma_f32 v[72:73], v[70:71], v[60:61], v[68:69] op_sel:[1,1,0] op_sel_hi:[0,1,1] neg_lo:[1,0,0]\n"
"v_cvt_pk_bf16_f32 v75, v62, -v63\n"
"v_cvt_pk_bf16_f32 v76, v72, -v73\n"
"v_pk_mul_f32 v[66:67], v[72:73], v[56:57] op_sel_hi:[1,0]\n"
"v_pk_mul_f32 v[68:69], v[72:73], v[60:61] op_sel_hi:[1,0]\n"
"ds_write_addtid_b32 v75 offset:7888\n"
"v_pk_fma_f32 v[62:63], v[72:73], v[56:57], v[66:67] op_sel:[1,1,0] op_sel_hi:[0,1,1] neg_lo:[1,0,0]\n"
"ds_write_addtid_b32 v76 offset:7072\n"
"v_pk_fma_f32 v[70:71], v[72:73], v[60:61], v[68:69] op_sel:[1,1,0] op_sel_hi:[0,1,1] neg_lo:[1,0,0]\n"
"v_cvt_pk_bf16_f32 v77, v62, -v63\n"
"v_cvt_pk_bf16_f32 v74, v70, -v71\n"
"v_pk_mul_f32 v[66:67], v[70:71], v[56:57] op_sel_hi:[1,0]\n"
"ds_write_addtid_b32 v77 offset:8160\n"
"v_pk_fma_f32 v[62:63], v[70:71], v[56:57], v[66:67] op_sel:[1,1,0] op_sel_hi:[0,1,1] neg_lo:[1,0,0]\n"
"ds_write_addtid_b32 v74 offset:7344\n"
"v_cvt_pk_bf16_f32 v75, v62, -v63\n"
"ds_write_addtid_b32 v75 offset:8432\n"
"ds_read_b128 v[112:115], v211 offset:0\n"
"ds_read_b128 v[116:119], v211 offset:32\n"
"ds_read_b128 v[120:123], v211 offset:64\n"
"ds_read_b128 v[124:127], v211 offset:96\n"
"ds_read_b128 v[128:131], v211 offset:128\n"
"ds_read_b128 v[132:135], v211 offset:160\n"
"ds_read_b128 v[136:139], v211 offset:192\n"
"ds_read_b128 v[140:143], v211 offset:224\n"
"s_mov_b64 exec, 0xff\n"
"buffer_load_dwordx4 v[220:223], v216, s[48:51], 0 offen sc1\n"
"s_mov_b64 exec, -1\n"
"v_add_u32 v74, s42, v232\n"
"s_waitcnt lgkmcnt(7)\n"
"s_nop 1\n"
"v_mfma_f32_32x32x16_bf16 v[160:175], v[112:115], v[80:83], 0\n"
"ds_write_b128 v74, v[148:151] offset:0\n"
"ds_write_b128 v74, v[152:155] offset:1024\n"
"ds_write_b128 v74, v[156:159] offset:2048\n"
"s_waitcnt lgkmcnt(9)\n"
"v_mfma_f32_32x32x16_bf16 v[160:175], v[116:119], v[84:87], v[160:175]\n"
"v_mov_b32 v4, 0\n"
"v_mov_b32 v5, 0\n"
"v_mov_b32 v6, 0\n"
"s_waitcnt lgkmcnt(8)\n"
"v_mfma_f32_32x32x16_bf16 v[160:175], v[120:123], v[88:91], v[160:175]\n"
"v_mov_b32 v7, 0\n"
"s_and_b32 s0, s43, 1\n"
"s_mul_i32 s0, s0, 4608\n"
"s_waitcnt lgkmcnt(7)\n"
"v_mfma_f32_32x32x16_bf16 v[160:175], v[124:127], v[92:95], v[160:175]\n"
"v_min_u32 v8, 29, v1\n"
"v_lshlrev_b32 v8, 4, v8\n"
"v_add_u32 v8, s0, v8\n"
"s_waitcnt lgkmcnt(6)\n"
"v_mfma_f32_32x32x16_bf16 v[160:175], v[128:131], v[96:99], v[160:175]\n"
"v_bfe_u32 v9, v1, 1, 3\n"
"v_mul_u32_u24 v9, 4160, v9\n"
"v_and_b32 v10, 1, v1\n"
"s_waitcnt lgkmcnt(5)\n"
"v_mfma_f32_32x32x16_bf16 v[160:175], v[132:135], v[100:103], v[160:175]\n"
"v_lshl_add_u32 v9, v10, 4, v9\n"
"v_lshlrev_b32 v12, 16, v37\n"
"v_and_b32 v13, 0xffff0000, v37\n"
"s_waitcnt lgkmcnt(4)\n"
"v_mfma_f32_32x32x16_bf16 v[160:175], v[136:139], v[104:107], v[160:175]\n"
"v_min_u32 v2, 27, v1\n"
"v_lshlrev_b32 v2, 7, v2\n"
"s_waitcnt lgkmcnt(3)\n"
"v_mfma_f32_32x32x16_bf16 v[160:175], v[140:143], v[108:111], v[160:175]\n"
"s_waitcnt vmcnt(1)\n"
"v_xor_b32 v66, s39, v177\n"
"v_xor_b32 v67, s39, v179\n"
"v_bitop3_b32 v66, v66, v193, s39 bitop3:0xf6\n"
"v_bitop3_b32 v67, v67, v195, s39 bitop3:0xf6\n"
"v_bitop3_b32 v66, v66, v181, s39 bitop3:0xf6\n"
"v_bitop3_b32 v67, v67, v183, s39 bitop3:0xf6\n"
"v_bitop3_b32 v66, v66, v197, s39 bitop3:0xf6\n"
"v_bitop3_b32 v67, v67, v199, s39 bitop3:0xf6\n"
"v_bitop3_b32 v66, v66, v185, s39 bitop3:0xf6\n"
"v_bitop3_b32 v67, v67, v187, s39 bitop3:0xf6\n"
"v_bitop3_b32 v66, v66, v201, s39 bitop3:0xf6\n"
"v_bitop3_b32 v67, v67, v203, s39 bitop3:0xf6\n"
"v_bitop3_b32 v66, v66, v189, s39 bitop3:0xf6\n"
"v_bitop3_b32 v67, v67, v191, s39 bitop3:0xf6\n"
"v_bitop3_b32 v66, v66, v205, s39 bitop3:0xf6\n"
"v_bitop3_b32 v67, v67, v207, s39 bitop3:0xf6\n"
"v_or_b32 v66, v66, v67\n"
"v_cmp_ne_u32 vcc, 0, v66\n"
"ds_write_b128 v74, v[164:167] offset:3072\n"
"ds_write_b128 v74, v[168:171] offset:4096\n"
"ds_write_b128 v74, v[172:175] offset:5120\n"
"s_nop 1\n"
"s_cbranch_vccnz Lsw_%=\n"
"Lswr_%=:\n"
"s_waitcnt lgkmcnt(0)\n"
"s_barrier\n"
"ds_read_b128 v[80:83], v212 offset:0\n"
"ds_read_b128 v[84:87], v213 offset:0\n"
"ds_read_b128 v[88:91], v214 offset:0\n"
"ds_read_b128 v[92:95], v212 offset:3072\n"
"ds_read_b128 v[96:99], v213 offset:3072\n"
"ds_read_b128 v[100:103], v214 offset:3072\n"
"ds_write_b128 v8, v[4:7]\n"
"s_or_b32 s0, s38, s43\n"
"s_cmp_lg_u32 s0, 0\n"
"s_cbranch_scc1 Lep_%=\n"
"v_mov_b32 v73, s39\n"
"global_store_dword v3, v73, s[24:25] sc1\n"
"Lep_%=:\n"
"v_and_b32 v25, 0xffff0000, v192\n"
"v_lshlrev_b32 v24, 16, v192\n"
"v_rcp_f32 v25, v25\n"
"v_and_b32 v27, 0xffff0000, v194\n"
"v_lshlrev_b32 v26, 16, v194\n"
"v_rcp_f32 v27, v27\n"
"v_and_b32 v29, 0xffff0000, v196\n"
"v_lshlrev_b32 v28, 16, v196\n"
"v_rcp_f32 v29, v29\n"
"v_and_b32 v31, 0xffff0000, v198\n"
"v_lshlrev_b32 v30, 16, v198\n"
"v_rcp_f32 v31, v31\n"
"v_and_b32 v177, 0xffff0000, v176\n"
"v_lshlrev_b32 v176, 16, v176\n"
"v_and_b32 v179, 0xffff0000, v178\n"
"v_lshlrev_b32 v178, 16, v178\n"
"v_and_b32 v181, 0xffff0000, v180\n"
"v_lshlrev_b32 v180, 16, v180\n"
"v_and_b32 v183, 0xffff0000, v182\n"
"v_lshlrev_b32 v182, 16, v182\n"
"v_pk_add_f32 v[192:193], v[176:177], v[24:25] op_sel_hi:[1,0] neg_lo:[0,1] neg_hi:[0,1]\n"
"v_pk_add_f32 v[194:195], v[178:179], v[26:27] op_sel_hi:[1,0] neg_lo:[0,1] neg_hi:[0,1]\n"
"v_pk_add_f32 v[196:197], v[180:181], v[28:29] op_sel_hi:[1,0] neg_lo:[0,1] neg_hi:[0,1]\n"
"v_pk_add_f32 v[198:199], v[182:183], v[30:31] op_sel_hi:[1,0] neg_lo:[0,1] neg_hi:[0,1]\n"
"v_pk_mul_f32 v[192:193], v[192:193], v[24:25] op_sel:[0,1] op_sel_hi:[1,1]\n"
"v_pk_mul_f32 v[194:195], v[194:195], v[26:27] op_sel:[0,1] op_sel_hi:[1,1]\n"
"v_pk_mul_f32 v[196:197], v[196:197], v[28:29] op_sel:[0,1] op_sel_hi:[1,1]\n"
"v_pk_mul_f32 v[198:199], v[198:199], v[30:31] op_sel:[0,1] op_sel_hi:[1,1]\n"
"s_waitcnt lgkmcnt(6)\n"
"v_pk_add_f32 v[144:145], v[144:145], v[80:81]\n"
"s_waitcnt lgkmcnt(4)\n"
"v_pk_add_f32 v[84:85], v[84:85], v[88:89]\n"
"v_pk_add_f32 v[146:147], v[146:147], v[82:83]\n"
"v_pk_add_f32 v[86:87], v[86:87], v[90:91]\n"
"v_pk_add_f32 v[144:145], v[144:145], v[84:85]\n"
"v_pk_add_f32 v[146:147], v[146:147], v[86:87]\n"
"s_waitcnt lgkmcnt(3)\n"
"v_pk_add_f32 v[160:161], v[160:161], v[92:93]\n"
"s_waitcnt lgkmcnt(1)\n"
"v_pk_add_f32 v[96:97], v[96:97], v[100:101]\n"
"v_pk_add_f32 v[162:163], v[162:163], v[94:95]\n"
"v_pk_add_f32 v[98:99], v[98:99], v[102:103]\n"
"v_pk_add_f32 v[160:161], v[160:161], v[96:97]\n"
"v_pk_add_f32 v[162:163], v[162:163], v[98:99]\n"
"s_barrier\n"
"s_cmp_eq_u32 s43, 0\n"
"s_cbranch_scc1 Lk0_%=\n"
"s_cmp_eq_u32 s43, 1\n"
"s_cbranch_scc1 Lk1_%=\n"
"s_cmp_eq_u32 s43, 2\n"
"s_cbranch_scc1 Lk2_%=\n"
"v_cvt_pk_bf16_f32 v80, v144, v144\n"
"v_cvt_pk_bf16_f32 v81, v144, v144\n"
"ds_write_b64 v215, v[80:81] offset:2048\n"
"v_cvt_pk_bf16_f32 v82, v145, v145\n"
"v_cvt_pk_bf16_f32 v83, v145, v145\n"
"ds_write_b64 v215, v[82:83] offset:2560\n"
"v_cvt_pk_bf16_f32 v84, v146, v146\n"
"v_cvt_pk_bf16_f32 v85, v146, v146\n"
"ds_write_b64 v215, v[84:85] offset:3072\n"
"v_cvt_pk_bf16_f32 v86, v147, v147\n"
"v_cvt_pk_bf16_f32 v87, v147, v147\n"
"ds_write_b64 v215, v[86:87] offset:3584\n"
"v_cvt_pk_bf16_f32 v88, v160, v160\n"
"v_cvt_pk_bf16_f32 v89, v160, v160\n"
"ds_write_b64 v215, v[88:89] offset:6656\n"
"v_cvt_pk_bf16_f32 v90, v161, v161\n"
"v_cvt_pk_bf16_f32 v91, v161, v161\n"
"ds_write_b64 v215, v[90:91] offset:7168\n"
"v_cvt_pk_bf16_f32 v92, v162, v162\n"
"v_cvt_pk_bf16_f32 v93, v162, v162\n"
"ds_write_b64 v215, v[92:93] offset:7680\n"
"v_cvt_pk_bf16_f32 v94, v163, v163\n"
"v_cvt_pk_bf16_f32 v95, v163, v163\n"
"ds_write_b64 v215, v[94:95] offset:8192\n"
"s_mov_b32 exec_lo, 0x55555555\n"
"s_mov_b32 exec_hi, 0\n"
"ds_write_b16 v215, v80 offset:2056\n"
"ds_write_b16 v215, v82 offset:2568\n"
"ds_write_b16 v215, v84 offset:3080\n"
"ds_write_b16 v215, v86 offset:3592\n"
"ds_write_b16 v215, v88 offset:6664\n"
"ds_write_b16 v215, v90 offset:7176\n"
"ds_write_b16 v215, v92 offset:7688\n"
"ds_write_b16 v215, v94 offset:8200\n"
"s_mov_b64 exec, -1\n"
"Lkd_%=:\n"
"v_and_b32 v25, 0xffff0000, v200\n"
"v_lshlrev_b32 v24, 16, v200\n"
"v_rcp_f32 v25, v25\n"
"v_and_b32 v27, 0xffff0000, v202\n"
"v_lshlrev_b32 v26, 16, v202\n"
"v_rcp_f32 v27, v27\n"
"v_and_b32 v29, 0xffff0000, v204\n"
"v_lshlrev_b32 v28, 16, v204\n"
"v_rcp_f32 v29, v29\n"
"v_and_b32 v31, 0xffff0000, v206\n"
"v_lshlrev_b32 v30, 16, v206\n"
"v_rcp_f32 v31, v31\n"
"v_and_b32 v185, 0xffff0000, v184\n"
"v_lshlrev_b32 v184, 16, v184\n"
"v_and_b32 v187, 0xffff0000, v186\n"
"v_lshlrev_b32 v186, 16, v186\n"
"v_and_b32 v189, 0xffff0000, v188\n"
"v_lshlrev_b32 v188, 16, v188\n"
"v_and_b32 v191, 0xffff0000, v190\n"
"v_lshlrev_b32 v190, 16, v190\n"
"v_pk_add_f32 v[200:201], v[184:185], v[24:25] op_sel_hi:[1,0] neg_lo:[0,1] neg_hi:[0,1]\n"
"v_pk_add_f32 v[202:203], v[186:187], v[26:27] op_sel_hi:[1,0] neg_lo:[0,1] neg_hi:[0,1]\n"
"v_pk_add_f32 v[204:205], v[188:189], v[28:29] op_sel_hi:[1,0] neg_lo:[0,1] neg_hi:[0,1]\n"
"v_pk_add_f32 v[206:207], v[190:191], v[30:31] op_sel_hi:[1,0] neg_lo:[0,1] neg_hi:[0,1]\n"
"v_pk_mul_f32 v[200:201], v[200:201], v[24:25] op_sel:[0,1] op_sel_hi:[1,1]\n"
"v_pk_mul_f32 v[202:203], v[202:203], v[26:27] op_sel:[0,1] op_sel_hi:[1,1]\n"
"v_pk_mul_f32 v[204:205], v[204:205], v[28:29] op_sel:[0,1] op_sel_hi:[1,1]\n"
"v_pk_mul_f32 v[206:207], v[206:207], v[30:31] op_sel:[0,1] op_sel_hi:[1,1]\n"
"s_waitcnt vmcnt(0)\n"
"v_xor_b32 v10, s39, v221\n"
"v_bitop3_b32 v10, v10, v223, s39 bitop3:0xf6\n"
"v_cmp_ne_u32 vcc, 0, v10\n"
"ds_write_b128 v9, v[4:7] offset:13312\n"
"s_nop 3\n"
"s_cbranch_vccnz Lsm_%=\n"
"Lsmr_%=:\n"
"v_readlane_b32 s8, v222, 0\n"
"v_readlane_b32 s9, v220, 1\n"
"v_readlane_b32 s10, v222, 1\n"
"v_readlane_b32 s12, v222, 2\n"
"v_readlane_b32 s13, v220, 3\n"
"v_readlane_b32 s14, v222, 3\n"
"v_add_f32_dpp v220, v220, v220 quad_perm:[1,0,3,2] row_mask:0xf bank_mask:0xf bound_ctrl:1\n"
"v_add_f32_dpp v222, v222, v222 quad_perm:[1,0,3,2] row_mask:0xf bank_mask:0xf bound_ctrl:1\n"
"v_mov_b32 v14, 0xffff0000\n"
"v_cvt_pk_bf16_f32 v222, v220, v222\n"
"v_lshlrev_b32 v220, 16, v222\n"
"v_and_b32 v222, v14, v222\n"
"v_add_f32 v10, 1.0, v220\n"
"v_add_f32 v11, 1.0, v222\n"
"v_readlane_b32 s4, v220, 4\n"
"v_cvt_pk_bf16_f32 v10, v10, v11\n"
"v_readlane_b32 s5, v222, 4\n"
"v_readlane_b32 s7, v10, 6\n"
"s_nop 3\n"
"s_lshl_b32 s6, s7, 16\n"
"s_and_b32 s7, s7, 0xffff0000\n"
"v_mov_b32 v16, s4\n"
"v_mov_b32 v17, s5\n"
"s_and_b32 s0, s12, 0xffff0000\n"
"v_rcp_f32 v27, s0\n"
"v_lshlrev_b32_e64 v26, 16, s12\n"
"v_and_b32 v139, s8, v14\n"
"v_lshlrev_b32_e64 v138, 16, s8\n"
"s_and_b32 s0, s13, 0xffff0000\n"
"v_rcp_f32 v29, s0\n"
"v_lshlrev_b32_e64 v28, 16, s13\n"
"v_and_b32 v141, s9, v14\n"
"v_lshlrev_b32_e64 v140, 16, s9\n"
"s_and_b32 s0, s14, 0xffff0000\n"
"v_rcp_f32 v31, s0\n"
"v_lshlrev_b32_e64 v30, 16, s14\n"
"v_and_b32 v143, s10, v14\n"
"v_lshlrev_b32_e64 v142, 16, s10\n"
"s_add_u32 m0, s83, 0\n"
"s_nop 0\n"
"buffer_load_dwordx4 v236, s[56:59], s65 offen offset:0 lds\n"
"buffer_load_dwordx4 v237, s[56:59], s65 offen offset:1024 lds\n"
"s_add_u32 s98, s82, 0\n"
"buffer_store_dwordx4 v[224:227], v232, s[48:51], s98 offen offset:0 sc1\n"
"v_pk_add_f32 v[146:147], v[138:139], v[26:27] op_sel_hi:[1,0] neg_lo:[0,1] neg_hi:[0,1]\n"
"v_pk_add_f32 v[148:149], v[140:141], v[28:29] op_sel_hi:[1,0] neg_lo:[0,1] neg_hi:[0,1]\n"
"v_pk_add_f32 v[150:151], v[142:143], v[30:31] op_sel_hi:[1,0] neg_lo:[0,1] neg_hi:[0,1]\n"
"v_pk_mul_f32 v[146:147], v[146:147], v[26:27] op_sel:[0,1] op_sel_hi:[1,1]\n"
"v_pk_mul_f32 v[148:149], v[148:149], v[28:29] op_sel:[0,1] op_sel_hi:[1,1]\n"
"v_pk_mul_f32 v[150:151], v[150:151], v[30:31] op_sel:[0,1] op_sel_hi:[1,1]\n"
"v_pk_fma_f32 v[146:147], v[146:147], s[6:7], v[16:17]\n"
"v_pk_fma_f32 v[148:149], v[148:149], s[6:7], v[16:17]\n"
"v_pk_fma_f32 v[150:151], v[150:151], s[6:7], v[16:17]\n"
"s_add_u32 m0, s83, 2048\n"
"s_nop 0\n"
"buffer_load_dwordx4 v236, s[56:59], s66 offen offset:0 lds\n"
"buffer_load_dwordx4 v237, s[56:59], s66 offen offset:1024 lds\n"
"s_cmp_lg_u32 s43, 0\n"
"s_cselect_b32 s0, -1, 0\n"
"v_cvt_pk_bf16_f32 v64, v150, v148\n"
"v_cvt_pk_bf16_f32 v65, v146, v146\n"
"v_cvt_pk_bf16_f32 v66, v151, v149\n"
"v_cvt_pk_bf16_f32 v67, v147, v147\n"
"v_and_b32 v64, s0, v64\n"
"v_and_b32 v65, s0, v65\n"
"v_and_b32 v66, s0, v66\n"
"v_and_b32 v67, s0, v67\n"
"v_pk_fma_f32 v[192:193], v[192:193], s[6:7], v[16:17]\n"
"v_pk_fma_f32 v[194:195], v[194:195], s[6:7], v[16:17]\n"
"v_pk_fma_f32 v[196:197], v[196:197], s[6:7], v[16:17]\n"
"v_pk_fma_f32 v[198:199], v[198:199], s[6:7], v[16:17]\n"
"s_add_u32 m0, s83, 4096\n"
"s_nop 0\n"
"buffer_load_dwordx4 v236, s[56:59], s67 offen offset:0 lds\n"
"buffer_load_dwordx4 v237, s[56:59], s67 offen offset:1024 lds\n"
"s_add_u32 s98, s82, 0\n"
"buffer_store_dwordx4 v[224:227], v232, s[48:51], s98 offen offset:1024 sc1\n"
"v_cvt_pk_bf16_f32 v48, v198, v196\n"
"v_cvt_pk_bf16_f32 v49, v194, v192\n"
"v_cvt_pk_bf16_f32 v56, v199, v197\n"
"v_cvt_pk_bf16_f32 v57, v195, v193\n"
"v_mov_b32 v50, v49\n"
"v_alignbit_b32 v52, v49, v48, 16\n"
"v_mov_b32 v58, v57\n"
"v_alignbit_b32 v60, v57, v56, 16\n"
"v_mov_b32_dpp v64, v48 wave_shr:1 row_mask:0xf bank_mask:0xf\n"
"v_mov_b32_dpp v65, v49 wave_shr:1 row_mask:0xf bank_mask:0xf\n"
"v_mov_b32_dpp v66, v56 wave_shr:1 row_mask:0xf bank_mask:0xf\n"
"v_mov_b32_dpp v67, v57 wave_shr:1 row_mask:0xf bank_mask:0xf\n"
"ds_write_b64 v243, v[48:49] offset:512\n"
"v_mov_b32 v51, v64\n"
"v_alignbit_b32 v53, v64, v49, 16\n"
"v_alignbit_b32 v54, v64, v49, 16\n"
"ds_write_b64 v243, v[50:51] offset:8832\n"
"s_add_u32 m0, s83, 6144\n"
"s_nop 0\n"
"buffer_load_dwordx4 v236, s[56:59], s68 offen offset:0 lds\n"
"buffer_load_dwordx4 v237, s[56:59], s68 offen offset:1024 lds\n"
"v_alignbit_b32 v55, v65, v64, 16\n"
"ds_write_b64 v243, v[52:53] offset:4672\n"
"ds_write_b64 v243, v[54:55] offset:12992\n"
"ds_write_b64 v243, v[56:57] offset:17152\n"
"v_mov_b32 v59, v66\n"
"v_alignbit_b32 v61, v66, v57, 16\n"
"v_alignbit_b32 v62, v66, v57, 16\n"
"ds_write_b64 v243, v[58:59] offset:25472\n"
"s_add_u32 m0, s83, 8192\n"
"s_nop 0\n"
"buffer_load_dwordx4 v236, s[56:59], s69 offen offset:0 lds\n"
"buffer_load_dwordx4 v237, s[56:59], s69 offen offset:1024 lds\n"
"v_alignbit_b32 v63, v67, v66, 16\n"
"ds_write_b64 v243, v[60:61] offset:21312\n"
"ds_write_b64 v243, v[62:63] offset:29632\n"
"s_add_u32 m0, s83, 10240\n"
"s_nop 0\n"
"buffer_load_dwordx4 v236, s[56:59], s70 offen offset:0 lds\n"
"buffer_load_dwordx4 v237, s[56:59], s70 offen offset:1024 lds\n"
"v_mov_b32_dpp v64, v48 wave_ror:1 row_mask:0xf bank_mask:0xf\n"
"v_mov_b32_dpp v65, v49 wave_ror:1 row_mask:0xf bank_mask:0xf\n"
"v_mov_b32_dpp v66, v56 wave_ror:1 row_mask:0xf bank_mask:0xf\n"
"v_mov_b32_dpp v67, v57 wave_ror:1 row_mask:0xf bank_mask:0xf\n"
"v_pk_fma_f32 v[200:201], v[200:201], s[6:7], v[16:17]\n"
"v_pk_fma_f32 v[202:203], v[202:203], s[6:7], v[16:17]\n"
"v_pk_fma_f32 v[204:205], v[204:205], s[6:7], v[16:17]\n"
"v_pk_fma_f32 v[206:207], v[206:207], s[6:7], v[16:17]\n"
"s_add_u32 m0, s83, 12288\n"
"s_nop 0\n"
"buffer_load_dwordx4 v236, s[56:59], s71 offen offset:0 lds\n"
"buffer_load_dwordx4 v237, s[56:59], s71 offen offset:1024 lds\n"
"s_add_u32 s98, s82, 4096\n"
"buffer_store_dwordx4 v[224:227], v232, s[48:51], s98 offen offset:0 sc1\n"
"v_cvt_pk_bf16_f32 v48, v206, v204\n"
"v_cvt_pk_bf16_f32 v49, v202, v200\n"
"v_cvt_pk_bf16_f32 v56, v207, v205\n"
"v_cvt_pk_bf16_f32 v57, v203, v201\n"
"v_mov_b32 v50, v49\n"
"v_alignbit_b32 v52, v49, v48, 16\n"
"v_mov_b32 v58, v57\n"
"v_alignbit_b32 v60, v57, v56, 16\n"
"v_mov_b32_dpp v64, v48 wave_shr:1 row_mask:0xf bank_mask:0xf\n"
"v_mov_b32_dpp v65, v49 wave_shr:1 row_mask:0xf bank_mask:0xf\n"
"v_mov_b32_dpp v66, v56 wave_shr:1 row_mask:0xf bank_mask:0xf\n"
"v_mov_b32_dpp v67, v57 wave_shr:1 row_mask:0xf bank_mask:0xf\n"
"ds_write_b64 v243, v[48:49] offset:0\n"
"v_mov_b32 v51, v64\n"
"v_alignbit_b32 v53, v64, v49, 16\n"
"v_alignbit_b32 v54, v64, v49, 16\n"
"ds_write_b64 v243, v[50:51] offset:8320\n"
"s_add_u32 m0, s83, 14336\n"
"s_nop 0\n"
"buffer_load_dwordx4 v238, s[56:59], s65 offen offset:0 lds\n"
"buffer_load_dwordx4 v239, s[56:59], s65 offen offset:1024 lds\n"
"v_alignbit_b32 v55, v65, v64, 16\n"
"ds_write_b64 v243, v[52:53] offset:4160\n"
"ds_write_b64 v243, v[54:55] offset:12480\n"
"ds_write_b64 v243, v[56:57] offset:16640\n"
"v_mov_b32 v59, v66\n"
"v_alignbit_b32 v61, v66, v57, 16\n"
"v_alignbit_b32 v62, v66, v57, 16\n"
"ds_write_b64 v243, v[58:59] offset:24960\n"
"s_add_u32 m0, s83, 16384\n"
"s_nop 0\n"
"buffer_load_dwordx4 v238, s[56:59], s66 offen offset:0 lds\n"
"buffer_load_dwordx4 v239, s[56:59], s66 offen offset:1024 lds\n"
"v_alignbit_b32 v63, v67, v66, 16\n"
"ds_write_b64 v243, v[60:61] offset:20800\n"
"ds_write_b64 v243, v[62:63] offset:29120\n"
"s_waitcnt lgkmcnt(0)\n"
"s_barrier\n"
"ds_read_b64 v[48:49], v234 offset:3776\n"
"ds_read_b64 v[50:51], v234 offset:3784\n"
"ds_read_b128 v[52:55], v235 offset:192\n"
"ds_read_b64 v[56:57], v234 offset:3264\n"
"ds_read_b64 v[58:59], v234 offset:3272\n"
"ds_read_b128 v[60:63], v235 offset:704\n"
"ds_read_b64 v[64:65], v234 offset:2752\n"
"ds_read_b64 v[66:67], v234 offset:2760\n"
"ds_read_b128 v[68:71], v235 offset:1216\n"
"s_waitcnt lgkmcnt(6)\n"
"v_mfma_f32_16x16x32_bf16 v[16:19], v[48:51], v[52:55], 0\n"
"ds_read_b64 v[72:73], v234 offset:2240\n"
"ds_read_b64 v[74:75], v234 offset:2248\n"
"s_waitcnt lgkmcnt(6)\n"
"v_mfma_f32_16x16x32_bf16 v[20:23], v[56:59], v[52:55], 0\n"
"ds_read_b128 v[76:79], v235 offset:1728\n"
"ds_read_b64 v[80:81], v234 offset:1728\n"
"s_waitcnt lgkmcnt(7)\n"
"v_mfma_f32_16x16x32_bf16 v[20:23], v[48:51], v[60:63], v[20:23]\n"
"ds_read_b64 v[82:83], v234 offset:1736\n"
"ds_read_b128 v[84:87], v235 offset:2240\n"
"v_mfma_f32_16x16x32_bf16 v[24:27], v[56:59], v[60:63], 0\n"
"ds_read_b64 v[88:89], v234 offset:1216\n"
"ds_read_b64 v[90:91], v234 offset:1224\n"
"s_add_u32 m0, s83, 18432\n"
"s_waitcnt lgkmcnt(9)\n"
"v_mfma_f32_16x16x32_bf16 v[24:27], v[64:67], v[52:55], v[24:27]\n"
"ds_read_b128 v[92:95], v235 offset:2752\n"
"ds_read_b64 v[96:97], v234 offset:704\n"
"buffer_load_dwordx4 v238, s[56:59], s67 offen offset:0 lds\n"
"s_waitcnt lgkmcnt(10)\n"
"v_mfma_f32_16x16x32_bf16 v[24:27], v[48:51], v[68:71], v[24:27]\n"
"ds_read_b64 v[98:99], v234 offset:712\n"
"ds_read_b128 v[100:103], v235 offset:3264\n"
"v_mfma_f32_16x16x32_bf16 v[28:31], v[64:67], v[60:63], 0\n"
"ds_read_b64 v[104:105], v234 offset:192\n"
"ds_read_b64 v[106:107], v234 offset:200\n"
"v_mfma_f32_16x16x32_bf16 v[28:31], v[56:59], v[68:71], v[28:31]\n"
"ds_read_b128 v[108:111], v235 offset:3776\n"
"buffer_load_dwordx4 v239, s[56:59], s67 offen offset:1024 lds\n"
"s_add_u32 s98, s82, 4096\n"
"buffer_store_dwordx4 v[224:227], v232, s[48:51], s98 offen offset:1024 sc1\n"
"v_mfma_f32_16x16x32_bf16 v[32:35], v[64:67], v[68:71], 0\n"
"s_waitcnt lgkmcnt(13)\n"
"v_mfma_f32_16x16x32_bf16 v[28:31], v[72:75], v[52:55], v[28:31]\n"
"s_waitcnt lgkmcnt(12)\n"
"v_mfma_f32_16x16x32_bf16 v[28:31], v[48:51], v[76:79], v[28:31]\n"
"ds_read_b64 v[112:113], v234 offset:3712\n"
"s_add_u32 m0, s83, 20480\n"
"v_mfma_f32_16x16x32_bf16 v[32:35], v[72:75], v[60:63], v[32:35]\n"
"ds_read_b64 v[114:115], v234 offset:3720\n"
"buffer_load_dwordx4 v238, s[56:59], s68 offen offset:0 lds\n"
"v_mfma_f32_16x16x32_bf16 v[32:35], v[56:59], v[76:79], v[32:35]\n"
"ds_read_b128 v[116:119], v235 offset:128\n"
"ds_read_b64 v[120:121], v234 offset:3200\n"
"v_mfma_f32_16x16x32_bf16 v[36:39], v[72:75], v[68:71], 0\n"
"ds_read_b64 v[122:123], v234 offset:3208\n"
"buffer_load_dwordx4 v239, s[56:59], s68 offen offset:1024 lds\n"
"v_mfma_f32_16x16x32_bf16 v[36:39], v[64:67], v[76:79], v[36:39]\n"
"ds_read_b128 v[124:127], v235 offset:640\n"
"ds_read_b64 v[128:129], v234 offset:2688\n"
"v_mfma_f32_16x16x32_bf16 v[40:43], v[72:75], v[76:79], 0\n"
"ds_read_b64 v[130:131], v234 offset:2696\n"
"ds_read_b128 v[132:135], v235 offset:1152\n"
"s_waitcnt lgkmcnt(14)\n"
"v_mfma_f32_16x16x32_bf16 v[32:35], v[80:83], v[52:55], v[32:35]\n"
"s_add_u32 m0, s83, 22528\n"
"ds_read_b64 v[136:137], v234 offset:2176\n"
"v_mfma_f32_16x16x32_bf16 v[32:35], v[48:51], v[84:87], v[32:35]\n"
"buffer_load_dwordx4 v238, s[56:59], s69 offen offset:0 lds\n"
"ds_read_b64 v[138:139], v234 offset:2184\n"
"v_mfma_f32_16x16x32_bf16 v[36:39], v[80:83], v[60:63], v[36:39]\n"
"ds_read_b128 v[140:143], v235 offset:1664\n"
"ds_read_b64 v[144:145], v234 offset:1664\n"
"v_mfma_f32_16x16x32_bf16 v[36:39], v[56:59], v[84:87], v[36:39]\n"
"ds_read_b64 v[146:147], v234 offset:1672\n"
"ds_read_b128 v[148:151], v235 offset:2176\n"
"v_mfma_f32_16x16x32_bf16 v[40:43], v[80:83], v[68:71], v[40:43]\n"
"buffer_load_dwordx4 v239, s[56:59], s69 offen offset:1024 lds\n"
"ds_read_b64 v[152:153], v234 offset:1152\n"
"v_mfma_f32_16x16x32_bf16 v[40:43], v[64:67], v[84:87], v[40:43]\n"
"ds_read_b64 v[154:155], v234 offset:1160\n"
"ds_read_b128 v[156:159], v235 offset:2688\n"
"v_mfma_f32_16x16x32_bf16 v[44:47], v[80:83], v[76:79], 0\n"
"ds_read_b64 v[160:161], v234 offset:640\n"
"ds_read_b64 v[162:163], v234 offset:648\n"
"v_mfma_f32_16x16x32_bf16 v[44:47], v[72:75], v[84:87], v[44:47]\n"
"s_add_u32 m0, s83, 24576\n"
"ds_read_b128 v[164:167], v235 offset:3200\n"
"v_mfma_f32_16x16x32_bf16 v[36:39], v[88:91], v[52:55], v[36:39]\n"
"buffer_load_dwordx4 v238, s[56:59], s70 offen offset:0 lds\n"
"v_mfma_f32_16x16x32_bf16 v[36:39], v[48:51], v[92:95], v[36:39]\n"
"s_add_u32 s98, s82, 8192\n"
"buffer_store_dwordx4 v[224:227], v232, s[48:51], s98 offen offset:0 sc1\n"
"v_mfma_f32_16x16x32_bf16 v[40:43], v[88:91], v[60:63], v[40:43]\n"
"ds_read_b64 v[168:169], v234 offset:128\n"
"ds_read_b64 v[170:171], v234 offset:136\n"
"v_mfma_f32_16x16x32_bf16 v[40:43], v[56:59], v[92:95], v[40:43]\n"
"buffer_load_dwordx4 v239, s[56:59], s70 offen offset:1024 lds\n"
"ds_read_b128 v[172:175], v235 offset:3712\n"
"v_mfma_f32_16x16x32_bf16 v[44:47], v[88:91], v[68:71], v[44:47]\n"
"s_waitcnt vmcnt(29)\n"
"ds_read_b64_tr_b16 a[0:1], v240 offset:0\n"
"v_mfma_f32_16x16x32_bf16 v[44:47], v[64:67], v[92:95], v[44:47]\n"
"ds_read_b64_tr_b16 a[2:3], v240 offset:1024\n"
"ds_read_b64_tr_b16 a[4:5], v241 offset:0\n"
"s_waitcnt lgkmcnt(14)\n"
"v_mfma_f32_16x16x32_bf16 v[40:43], v[96:99], v[52:55], v[40:43]\n"
"s_add_u32 m0, s83, 26624\n"
"v_mfma_f32_16x16x32_bf16 v[40:43], v[48:51], v[100:103], v[40:43]\n"
"buffer_load_dwordx4 v238, s[56:59], s71 offen offset:0 lds\n"
"v_mfma_f32_16x16x32_bf16 v[44:47], v[96:99], v[60:63], v[44:47]\n"
"s_mov_b64 exec, 1\n"
"buffer_load_dwordx4 v[220:223], v3, s[48:51], s41 offen sc1\n"
"s_mov_b64 exec, -1\n"
"s_nop 3\n"
"v_mfma_f32_16x16x32_bf16 v[44:47], v[56:59], v[100:103], v[44:47]\n"
"ds_read_b64_tr_b16 a[6:7], v241 offset:1024\n"
"v_mfma_f32_16x16x32_bf16 v[44:47], v[104:107], v[52:55], v[44:47]\n"
"buffer_load_dwordx4 v239, s[56:59], s71 offen offset:1024 lds\n"
"v_mfma_f32_16x16x32_bf16 v[44:47], v[48:51], v[108:111], v[44:47]\n"
"v_add_u32 v236, 57344, v236\n"
"v_add_u32 v237, 57344, v237\n"
"v_mfma_f32_16x16x32_bf16 v[16:19], v[112:115], v[116:119], v[16:19]\n"
"v_add_u32 v238, 57344, v238\n"
"v_add_u32 v239, 57344, v239\n"
"v_mfma_f32_16x16x32_bf16 v[20:23], v[120:123], v[116:119], v[20:23]\n"
"ds_read_b64 v[48:49], v234 offset:3648\n"
"ds_read_b64 v[50:51], v234 offset:3656\n"
"v_mfma_f32_16x16x32_bf16 v[20:23], v[112:115], v[124:127], v[20:23]\n"
"s_waitcnt vmcnt(29)\n"
"ds_read_b64_tr_b16 a[8:9], v240 offset:2048\n"
"s_add_u32 m0, s83, 0\n"
"v_mfma_f32_16x16x32_bf16 v[24:27], v[120:123], v[124:127], v[24:27]\n"
"ds_read_b128 v[52:55], v235 offset:64\n"
"ds_read_b64_tr_b16 a[10:11], v240 offset:3072\n"
"s_waitcnt lgkmcnt(5)\n"
"buffer_load_dwordx4 v236, s[56:59], s65 offen offset:0 lds\n"
"v_mfma_f32_16x16x32_bf16 v[24:27], v[128:131], v[116:119], v[24:27]\n"
"ds_read_b64 v[56:57], v234 offset:3136\n"
"ds_read_b64_tr_b16 a[12:13], v241 offset:2048\n"
"v_mfma_f32_16x16x32_bf16 v[24:27], v[112:115], v[132:135], v[24:27]\n"
"ds_read_b64_tr_b16 a[14:15], v241 offset:3072\n"
"v_mfma_f32_16x16x32_bf16 v[28:31], v[128:131], v[124:127], v[28:31]\n"
"ds_read_b64 v[58:59], v234 offset:3144\n"
"buffer_load_dwordx4 v237, s[56:59], s65 offen offset:1024 lds\n"
"v_mfma_f32_16x16x32_bf16 v[28:31], v[120:123], v[132:135], v[28:31]\n"
"ds_read_b128 v[60:63], v235 offset:576\n"
"v_mfma_f32_16x16x32_bf16 v[32:35], v[128:131], v[132:135], v[32:35]\n"
"ds_read_b64 v[64:65], v234 offset:2624\n"
"v_mfma_f32_16x16x32_bf16 v[28:31], v[136:139], v[116:119], v[28:31]\n"
"s_waitcnt vmcnt(29)\n"
"ds_read_b64_tr_b16 a[16:17], v240 offset:4096\n"
"s_add_u32 m0, s83, 2048\n"
"v_mfma_f32_16x16x32_bf16 v[28:31], v[112:115], v[140:143], v[28:31]\n"
"ds_read_b64 v[66:67], v234 offset:2632\n"
"ds_read_b64_tr_b16 a[18:19], v240 offset:5120\n"
"s_waitcnt lgkmcnt(6)\n"
"buffer_load_dwordx4 v236, s[56:59], s66 offen offset:0 lds\n"
"v_mfma_f32_16x16x32_bf16 v[32:35], v[136:139], v[124:127], v[32:35]\n"
"ds_read_b128 v[68:71], v235 offset:1088\n"
"ds_read_b64_tr_b16 a[20:21], v241 offset:4096\n"
"s_add_u32 s98, s82, 8192\n"
"buffer_store_dwordx4 v[224:227], v232, s[48:51], s98 offen offset:1024 sc1\n"
"v_mfma_f32_16x16x32_bf16 v[32:35], v[120:123], v[140:143], v[32:35]\n"
"ds_read_b64 v[72:73], v234 offset:2112\n"
"ds_read_b64_tr_b16 a[22:23], v241 offset:5120\n"
"v_mfma_f32_16x16x32_bf16 v[36:39], v[136:139], v[132:135], v[36:39]\n"
"buffer_load_dwordx4 v237, s[56:59], s66 offen offset:1024 lds\n"
"v_mfma_f32_16x16x32_bf16 v[36:39], v[128:131], v[140:143], v[36:39]\n"
"ds_read_b64 v[74:75], v234 offset:2120\n"
"v_mfma_f32_16x16x32_bf16 v[40:43], v[136:139], v[140:143], v[40:43]\n"
"ds_read_b128 v[76:79], v235 offset:1600\n"
"v_mfma_f32_16x16x32_bf16 v[32:35], v[144:147], v[116:119], v[32:35]\n"
"ds_read_b64 v[80:81], v234 offset:1600\n"
"s_waitcnt vmcnt(29)\n"
"ds_read_b64_tr_b16 a[24:25], v240 offset:6144\n"
"s_add_u32 m0, s83, 4096\n"
"v_mfma_f32_16x16x32_bf16 v[32:35], v[112:115], v[148:151], v[32:35]\n"
"ds_read_b64 v[82:83], v234 offset:1608\n"
"ds_read_b64_tr_b16 a[26:27], v240 offset:7168\n"
"s_waitcnt lgkmcnt(6)\n"
"buffer_load_dwordx4 v236, s[56:59], s67 offen offset:0 lds\n"
"v_mfma_f32_16x16x32_bf16 v[36:39], v[144:147], v[124:127], v[36:39]\n"
"ds_read_b64_tr_b16 a[28:29], v241 offset:6144\n"
"v_mfma_f32_16x16x32_bf16 v[36:39], v[120:123], v[148:151], v[36:39]\n"
"ds_read_b128 v[84:87], v235 offset:2112\n"
"ds_read_b64_tr_b16 a[30:31], v241 offset:7168\n"
"v_mfma_f32_16x16x32_bf16 v[40:43], v[144:147], v[132:135], v[40:43]\n"
"ds_read_b64 v[88:89], v234 offset:1088\n"
"buffer_load_dwordx4 v237, s[56:59], s67 offen offset:1024 lds\n"
"v_mfma_f32_16x16x32_bf16 v[40:43], v[128:131], v[148:151], v[40:43]\n"
"ds_read_b64 v[90:91], v234 offset:1096\n"
"v_mfma_f32_16x16x32_bf16 v[44:47], v[144:147], v[140:143], v[44:47]\n"
"v_mfma_f32_16x16x32_bf16 v[44:47], v[136:139], v[148:151], v[44:47]\n"
"ds_read_b128 v[92:95], v235 offset:2624\n"
"s_waitcnt vmcnt(29)\n"
"ds_read_b64_tr_b16 a[32:33], v240 offset:8192\n"
"s_add_u32 m0, s83, 6144\n"
"v_mfma_f32_16x16x32_bf16 v[36:39], v[152:155], v[116:119], v[36:39]\n"
"ds_read_b64 v[96:97], v234 offset:576\n"
"ds_read_b64_tr_b16 a[34:35], v240 offset:9216\n"
"s_waitcnt lgkmcnt(6)\n"
"buffer_load_dwordx4 v236, s[56:59], s68 offen offset:0 lds\n"
"v_mfma_f32_16x16x32_bf16 v[36:39], v[112:115], v[156:159], v[36:39]\n"
"ds_read_b64 v[98:99], v234 offset:584\n"
"ds_read_b64_tr_b16 a[36:37], v241 offset:8192\n"
"v_mfma_f32_16x16x32_bf16 v[40:43], v[152:155], v[124:127], v[40:43]\n"
"ds_read_b64_tr_b16 a[38:39], v241 offset:9216\n"
"v_mfma_f32_16x16x32_bf16 v[40:43], v[120:123], v[156:159], v[40:43]\n"
"ds_read_b128 v[100:103], v235 offset:3136\n"
"buffer_load_dwordx4 v237, s[56:59], s68 offen offset:1024 lds\n"
"v_mfma_f32_16x16x32_bf16 v[44:47], v[152:155], v[132:135], v[44:47]\n"
"ds_read_b64 v[104:105], v234 offset:64\n"
"v_mfma_f32_16x16x32_bf16 v[44:47], v[128:131], v[156:159], v[44:47]\n"
"ds_read_b64 v[106:107], v234 offset:72\n"
"v_mfma_f32_16x16x32_bf16 v[40:43], v[160:163], v[116:119], v[40:43]\n"
"s_waitcnt vmcnt(29)\n"
"ds_read_b64_tr_b16 a[40:41], v240 offset:10240\n"
"s_add_u32 m0, s83, 8192\n"
"v_mfma_f32_16x16x32_bf16 v[40:43], v[112:115], v[164:167], v[40:43]\n"
"ds_read_b128 v[108:111], v235 offset:3648\n"
"ds_read_b64_tr_b16 a[42:43], v240 offset:11264\n"
"s_waitcnt lgkmcnt(6)\n"
"buffer_load_dwordx4 v236, s[56:59], s69 offen offset:0 lds\n"
"s_add_u32 s98, s82, 12288\n"
"buffer_store_dwordx4 v[224:227], v232, s[48:51], s98 offen offset:0 sc1\n"
"v_mfma_f32_16x16x32_bf16 v[44:47], v[160:163], v[124:127], v[44:47]\n"
"ds_read_b64_tr_b16 a[44:45], v241 offset:10240\n"
"v_mfma_f32_16x16x32_bf16 v[44:47], v[120:123], v[164:167], v[44:47]\n"
"ds_read_b64_tr_b16 a[46:47], v241 offset:11264\n"
"v_mfma_f32_16x16x32_bf16 v[44:47], v[168:171], v[116:119], v[44:47]\n"
"buffer_load_dwordx4 v237, s[56:59], s69 offen offset:1024 lds\n"
"v_mfma_f32_16x16x32_bf16 v[44:47], v[112:115], v[172:175], v[44:47]\n"
"s_waitcnt vmcnt(13)\n"
"v_xor_b32 v10, s39, v221\n"
"v_mfma_f32_16x16x32_bf16 v[16:19], v[48:51], v[52:55], v[16:19]\n"
"v_bitop3_b32 v10, v10, v223, s39 bitop3:0xf6\n"
"v_mfma_f32_16x16x32_bf16 v[20:23], v[56:59], v[52:55], v[20:23]\n"
"ds_read_b64 v[112:113], v234 offset:3584\n"
"ds_read_b64_tr_b16 a[48:49], v240 offset:12288\n"
"s_add_u32 m0, s83, 10240\n"
"v_cmp_ne_u32 vcc, 0, v10\n"
"v_mfma_f32_16x16x32_bf16 v[20:23], v[48:51], v[60:63], v[20:23]\n"
"ds_read_b64 v[114:115], v234 offset:3592\n"
"ds_read_b64_tr_b16 a[50:51], v240 offset:13312\n"
"s_waitcnt lgkmcnt(4)\n"
"buffer_load_dwordx4 v236, s[56:59], s70 offen offset:0 lds\n"
"v_mfma_f32_16x16x32_bf16 v[24:27], v[56:59], v[60:63], v[24:27]\n"
"ds_read_b128 v[116:119], v235 offset:0\n"
"ds_read_b64_tr_b16 a[52:53], v241 offset:12288\n"
"v_mfma_f32_16x16x32_bf16 v[24:27], v[64:67], v[52:55], v[24:27]\n"
"ds_read_b64 v[120:121], v234 offset:3072\n"
"ds_read_b64_tr_b16 a[54:55], v241 offset:13312\n"
"v_mfma_f32_16x16x32_bf16 v[24:27], v[48:51], v[68:71], v[24:27]\n"
"buffer_load_dwordx4 v237, s[56:59], s70 offen offset:1024 lds\n"
"s_cbranch_vccnz Lga_%=\n"
"Lgar_%=:\n"
"v_mfma_f32_16x16x32_bf16 v[28:31], v[64:67], v[60:63], v[28:31]\n"
"ds_read_b64 v[122:123], v234 offset:3080\n"
"v_mfma_f32_16x16x32_bf16 v[28:31], v[56:59], v[68:71], v[28:31]\n"
"ds_read_b128 v[124:127], v235 offset:512\n"
"v_mfma_f32_16x16x32_bf16 v[32:35], v[64:67], v[68:71], v[32:35]\n"
"ds_read_b64 v[128:129], v234 offset:2560\n"
"ds_read_b64_tr_b16 a[56:57], v240 offset:14336\n"
"s_add_u32 m0, s83, 12288\n"
"v_mfma_f32_16x16x32_bf16 v[28:31], v[72:75], v[52:55], v[28:31]\n"
"ds_read_b64_tr_b16 a[58:59], v240 offset:15360\n"
"s_waitcnt lgkmcnt(5)\n"
"buffer_load_dwordx4 v236, s[56:59], s71 offen offset:0 lds\n"
"v_mfma_f32_16x16x32_bf16 v[28:31], v[48:51], v[76:79], v[28:31]\n"
"ds_read_b64 v[130:131], v234 offset:2568\n"
"ds_read_b64_tr_b16 a[60:61], v241 offset:14336\n"
"v_mfma_f32_16x16x32_bf16 v[32:35], v[72:75], v[60:63], v[32:35]\n"
"ds_read_b128 v[132:135], v235 offset:1024\n"
"ds_read_b64_tr_b16 a[62:63], v241 offset:15360\n"
"v_mfma_f32_16x16x32_bf16 v[32:35], v[56:59], v[76:79], v[32:35]\n"
"ds_read_b64 v[136:137], v234 offset:2048\n"
"buffer_load_dwordx4 v237, s[56:59], s71 offen offset:1024 lds\n"
"v_mfma_f32_16x16x32_bf16 v[36:39], v[72:75], v[68:71], v[36:39]\n"
"v_mfma_f32_16x16x32_bf16 v[36:39], v[64:67], v[76:79], v[36:39]\n"
"ds_read_b64 v[138:139], v234 offset:2056\n"
"v_mfma_f32_16x16x32_bf16 v[40:43], v[72:75], v[76:79], v[40:43]\n"
"ds_read_b128 v[140:143], v235 offset:1536\n"
"ds_read_b64_tr_b16 a[64:65], v240 offset:16384\n"
"s_add_u32 m0, s83, 14336\n"
"s_add_u32 s98, s82, 12288\n"
"buffer_store_dwordx4 v[224:227], v232, s[48:51], s98 offen offset:1024 sc1\n"
"v_mfma_f32_16x16x32_bf16 v[32:35], v[80:83], v[52:55], v[32:35]\n"
"ds_read_b64 v[144:145], v234 offset:1536\n"
"ds_read_b64_tr_b16 a[66:67], v240 offset:17408\n"
"s_waitcnt lgkmcnt(6)\n"
"buffer_load_dwordx4 v238, s[56:59], s65 offen offset:0 lds\n"
"v_mfma_f32_16x16x32_bf16 v[32:35], v[48:51], v[84:87], v[32:35]\n"
"ds_read_b64 v[146:147], v234 offset:1544\n"
"ds_read_b64_tr_b16 a[68:69], v241 offset:16384\n"
"v_mfma_f32_16x16x32_bf16 v[36:39], v[80:83], v[60:63], v[36:39]\n"
"ds_read_b64_tr_b16 a[70:71], v241 offset:17408\n"
"v_mfma_f32_16x16x32_bf16 v[36:39], v[56:59], v[84:87], v[36:39]\n"
"ds_read_b128 v[148:151], v235 offset:2048\n"
"buffer_load_dwordx4 v239, s[56:59], s65 offen offset:1024 lds\n"
"v_mfma_f32_16x16x32_bf16 v[40:43], v[80:83], v[68:71], v[40:43]\n"
"ds_read_b64 v[152:153], v234 offset:1024\n"
"v_mfma_f32_16x16x32_bf16 v[40:43], v[64:67], v[84:87], v[40:43]\n"
"ds_read_b64 v[154:155], v234 offset:1032\n"
"v_mfma_f32_16x16x32_bf16 v[44:47], v[80:83], v[76:79], v[44:47]\n"
"ds_read_b64_tr_b16 a[72:73], v240 offset:18432\n"
"s_add_u32 m0, s83, 16384\n"
"v_mfma_f32_16x16x32_bf16 v[44:47], v[72:75], v[84:87], v[44:47]\n"
"ds_read_b128 v[156:159], v235 offset:2560\n"
"ds_read_b64_tr_b16 a[74:75], v240 offset:19456\n"
"s_waitcnt lgkmcnt(6)\n"
"buffer_load_dwordx4 v238, s[56:59], s66 offen offset:0 lds\n"
"v_mfma_f32_16x16x32_bf16 v[36:39], v[88:91], v[52:55], v[36:39]\n"
"ds_read_b64 v[160:161], v234 offset:512\n"
"ds_read_b64_tr_b16 a[76:77], v241 offset:18432\n"
"v_mfma_f32_16x16x32_bf16 v[36:39], v[48:51], v[92:95], v[36:39]\n"
"ds_read_b64 v[162:163], v234 offset:520\n"
"ds_read_b64_tr_b16 a[78:79], v241 offset:19456\n"
"v_mfma_f32_16x16x32_bf16 v[40:43], v[88:91], v[60:63], v[40:43]\n"
"buffer_load_dwordx4 v239, s[56:59], s66 offen offset:1024 lds\n"
"v_mfma_f32_16x16x32_bf16 v[40:43], v[56:59], v[92:95], v[40:43]\n"
"ds_read_b128 v[164:167], v235 offset:3072\n"
"v_mfma_f32_16x16x32_bf16 v[44:47], v[88:91], v[68:71], v[44:47]\n"
"ds_read_b64 v[168:169], v234 offset:0\n"
"v_mfma_f32_16x16x32_bf16 v[44:47], v[64:67], v[92:95], v[44:47]\n"
"ds_read_b64 v[170:171], v234 offset:8\n"
"ds_read_b64_tr_b16 a[80:81], v240 offset:20480\n"
"s_add_u32 m0, s83, 18432\n"
"v_mfma_f32_16x16x32_bf16 v[40:43], v[96:99], v[52:55], v[40:43]\n"
"ds_read_b64_tr_b16 a[82:83], v240 offset:21504\n"
"s_waitcnt lgkmcnt(5)\n"
"buffer_load_dwordx4 v238, s[56:59], s67 offen offset:0 lds\n"
"v_mfma_f32_16x16x32_bf16 v[40:43], v[48:51], v[100:103], v[40:43]\n"
"ds_read_b128 v[172:175], v235 offset:3584\n"
"ds_read_b64_tr_b16 a[84:85], v241 offset:20480\n"
"v_mfma_f32_16x16x32_bf16 v[44:47], v[96:99], v[60:63], v[44:47]\n"
"ds_read_b64_tr_b16 a[86:87], v241 offset:21504\n"
"v_mfma_f32_16x16x32_bf16 v[44:47], v[56:59], v[100:103], v[44:47]\n"
"buffer_load_dwordx4 v239, s[56:59], s67 offen offset:1024 lds\n"
"v_mfma_f32_16x16x32_bf16 v[44:47], v[104:107], v[52:55], v[44:47]\n"
"v_mfma_f32_16x16x32_bf16 v[44:47], v[48:51], v[108:111], v[44:47]\n"
"s_waitcnt lgkmcnt(2)\n"
"v_mfma_f32_16x16x32_bf16 v[44:47], v[112:115], v[172:175], v[44:47]\n"
"ds_read_b64_tr_b16 a[88:89], v240 offset:22528\n"
"s_add_u32 m0, s83, 20480\n"
"v_mfma_f32_16x16x32_bf16 v[44:47], v[120:123], v[164:167], v[44:47]\n"
"ds_read_b64_tr_b16 a[90:91], v240 offset:23552\n"
"s_waitcnt lgkmcnt(2)\n"
"buffer_load_dwordx4 v238, s[56:59], s68 offen offset:0 lds\n"
"v_mfma_f32_16x16x32_bf16 v[44:47], v[128:131], v[156:159], v[44:47]\n"
"ds_read_b64_tr_b16 a[92:93], v241 offset:22528\n"
"v_mfma_f32_16x16x32_bf16 v[44:47], v[136:139], v[148:151], v[44:47]\n"
"ds_read_b64_tr_b16 a[94:95], v241 offset:23552\n"
"buffer_load_dword v15, v2, s[48:51], s3 offen sc1\n"
"v_mfma_f32_16x16x32_bf16 v[44:47], v[144:147], v[140:143], v[44:47]\n"
"buffer_load_dwordx4 v239, s[56:59], s68 offen offset:1024 lds\n"
"v_mfma_f32_16x16x32_bf16 v[44:47], v[152:155], v[132:135], v[44:47]\n"
"s_barrier\n"
"v_mfma_f32_16x16x32_bf16 v[44:47], v[160:163], v[124:127], v[44:47]\n"
"ds_read_b64_tr_b16 a[96:97], v240 offset:24576\n"
"v_mfma_f32_16x16x32_bf16 v[44:47], v[168:171], v[116:119], v[44:47]\n"
"s_add_u32 m0, s83, 22528\n"
"ds_read_b64_tr_b16 a[98:99], v240 offset:25600\n"
"v_mfma_f32_16x16x32_bf16 v[40:43], v[112:115], v[164:167], v[40:43]\n"
"s_waitcnt lgkmcnt(2)\n"
"buffer_load_dwordx4 v238, s[56:59], s69 offen offset:0 lds\n"
"v_mfma_f32_16x16x32_bf16 v[40:43], v[120:123], v[156:159], v[40:43]\n"
"ds_read_b64_tr_b16 a[100:101], v241 offset:24576\n"
"ds_read_b64_tr_b16 a[102:103], v241 offset:25600\n"
"v_mfma_f32_16x16x32_bf16 v[40:43], v[128:131], v[148:151], v[40:43]\n"
"buffer_load_dwordx4 v239, s[56:59], s69 offen offset:1024 lds\n"
"v_mfma_f32_16x16x32_bf16 v[40:43], v[136:139], v[140:143], v[40:43]\n"
"ds_write_b128 v245, v[44:47] offset:7168\n"
"v_mfma_f32_16x16x32_bf16 v[40:43], v[144:147], v[132:135], v[40:43]\n"
"v_mfma_f32_16x16x32_bf16 v[40:43], v[152:155], v[124:127], v[40:43]\n"
"v_mfma_f32_16x16x32_bf16 v[40:43], v[160:163], v[116:119], v[40:43]\n"
"s_waitcnt vmcnt(28)\n"
"ds_read_b64_tr_b16 a[104:105], v240 offset:26624\n"
"s_add_u32 m0, s83, 24576\n"
"v_mfma_f32_16x16x32_bf16 v[36:39], v[112:115], v[156:159], v[36:39]\n"
"ds_read_b64_tr_b16 a[106:107], v240 offset:27648\n"
"s_waitcnt lgkmcnt(3)\n"
"buffer_load_dwordx4 v238, s[56:59], s70 offen offset:0 lds\n"
"v_mfma_f32_16x16x32_bf16 v[36:39], v[120:123], v[148:151], v[36:39]\n"
"ds_read_b64_tr_b16 a[108:109], v241 offset:26624\n"
"v_mfma_f32_16x16x32_bf16 v[36:39], v[128:131], v[140:143], v[36:39]\n"
"ds_read_b64_tr_b16 a[110:111], v241 offset:27648\n"
"buffer_load_dwordx4 v239, s[56:59], s70 offen offset:1024 lds\n"
"v_mfma_f32_16x16x32_bf16 v[36:39], v[136:139], v[132:135], v[36:39]\n"
"ds_write_b128 v245, v[40:43] offset:6144\n"
"v_mfma_f32_16x16x32_bf16 v[36:39], v[144:147], v[124:127], v[36:39]\n"
"v_mfma_f32_16x16x32_bf16 v[36:39], v[152:155], v[116:119], v[36:39]\n"
"v_mfma_f32_16x16x32_bf16 v[32:35], v[112:115], v[148:151], v[32:35]\n"
"s_add_u32 m0, s83, 26624\n"
"v_mfma_f32_16x16x32_bf16 v[32:35], v[120:123], v[140:143], v[32:35]\n"
"s_waitcnt lgkmcnt(1)\n"
"buffer_load_dwordx4 v238, s[56:59], s71 offen offset:0 lds\n"
"v_mfma_f32_16x16x32_bf16 v[32:35], v[128:131], v[132:135], v[32:35]\n"
"s_nop 1\n"
"ds_write_b128 v245, v[36:39] offset:5120\n"
"v_mfma_f32_16x16x32_bf16 v[32:35], v[136:139], v[124:127], v[32:35]\n"
"v_mfma_f32_16x16x32_bf16 v[32:35], v[144:147], v[116:119], v[32:35]\n"
"buffer_load_dwordx4 v239, s[56:59], s71 offen offset:1024 lds\n"
"v_mfma_f32_16x16x32_bf16 v[28:31], v[112:115], v[140:143], v[28:31]\n"
"v_readfirstlane_b32 s44, v220\n"
"v_mfma_f32_16x16x32_bf16 v[28:31], v[120:123], v[132:135], v[28:31]\n"
"v_readfirstlane_b32 s45, v222\n"
"v_mul_f32 v12, v12, v220\n"
"v_mfma_f32_16x16x32_bf16 v[28:31], v[128:131], v[124:127], v[28:31]\n"
"s_nop 0\n"
"ds_write_b128 v245, v[32:35] offset:4096\n"
"v_mfma_f32_16x16x32_bf16 v[28:31], v[136:139], v[116:119], v[28:31]\n"
"v_mfma_f32_16x16x32_bf16 v[24:27], v[112:115], v[132:135], v[24:27]\n"
"v_mfma_f32_16x16x32_bf16 v[24:27], v[120:123], v[124:127], v[24:27]\n"
"v_mul_f32 v13, v13, v222\n"
"v_nop\n"
"v_mfma_f32_16x16x32_bf16 v[24:27], v[128:131], v[116:119], v[24:27]\n"
"v_readfirstlane_b32 s46, v12\n"
"v_readfirstlane_b32 s47, v13\n"
"v_nop\n"
"v_mfma_f32_16x16x32_bf16 v[20:23], v[112:115], v[124:127], v[20:23]\n"
"v_fmac_f32 v176, s46, v192\n"
"v_fmac_f32 v177, s47, v193\n"
"v_fmac_f32 v178, s46, v194\n"
"v_fmac_f32 v179, s47, v195\n"
"v_fmac_f32 v180, s46, v196\n"
"v_fmac_f32 v181, s47, v197\n"
"v_fmac_f32 v182, s46, v198\n"
"v_fmac_f32 v183, s47, v199\n"
"v_fmac_f32 v184, s46, v200\n"
"v_fmac_f32 v185, s47, v201\n"
"v_fmac_f32 v186, s46, v202\n"
"v_fmac_f32 v187, s47, v203\n"
"v_fmac_f32 v188, s46, v204\n"
"v_fmac_f32 v189, s47, v205\n"
"v_fmac_f32 v190, s46, v206\n"
"v_fmac_f32 v191, s47, v207\n"
"v_mfma_f32_16x16x32_bf16 v[20:23], v[120:123], v[116:119], v[20:23]\n"
"ds_write_b128 v245, v[28:31] offset:3072\n"
"v_mfma_f32_16x16x32_bf16 v[16:19], v[112:115], v[116:119], v[16:19]\n"
"ds_write_b128 v245, v[24:27] offset:2048\n"
"s_nop 4\n"
"ds_write_b128 v245, v[20:23] offset:1024\n"
"s_nop 0\n"
"ds_write_b128 v245, v[16:19] offset:0\n"
"s_waitcnt lgkmcnt(0)\n"
"s_barrier\n"
"ds_read_b128 v[48:51], v244 offset:0\n"
"ds_read_b128 v[52:55], v244 offset:8192\n"
"ds_read_b128 v[56:59], v244 offset:16384\n"
"ds_read_b128 v[60:63], v244 offset:24576\n"
"ds_read_b128 v[64:67], v244 offset:1024\n"
"ds_read_b128 v[68:71], v244 offset:9216\n"
"ds_read_b128 v[72:75], v244 offset:17408\n"
"ds_read_b128 v[76:79], v244 offset:25600\n"
"s_waitcnt vmcnt(15)\n"
"s_waitcnt lgkmcnt(6)\n"
"v_pk_add_f32 v[48:49], v[48:49], v[52:53]\n"
"v_pk_add_f32 v[50:51], v[50:51], v[54:55]\n"
"s_waitcnt lgkmcnt(4)\n"
"v_pk_add_f32 v[56:57], v[56:57], v[60:61]\n"
"v_pk_add_f32 v[58:59], v[58:59], v[62:63]\n"
"v_fmac_f32 v176, s44, v48\n"
"v_fmac_f32 v177, s45, v56\n"
"v_fmac_f32 v178, s44, v49\n"
"v_fmac_f32 v179, s45, v57\n"
"v_fmac_f32 v180, s44, v50\n"
"v_fmac_f32 v181, s45, v58\n"
"v_fmac_f32 v182, s44, v51\n"
"v_fmac_f32 v183, s45, v59\n"
"v_cvt_pk_bf16_f32 v80, v176, v177\n"
"v_cvt_pk_bf16_f32 v81, v178, v179\n"
"v_cvt_pk_bf16_f32 v82, v180, v181\n"
"v_cvt_pk_bf16_f32 v83, v182, v183\n"
"buffer_store_dwordx4 v[80:83], v242, s[48:51], s84 offen sc1\n"
"s_waitcnt lgkmcnt(2)\n"
"v_pk_add_f32 v[64:65], v[64:65], v[68:69]\n"
"v_pk_add_f32 v[66:67], v[66:67], v[70:71]\n"
"s_waitcnt lgkmcnt(0)\n"
"v_pk_add_f32 v[72:73], v[72:73], v[76:77]\n"
"v_pk_add_f32 v[74:75], v[74:75], v[78:79]\n"
"v_fmac_f32 v184, s44, v64\n"
"v_fmac_f32 v185, s45, v72\n"
"v_fmac_f32 v186, s44, v65\n"
"v_fmac_f32 v187, s45, v73\n"
"v_fmac_f32 v188, s44, v66\n"
"v_fmac_f32 v189, s45, v74\n"
"v_fmac_f32 v190, s44, v67\n"
"v_fmac_f32 v191, s45, v75\n"
"v_cvt_pk_bf16_f32 v84, v184, v185\n"
"v_cvt_pk_bf16_f32 v85, v186, v187\n"
"v_cvt_pk_bf16_f32 v86, v188, v189\n"
"v_cvt_pk_bf16_f32 v87, v190, v191\n"
"buffer_store_dwordx4 v[84:87], v242, s[48:51], s85 offen sc1\n"
"s_waitcnt vmcnt(9)\n"
"v_cmp_ne_u32 vcc, s39, v15\n"
"ds_read_b64_tr_b16 a[112:113], v240 offset:0\n"
"ds_read_b64_tr_b16 a[114:115], v240 offset:1024\n"
"ds_read_b64_tr_b16 a[116:117], v241 offset:0\n"
"ds_read_b64_tr_b16 a[118:119], v241 offset:1024\n"
"ds_read_b64_tr_b16 a[120:121], v240 offset:2048\n"
"ds_read_b64_tr_b16 a[122:123], v240 offset:3072\n"
"ds_read_b64_tr_b16 a[124:125], v241 offset:2048\n"
"ds_read_b64_tr_b16 a[126:127], v241 offset:3072\n"
"ds_read_b64_tr_b16 a[128:129], v240 offset:4096\n"
"ds_read_b64_tr_b16 a[130:131], v240 offset:5120\n"
"ds_read_b64_tr_b16 a[132:133], v241 offset:4096\n"
"ds_read_b64_tr_b16 a[134:135], v241 offset:5120\n"
"s_cbranch_vccnz Lfl_%=\n"
"Lflr_%=:\n"
"s_mov_b32 m0, s80\n"
"s_nop 0\n"
"buffer_load_dword v233, s[48:51], s64 offen sc1 lds\n"
"ds_read_b64_tr_b16 a[136:137], v240 offset:6144\n"
"ds_read_b64_tr_b16 a[138:139], v240 offset:7168\n"
"ds_read_b64_tr_b16 a[140:141], v241 offset:6144\n"
"ds_read_b64_tr_b16 a[142:143], v241 offset:7168\n"
"s_waitcnt lgkmcnt(12)\n"
"s_add_u32 m0, s83, 0\n"
"s_add_u32 s87, s81, 0\n"
"buffer_load_dwordx4 v232, s[48:51], s87 offen offset:0 sc1 lds\n"
"buffer_load_dwordx4 v232, s[48:51], s87 offen offset:1024 sc1 lds\n"
"ds_read_b64_tr_b16 a[144:145], v240 offset:8192\n"
"ds_read_b64_tr_b16 a[146:147], v240 offset:9216\n"
"ds_read_b64_tr_b16 a[148:149], v241 offset:8192\n"
"ds_read_b64_tr_b16 a[150:151], v241 offset:9216\n"
"s_waitcnt lgkmcnt(12)\n"
"buffer_load_dwordx4 v232, s[48:51], s87 offen offset:2048 sc1 lds\n"
"buffer_load_dwordx4 v232, s[48:51], s87 offen offset:3072 sc1 lds\n"
"ds_read_b64_tr_b16 a[152:153], v240 offset:10240\n"
"ds_read_b64_tr_b16 a[154:155], v240 offset:11264\n"
"ds_read_b64_tr_b16 a[156:157], v241 offset:10240\n"
"ds_read_b64_tr_b16 a[158:159], v241 offset:11264\n"
"s_waitcnt lgkmcnt(12)\n"
"s_add_u32 m0, s83, 4096\n"
"s_add_u32 s87, s81, 4096\n"
"buffer_load_dwordx4 v232, s[48:51], s87 offen offset:0 sc1 lds\n"
"buffer_load_dwordx4 v232, s[48:51], s87 offen offset:1024 sc1 lds\n"
"ds_read_b64_tr_b16 a[160:161], v240 offset:12288\n"
"ds_read_b64_tr_b16 a[162:163], v240 offset:13312\n"
"ds_read_b64_tr_b16 a[164:165], v241 offset:12288\n"
"ds_read_b64_tr_b16 a[166:167], v241 offset:13312\n"
"s_waitcnt lgkmcnt(12)\n"
"buffer_load_dwordx4 v232, s[48:51], s87 offen offset:2048 sc1 lds\n"
"buffer_load_dwordx4 v232, s[48:51], s87 offen offset:3072 sc1 lds\n"
"ds_read_b64_tr_b16 a[168:169], v240 offset:14336\n"
"ds_read_b64_tr_b16 a[170:171], v240 offset:15360\n"
"ds_read_b64_tr_b16 a[172:173], v241 offset:14336\n"
"ds_read_b64_tr_b16 a[174:175], v241 offset:15360\n"
"s_waitcnt lgkmcnt(12)\n"
"s_add_u32 m0, s83, 8192\n"
"s_add_u32 s87, s81, 8192\n"
"buffer_load_dwordx4 v232, s[48:51], s87 offen offset:0 sc1 lds\n"
"buffer_load_dwordx4 v232, s[48:51], s87 offen offset:1024 sc1 lds\n"
"ds_read_b64_tr_b16 a[176:177], v240 offset:16384\n"
"ds_read_b64_tr_b16 a[178:179], v240 offset:17408\n"
"ds_read_b64_tr_b16 a[180:181], v241 offset:16384\n"
"ds_read_b64_tr_b16 a[182:183], v241 offset:17408\n"
"s_waitcnt lgkmcnt(12)\n"
"buffer_load_dwordx4 v232, s[48:51], s87 offen offset:2048 sc1 lds\n"
"buffer_load_dwordx4 v232, s[48:51], s87 offen offset:3072 sc1 lds\n"
"ds_read_b64_tr_b16 a[184:185], v240 offset:18432\n"
"ds_read_b64_tr_b16 a[186:187], v240 offset:19456\n"
"ds_read_b64_tr_b16 a[188:189], v241 offset:18432\n"
"ds_read_b64_tr_b16 a[190:191], v241 offset:19456\n"
"s_waitcnt lgkmcnt(12)\n"
"s_add_u32 m0, s83, 12288\n"
"s_add_u32 s87, s81, 12288\n"
"buffer_load_dwordx4 v232, s[48:51], s87 offen offset:0 sc1 lds\n"
"buffer_load_dwordx4 v232, s[48:51], s87 offen offset:1024 sc1 lds\n"
"s_waitcnt vmcnt(23)\n"
"ds_read_b64_tr_b16 a[192:193], v240 offset:20480\n"
"ds_read_b64_tr_b16 a[194:195], v240 offset:21504\n"
"ds_read_b64_tr_b16 a[196:197], v241 offset:20480\n"
"ds_read_b64_tr_b16 a[198:199], v241 offset:21504\n"
"s_waitcnt vmcnt(21)\n"
"ds_read_b64_tr_b16 a[200:201], v240 offset:22528\n"
"ds_read_b64_tr_b16 a[202:203], v240 offset:23552\n"
"ds_read_b64_tr_b16 a[204:205], v241 offset:22528\n"
"ds_read_b64_tr_b16 a[206:207], v241 offset:23552\n"
"s_waitcnt vmcnt(19)\n"
"ds_read_b64_tr_b16 a[208:209], v240 offset:24576\n"
"ds_read_b64_tr_b16 a[210:211], v240 offset:25600\n"
"ds_read_b64_tr_b16 a[212:213], v241 offset:24576\n"
"ds_read_b64_tr_b16 a[214:215], v241 offset:25600\n"
"s_waitcnt vmcnt(17)\n"
"ds_read_b64_tr_b16 a[216:217], v240 offset:26624\n"
"ds_read_b64_tr_b16 a[218:219], v240 offset:27648\n"
"ds_read_b64_tr_b16 a[220:221], v241 offset:26624\n"
"ds_read_b64_tr_b16 a[222:223], v241 offset:27648\n"
"s_waitcnt lgkmcnt(0)\n"
"v_add_u32 v228, 49152, v232\n"
"v_add_u32 v229, 77824, v232\n"
"v_add_u32 v230, 106496, v232\n"
"v_add_u32 v231, 135168, v232\n"
"v_mov_b32 v184, 0\n"
"v_mov_b32 v185, 0\n"
"v_mov_b32 v186, 0\n"
"v_mov_b32 v187, 0\n"
"v_mov_b32 v189, 0x3f803f80\n"
"s_add_u32 s4, s48, s79\n"
"s_addc_u32 s5, s49, 0\n"
"s_add_u32 s4, s4, 4096\n"
"s_addc_u32 s5, s5, 0\n"
"s_add_u32 s6, s4, 8192\n"
"s_addc_u32 s7, s5, 0\n"
"s_add_u32 s8, s4, 16384\n"
"s_addc_u32 s9, s5, 0\n"
"s_add_u32 s10, s4, 24576\n"
"s_addc_u32 s11, s5, 0\n"
"global_load_dword v16, v233, s[4:5] offset:-4096 sc1\n"
"global_load_dword v17, v233, s[4:5] offset:-3840 sc1\n"
"global_load_dword v18, v233, s[4:5] offset:-3584 sc1\n"
"global_load_dword v19, v233, s[4:5] offset:-3328 sc1\n"
"global_load_dword v20, v233, s[4:5] offset:-3072 sc1\n"
"global_load_dword v21, v233, s[4:5] offset:-2816 sc1\n"
"global_load_dword v22, v233, s[4:5] offset:-2560 sc1\n"
"global_load_dword v23, v233, s[4:5] offset:-2304 sc1\n"
"global_load_dword v24, v233, s[4:5] offset:-2048 sc1\n"
"global_load_dword v25, v233, s[4:5] offset:-1792 sc1\n"
"global_load_dword v26, v233, s[4:5] offset:-1536 sc1\n"
"global_load_dword v27, v233, s[4:5] offset:-1280 sc1\n"
"global_load_dword v28, v233, s[4:5] offset:-1024 sc1\n"
"global_load_dword v29, v233, s[4:5] offset:-768 sc1\n"
"global_load_dword v30, v233, s[4:5] offset:-512 sc1\n"
"global_load_dword v31, v233, s[4:5] offset:-256 sc1\n"
"global_load_dword v32, v233, s[4:5] offset:0 sc1\n"
"global_load_dword v33, v233, s[4:5] offset:256 sc1\n"
"global_load_dword v34, v233, s[4:5] offset:512 sc1\n"
"global_load_dword v35, v233, s[4:5] offset:768 sc1\n"
"global_load_dword v36, v233, s[4:5] offset:1024 sc1\n"
"global_load_dword v37, v233, s[4:5] offset:1280 sc1\n"
"global_load_dword v38, v233, s[4:5] offset:1536 sc1\n"
"global_load_dword v39, v233, s[4:5] offset:1792 sc1\n"
"global_load_dword v40, v233, s[4:5] offset:2048 sc1\n"
"global_load_dword v41, v233, s[4:5] offset:2304 sc1\n"
"global_load_dword v42, v233, s[4:5] offset:2560 sc1\n"
"global_load_dword v43, v233, s[4:5] offset:2816 sc1\n"
"global_load_dword v44, v233, s[4:5] offset:3072 sc1\n"
"global_load_dword v45, v233, s[4:5] offset:3328 sc1\n"
"global_load_dword v46, v233, s[4:5] offset:3584 sc1\n"
"global_load_dword v47, v233, s[4:5] offset:3840 sc1\n"
"global_load_dword v48, v233, s[6:7] offset:-4096 sc1\n"
"global_load_dword v49, v233, s[6:7] offset:-3840 sc1\n"
"global_load_dword v50, v233, s[6:7] offset:-3584 sc1\n"
"global_load_dword v51, v233, s[6:7] offset:-3328 sc1\n"
"global_load_dword v52, v233, s[6:7] offset:-3072 sc1\n"
"global_load_dword v53, v233, s[6:7] offset:-2816 sc1\n"
"global_load_dword v54, v233, s[6:7] offset:-2560 sc1\n"
"global_load_dword v55, v233, s[6:7] offset:-2304 sc1\n"
"global_load_dword v56, v233, s[6:7] offset:-2048 sc1\n"
"global_load_dword v57, v233, s[6:7] offset:-1792 sc1\n"
"global_load_dword v58, v233, s[6:7] offset:-1536 sc1\n"
"global_load_dword v59, v233, s[6:7] offset:-1280 sc1\n"
"global_load_dword v60, v233, s[6:7] offset:-1024 sc1\n"
"global_load_dword v61, v233, s[6:7] offset:-768 sc1\n"
"global_load_dword v62, v233, s[6:7] offset:-512 sc1\n"
"global_load_dword v63, v233, s[6:7] offset:-256 sc1\n"
"global_load_dword v64, v233, s[6:7] offset:0 sc1\n"
"global_load_dword v65, v233, s[6:7] offset:256 sc1\n"
"global_load_dword v66, v233, s[6:7] offset:512 sc1\n"
"global_load_dword v67, v233, s[6:7] offset:768 sc1\n"
"global_load_dword v68, v233, s[6:7] offset:1024 sc1\n"
"global_load_dword v69, v233, s[6:7] offset:1280 sc1\n"
"global_load_dword v70, v233, s[6:7] offset:1536 sc1\n"
"global_load_dword v71, v233, s[6:7] offset:1792 sc1\n"
"s_waitcnt vmcnt(56)\n"
"s_barrier\n"
"ds_read_b128 v[160:163], v228 offset:0\n"
"ds_read_b128 v[164:167], v229 offset:0\n"
"ds_read_b128 v[168:171], v230 offset:0\n"
"ds_read_b128 v[172:175], v231 offset:0\n"
"ds_read_b128 v[176:179], v228 offset:1024\n"
"s_waitcnt vmcnt(48)\n"
"v_dot2c_f32_bf16 v186, v16, v189\n"
"v_dot2c_f32_bf16 v187, v16, v16\n"
"v_dot2c_f32_bf16 v186, v17, v189\n"
"v_dot2c_f32_bf16 v187, v17, v17\n"
"v_dot2c_f32_bf16 v186, v18, v189\n"
"v_dot2c_f32_bf16 v187, v18, v18\n"
"v_dot2c_f32_bf16 v186, v19, v189\n"
"v_dot2c_f32_bf16 v187, v19, v19\n"
"v_dot2c_f32_bf16 v186, v20, v189\n"
"v_dot2c_f32_bf16 v187, v20, v20\n"
"v_dot2c_f32_bf16 v186, v21, v189\n"
"v_dot2c_f32_bf16 v187, v21, v21\n"
"v_dot2c_f32_bf16 v186, v22, v189\n"
"v_dot2c_f32_bf16 v187, v22, v22\n"
"v_dot2c_f32_bf16 v186, v23, v189\n"
"v_dot2c_f32_bf16 v187, v23, v23\n"
"s_nop 2\n"
"v_cmp_u_f32 vcc, v187, v187\n"
"s_nop 4\n"
"s_cbranch_vccnz Lx0_%=\n"
"Lxr0_%=:\n"
"v_add_f32 v184, v184, v186\n"
"v_add_f32 v185, v185, v187\n"
"v_mov_b32 v186, 0\n"
"v_mov_b32 v187, 0\n"
"s_waitcnt vmcnt(40)\n"
"v_dot2c_f32_bf16 v186, v24, v189\n"
"v_dot2c_f32_bf16 v187, v24, v24\n"
"v_dot2c_f32_bf16 v186, v25, v189\n"
"v_dot2c_f32_bf16 v187, v25, v25\n"
"v_dot2c_f32_bf16 v186, v26, v189\n"
"v_dot2c_f32_bf16 v187, v26, v26\n"
"v_dot2c_f32_bf16 v186, v27, v189\n"
"v_dot2c_f32_bf16 v187, v27, v27\n"
"v_dot2c_f32_bf16 v186, v28, v189\n"
"v_dot2c_f32_bf16 v187, v28, v28\n"
"v_dot2c_f32_bf16 v186, v29, v189\n"
"v_dot2c_f32_bf16 v187, v29, v29\n"
"v_dot2c_f32_bf16 v186, v30, v189\n"
"v_dot2c_f32_bf16 v187, v30, v30\n"
"v_dot2c_f32_bf16 v186, v31, v189\n"
"v_dot2c_f32_bf16 v187, v31, v31\n"
"s_nop 2\n"
"v_cmp_u_f32 vcc, v187, v187\n"
"s_waitcnt lgkmcnt(4)\n"
"v_mfma_f32_32x32x16_bf16 v[128:143], v[160:163], v[16:19], 0\n"
"ds_read_b128 v[180:183], v229 offset:1024\n"
"global_load_dword v72, v233, s[6:7] offset:2048 sc1\n"
"global_load_dword v73, v233, s[6:7] offset:2304 sc1\n"
"global_load_dword v74, v233, s[6:7] offset:2560 sc1\n"
"global_load_dword v75, v233, s[6:7] offset:2816 sc1\n"
"s_waitcnt lgkmcnt(4)\n"
"v_mfma_f32_32x32x16_bf16 v[128:143], v[164:167], v[20:23], v[128:143]\n"
"ds_read_b128 v[160:163], v230 offset:1024\n"
"global_load_dword v76, v233, s[6:7] offset:3072 sc1\n"
"global_load_dword v77, v233, s[6:7] offset:3328 sc1\n"
"global_load_dword v78, v233, s[6:7] offset:3584 sc1\n"
"global_load_dword v79, v233, s[6:7] offset:3840 sc1\n"
"s_cbranch_vccnz Lx1_%=\n"
"Lxr1_%=:\n"
"v_add_f32 v184, v184, v186\n"
"v_add_f32 v185, v185, v187\n"
"v_mov_b32 v186, 0\n"
"v_mov_b32 v187, 0\n"
"s_waitcnt vmcnt(40)\n"
"v_dot2c_f32_bf16 v186, v32, v189\n"
"v_dot2c_f32_bf16 v187, v32, v32\n"
"v_dot2c_f32_bf16 v186, v33, v189\n"
"v_dot2c_f32_bf16 v187, v33, v33\n"
"v_dot2c_f32_bf16 v186, v34, v189\n"
"v_dot2c_f32_bf16 v187, v34, v34\n"
"v_dot2c_f32_bf16 v186, v35, v189\n"
"v_dot2c_f32_bf16 v187, v35, v35\n"
"v_dot2c_f32_bf16 v186, v36, v189\n"
"v_dot2c_f32_bf16 v187, v36, v36\n"
"v_dot2c_f32_bf16 v186, v37, v189\n"
"v_dot2c_f32_bf16 v187, v37, v37\n"
"v_dot2c_f32_bf16 v186, v38, v189\n"
"v_dot2c_f32_bf16 v187, v38, v38\n"
"v_dot2c_f32_bf16 v186, v39, v189\n"
"v_dot2c_f32_bf16 v187, v39, v39\n"
"s_nop 2\n"
"v_cmp_u_f32 vcc, v187, v187\n"
"s_waitcnt lgkmcnt(4)\n"
"v_mfma_f32_32x32x16_bf16 v[128:143], v[168:171], v[24:27], v[128:143]\n"
"ds_read_b128 v[164:167], v231 offset:1024\n"
"global_load_dword v80, v233, s[8:9] offset:-4096 sc1\n"
"global_load_dword v81, v233, s[8:9] offset:-3840 sc1\n"
"global_load_dword v82, v233, s[8:9] offset:-3584 sc1\n"
"global_load_dword v83, v233, s[8:9] offset:-3328 sc1\n"
"s_waitcnt lgkmcnt(4)\n"
"v_mfma_f32_32x32x16_bf16 v[128:143], v[172:175], v[28:31], v[128:143]\n"
"ds_read_b128 v[168:171], v228 offset:2048\n"
"global_load_dword v84, v233, s[8:9] offset:-3072 sc1\n"
"global_load_dword v85, v233, s[8:9] offset:-2816 sc1\n"
"global_load_dword v86, v233, s[8:9] offset:-2560 sc1\n"
"global_load_dword v87, v233, s[8:9] offset:-2304 sc1\n"
"s_cbranch_vccnz Lx2_%=\n"
"Lxr2_%=:\n"
"v_add_f32 v184, v184, v186\n"
"v_add_f32 v185, v185, v187\n"
"v_mov_b32 v186, 0\n"
"v_mov_b32 v187, 0\n"
"s_waitcnt vmcnt(40)\n"
"v_dot2c_f32_bf16 v186, v40, v189\n"
"v_dot2c_f32_bf16 v187, v40, v40\n"
"v_dot2c_f32_bf16 v186, v41, v189\n"
"v_dot2c_f32_bf16 v187, v41, v41\n"
"v_dot2c_f32_bf16 v186, v42, v189\n"
"v_dot2c_f32_bf16 v187, v42, v42\n"
"v_dot2c_f32_bf16 v186, v43, v189\n"
"v_dot2c_f32_bf16 v187, v43, v43\n"
"v_dot2c_f32_bf16 v186, v44, v189\n"
"v_dot2c_f32_bf16 v187, v44, v44\n"
"v_dot2c_f32_bf16 v186, v45, v189\n"
"v_dot2c_f32_bf16 v187, v45, v45\n"
"v_dot2c_f32_bf16 v186, v46, v189\n"
"v_dot2c_f32_bf16 v187, v46, v46\n"
"v_dot2c_f32_bf16 v186, v47, v189\n"
"v_dot2c_f32_bf16 v187, v47, v47\n"
"s_nop 2\n"
"v_cmp_u_f32 vcc, v187, v187\n"
"s_waitcnt lgkmcnt(4)\n"
"v_mfma_f32_32x32x16_bf16 v[128:143], v[176:179], v[32:35], v[128:143]\n"
"ds_read_b128 v[172:175], v229 offset:2048\n"
"global_load_dword v88, v233, s[8:9] offset:-2048 sc1\n"
"global_load_dword v89, v233, s[8:9] offset:-1792 sc1\n"
"global_load_dword v90, v233, s[8:9] offset:-1536 sc1\n"
"global_load_dword v91, v233, s[8:9] offset:-1280 sc1\n"
"s_waitcnt lgkmcnt(4)\n"
"v_mfma_f32_32x32x16_bf16 v[128:143], v[180:183], v[36:39], v[128:143]\n"
"ds_read_b128 v[176:179], v230 offset:2048\n"
"global_load_dword v92, v233, s[8:9] offset:-1024 sc1\n"
"global_load_dword v93, v233, s[8:9] offset:-768 sc1\n"
"global_load_dword v94, v233, s[8:9] offset:-512 sc1\n"
"global_load_dword v95, v233, s[8:9] offset:-256 sc1\n"
"s_cbranch_vccnz Lx3_%=\n"
"Lxr3_%=:\n"
"v_add_f32 v184, v184, v186\n"
"v_add_f32 v185, v185, v187\n"
"v_mov_b32 v186, 0\n"
"v_mov_b32 v187, 0\n"
"s_waitcnt vmcnt(40)\n"
"v_dot2c_f32_bf16 v186, v48, v189\n"
"v_dot2c_f32_bf16 v187, v48, v48\n"
"v_dot2c_f32_bf16 v186, v49, v189\n"
"v_dot2c_f32_bf16 v187, v49, v49\n"
"v_dot2c_f32_bf16 v186, v50, v189\n"
"v_dot2c_f32_bf16 v187, v50, v50\n"
"v_dot2c_f32_bf16 v186, v51, v189\n"
"v_dot2c_f32_bf16 v187, v51, v51\n"
"v_dot2c_f32_bf16 v186, v52, v189\n"
"v_dot2c_f32_bf16 v187, v52, v52\n"
"v_dot2c_f32_bf16 v186, v53, v189\n"
"v_dot2c_f32_bf16 v187, v53, v53\n"
"v_dot2c_f32_bf16 v186, v54, v189\n"
"v_dot2c_f32_bf16 v187, v54, v54\n"
"v_dot2c_f32_bf16 v186, v55, v189\n"
"v_dot2c_f32_bf16 v187, v55, v55\n"
"s_nop 2\n"
"v_cmp_u_f32 vcc, v187, v187\n"
"s_waitcnt lgkmcnt(4)\n"
"v_mfma_f32_32x32x16_bf16 v[128:143], v[160:163], v[40:43], v[128:143]\n"
"ds_read_b128 v[180:183], v231 offset:2048\n"
"global_load_dword v96, v233, s[8:9] offset:0 sc1\n"
"global_load_dword v97, v233, s[8:9] offset:256 sc1\n"
"global_load_dword v98, v233, s[8:9] offset:512 sc1\n"
"global_load_dword v99, v233, s[8:9] offset:768 sc1\n"
"s_waitcnt lgkmcnt(4)\n"
"v_mfma_f32_32x32x16_bf16 v[128:143], v[164:167], v[44:47], v[128:143]\n"
"ds_read_b128 v[160:163], v228 offset:3072\n"
"global_load_dword v100, v233, s[8:9] offset:1024 sc1\n"
"global_load_dword v101, v233, s[8:9] offset:1280 sc1\n"
"global_load_dword v102, v233, s[8:9] offset:1536 sc1\n"
"global_load_dword v103, v233, s[8:9] offset:1792 sc1\n"
"s_cbranch_vccnz Lx4_%=\n"
"Lxr4_%=:\n"
"v_add_f32 v184, v184, v186\n"
"v_add_f32 v185, v185, v187\n"
"v_mov_b32 v186, 0\n"
"v_mov_b32 v187, 0\n"
"s_waitcnt vmcnt(40)\n"
"v_dot2c_f32_bf16 v186, v56, v189\n"
"v_dot2c_f32_bf16 v187, v56, v56\n"
"v_dot2c_f32_bf16 v186, v57, v189\n"
"v_dot2c_f32_bf16 v187, v57, v57\n"
"v_dot2c_f32_bf16 v186, v58, v189\n"
"v_dot2c_f32_bf16 v187, v58, v58\n"
"v_dot2c_f32_bf16 v186, v59, v189\n"
"v_dot2c_f32_bf16 v187, v59, v59\n"
"v_dot2c_f32_bf16 v186, v60, v189\n"
"v_dot2c_f32_bf16 v187, v60, v60\n"
"v_dot2c_f32_bf16 v186, v61, v189\n"
"v_dot2c_f32_bf16 v187, v61, v61\n"
"v_dot2c_f32_bf16 v186, v62, v189\n"
"v_dot2c_f32_bf16 v187, v62, v62\n"
"v_dot2c_f32_bf16 v186, v63, v189\n"
"v_dot2c_f32_bf16 v187, v63, v63\n"
"s_nop 2\n"
"v_cmp_u_f32 vcc, v187, v187\n"
"s_waitcnt lgkmcnt(4)\n"
"v_mfma_f32_32x32x16_bf16 v[128:143], v[168:171], v[48:51], v[128:143]\n"
"ds_read_b128 v[164:167], v229 offset:3072\n"
"global_load_dword v104, v233, s[8:9] offset:2048 sc1\n"
"global_load_dword v105, v233, s[8:9] offset:2304 sc1\n"
"global_load_dword v106, v233, s[8:9] offset:2560 sc1\n"
"global_load_dword v107, v233, s[8:9] offset:2816 sc1\n"
"s_waitcnt lgkmcnt(4)\n"
"v_mfma_f32_32x32x16_bf16 v[128:143], v[172:175], v[52:55], v[128:143]\n"
"ds_read_b128 v[168:171], v230 offset:3072\n"
"global_load_dword v108, v233, s[8:9] offset:3072 sc1\n"
"global_load_dword v109, v233, s[8:9] offset:3328 sc1\n"
"global_load_dword v110, v233, s[8:9] offset:3584 sc1\n"
"global_load_dword v111, v233, s[8:9] offset:3840 sc1\n"
"s_cbranch_vccnz Lx5_%=\n"
"Lxr5_%=:\n"
"v_add_f32 v184, v184, v186\n"
"v_add_f32 v185, v185, v187\n"
"v_mov_b32 v186, 0\n"
"v_mov_b32 v187, 0\n"
"s_waitcnt vmcnt(40)\n"
"v_dot2c_f32_bf16 v186, v64, v189\n"
"v_dot2c_f32_bf16 v187, v64, v64\n"
"v_dot2c_f32_bf16 v186, v65, v189\n"
"v_dot2c_f32_bf16 v187, v65, v65\n"
"v_dot2c_f32_bf16 v186, v66, v189\n"
"v_dot2c_f32_bf16 v187, v66, v66\n"
"v_dot2c_f32_bf16 v186, v67, v189\n"
"v_dot2c_f32_bf16 v187, v67, v67\n"
"v_dot2c_f32_bf16 v186, v68, v189\n"
"v_dot2c_f32_bf16 v187, v68, v68\n"
"v_dot2c_f32_bf16 v186, v69, v189\n"
"v_dot2c_f32_bf16 v187, v69, v69\n"
"v_dot2c_f32_bf16 v186, v70, v189\n"
"v_dot2c_f32_bf16 v187, v70, v70\n"
"v_dot2c_f32_bf16 v186, v71, v189\n"
"v_dot2c_f32_bf16 v187, v71, v71\n"
"s_nop 2\n"
"v_cmp_u_f32 vcc, v187, v187\n"
"s_waitcnt lgkmcnt(4)\n"
"v_mfma_f32_32x32x16_bf16 v[128:143], v[176:179], v[56:59], v[128:143]\n"
"ds_read_b128 v[172:175], v231 offset:3072\n"
"global_load_dword v112, v233, s[10:11] offset:-4096 sc1\n"
"global_load_dword v113, v233, s[10:11] offset:-3840 sc1\n"
"global_load_dword v114, v233, s[10:11] offset:-3584 sc1\n"
"global_load_dword v115, v233, s[10:11] offset:-3328 sc1\n"
"s_waitcnt lgkmcnt(4)\n"
"v_mfma_f32_32x32x16_bf16 v[128:143], v[180:183], v[60:63], v[128:143]\n"
"ds_read_b128 v[176:179], v228 offset:4096\n"
"global_load_dword v116, v233, s[10:11] offset:-3072 sc1\n"
"global_load_dword v117, v233, s[10:11] offset:-2816 sc1\n"
"global_load_dword v118, v233, s[10:11] offset:-2560 sc1\n"
"global_load_dword v119, v233, s[10:11] offset:-2304 sc1\n"
"s_cbranch_vccnz Lx6_%=\n"
"Lxr6_%=:\n"
"v_add_f32 v184, v184, v186\n"
"v_add_f32 v185, v185, v187\n"
"v_mov_b32 v186, 0\n"
"v_mov_b32 v187, 0\n"
"s_waitcnt vmcnt(40)\n"
"v_dot2c_f32_bf16 v186, v72, v189\n"
"v_dot2c_f32_bf16 v187, v72, v72\n"
"v_dot2c_f32_bf16 v186, v73, v189\n"
"v_dot2c_f32_bf16 v187, v73, v73\n"
"v_dot2c_f32_bf16 v186, v74, v189\n"
"v_dot2c_f32_bf16 v187, v74, v74\n"
"v_dot2c_f32_bf16 v186, v75, v189\n"
"v_dot2c_f32_bf16 v187, v75, v75\n"
"v_dot2c_f32_bf16 v186, v76, v189\n"
"v_dot2c_f32_bf16 v187, v76, v76\n"
"v_dot2c_f32_bf16 v186, v77, v189\n"
"v_dot2c_f32_bf16 v187, v77, v77\n"
"v_dot2c_f32_bf16 v186, v78, v189\n"
"v_dot2c_f32_bf16 v187, v78, v78\n"
"v_dot2c_f32_bf16 v186, v79, v189\n"
"v_dot2c_f32_bf16 v187, v79, v79\n"
"s_nop 2\n"
"v_cmp_u_f32 vcc, v187, v187\n"
"s_waitcnt lgkmcnt(4)\n"
"v_mfma_f32_32x32x16_bf16 v[128:143], v[160:163], v[64:67], v[128:143]\n"
"ds_read_b128 v[180:183], v229 offset:4096\n"
"global_load_dword v120, v233, s[10:11] offset:-2048 sc1\n"
"global_load_dword v121, v233, s[10:11] offset:-1792 sc1\n"
"global_load_dword v122, v233, s[10:11] offset:-1536 sc1\n"
"global_load_dword v123, v233, s[10:11] offset:-1280 sc1\n"
"s_waitcnt lgkmcnt(4)\n"
"v_mfma_f32_32x32x16_bf16 v[128:143], v[164:167], v[68:71], v[128:143]\n"
"ds_read_b128 v[160:163], v230 offset:4096\n"
"global_load_dword v124, v233, s[10:11] offset:-1024 sc1\n"
"global_load_dword v125, v233, s[10:11] offset:-768 sc1\n"
"global_load_dword v126, v233, s[10:11] offset:-512 sc1\n"
"global_load_dword v127, v233, s[10:11] offset:-256 sc1\n"
"s_cbranch_vccnz Lx7_%=\n"
"Lxr7_%=:\n"
"v_add_f32 v184, v184, v186\n"
"v_add_f32 v185, v185, v187\n"
"v_mov_b32 v186, 0\n"
"v_mov_b32 v187, 0\n"
"s_waitcnt vmcnt(40)\n"
"v_dot2c_f32_bf16 v186, v80, v189\n"
"v_dot2c_f32_bf16 v187, v80, v80\n"
"v_dot2c_f32_bf16 v186, v81, v189\n"
"v_dot2c_f32_bf16 v187, v81, v81\n"
"v_dot2c_f32_bf16 v186, v82, v189\n"
"v_dot2c_f32_bf16 v187, v82, v82\n"
"v_dot2c_f32_bf16 v186, v83, v189\n"
"v_dot2c_f32_bf16 v187, v83, v83\n"
"v_dot2c_f32_bf16 v186, v84, v189\n"
"v_dot2c_f32_bf16 v187, v84, v84\n"
"v_dot2c_f32_bf16 v186, v85, v189\n"
"v_dot2c_f32_bf16 v187, v85, v85\n"
"v_dot2c_f32_bf16 v186, v86, v189\n"
"v_dot2c_f32_bf16 v187, v86, v86\n"
"v_dot2c_f32_bf16 v186, v87, v189\n"
"v_dot2c_f32_bf16 v187, v87, v87\n"
"s_nop 2\n"
"v_cmp_u_f32 vcc, v187, v187\n"
"s_waitcnt lgkmcnt(4)\n"
"v_mfma_f32_32x32x16_bf16 v[128:143], v[168:171], v[72:75], v[128:143]\n"
"ds_read_b128 v[164:167], v231 offset:4096\n"
"buffer_load_dwordx4 v232, s[48:51], s87 offen offset:2048 sc1 lds\n"
"s_waitcnt lgkmcnt(4)\n"
"v_mfma_f32_32x32x16_bf16 v[128:143], v[172:175], v[76:79], v[128:143]\n"
"ds_read_b128 v[168:171], v228 offset:5120\n"
"buffer_load_dwordx4 v232, s[48:51], s87 offen offset:3072 sc1 lds\n"
"s_cbranch_vccnz Lx8_%=\n"
"Lxr8_%=:\n"
"v_add_f32 v184, v184, v186\n"
"v_add_f32 v185, v185, v187\n"
"v_mov_b32 v186, 0\n"
"v_mov_b32 v187, 0\n"
"s_waitcnt vmcnt(34)\n"
"v_dot2c_f32_bf16 v186, v88, v189\n"
"v_dot2c_f32_bf16 v187, v88, v88\n"
"v_dot2c_f32_bf16 v186, v89, v189\n"
"v_dot2c_f32_bf16 v187, v89, v89\n"
"v_dot2c_f32_bf16 v186, v90, v189\n"
"v_dot2c_f32_bf16 v187, v90, v90\n"
"v_dot2c_f32_bf16 v186, v91, v189\n"
"v_dot2c_f32_bf16 v187, v91, v91\n"
"v_dot2c_f32_bf16 v186, v92, v189\n"
"v_dot2c_f32_bf16 v187, v92, v92\n"
"v_dot2c_f32_bf16 v186, v93, v189\n"
"v_dot2c_f32_bf16 v187, v93, v93\n"
"v_dot2c_f32_bf16 v186, v94, v189\n"
"v_dot2c_f32_bf16 v187, v94, v94\n"
"v_dot2c_f32_bf16 v186, v95, v189\n"
"v_dot2c_f32_bf16 v187, v95, v95\n"
"s_nop 2\n"
"v_cmp_u_f32 vcc, v187, v187\n"
"s_waitcnt lgkmcnt(4)\n"
"v_mfma_f32_32x32x16_bf16 v[128:143], v[176:179], v[80:83], v[128:143]\n"
"ds_read_b128 v[172:175], v229 offset:5120\n"
"s_add_u32 m0, s83, 16384\n"
"s_add_u32 s87, s81, 16384\n"
"buffer_load_dwordx4 v232, s[48:51], s87 offen offset:0 sc1 lds\n"
"s_waitcnt lgkmcnt(4)\n"
"v_mfma_f32_32x32x16_bf16 v[128:143], v[180:183], v[84:87], v[128:143]\n"
"ds_read_b128 v[176:179], v230 offset:5120\n"
"buffer_load_dwordx4 v232, s[48:51], s87 offen offset:1024 sc1 lds\n"
"s_cbranch_vccnz Lx9_%=\n"
"Lxr9_%=:\n"
"v_add_f32 v184, v184, v186\n"
"v_add_f32 v185, v185, v187\n"
"v_mov_b32 v186, 0\n"
"v_mov_b32 v187, 0\n"
"s_waitcnt vmcnt(28)\n"
"v_dot2c_f32_bf16 v186, v96, v189\n"
"v_dot2c_f32_bf16 v187, v96, v96\n"
"v_dot2c_f32_bf16 v186, v97, v189\n"
"v_dot2c_f32_bf16 v187, v97, v97\n"
"v_dot2c_f32_bf16 v186, v98, v189\n"
"v_dot2c_f32_bf16 v187, v98, v98\n"
"v_dot2c_f32_bf16 v186, v99, v189\n"
"v_dot2c_f32_bf16 v187, v99, v99\n"
"v_dot2c_f32_bf16 v186, v100, v189\n"
"v_dot2c_f32_bf16 v187, v100, v100\n"
"v_dot2c_f32_bf16 v186, v101, v189\n"
"v_dot2c_f32_bf16 v187, v101, v101\n"
"v_dot2c_f32_bf16 v186, v102, v189\n"
"v_dot2c_f32_bf16 v187, v102, v102\n"
"v_dot2c_f32_bf16 v186, v103, v189\n"
"v_dot2c_f32_bf16 v187, v103, v103\n"
"s_nop 2\n"
"v_cmp_u_f32 vcc, v187, v187\n"
"s_waitcnt lgkmcnt(4)\n"
"v_mfma_f32_32x32x16_bf16 v[128:143], v[160:163], v[88:91], v[128:143]\n"
"ds_read_b128 v[180:183], v231 offset:5120\n"
"buffer_load_dwordx4 v232, s[48:51], s87 offen offset:2048 sc1 lds\n"
"s_waitcnt lgkmcnt(4)\n"
"v_mfma_f32_32x32x16_bf16 v[128:143], v[164:167], v[92:95], v[128:143]\n"
"ds_read_b128 v[160:163], v228 offset:6144\n"
"buffer_load_dwordx4 v232, s[48:51], s87 offen offset:3072 sc1 lds\n"
"s_cbranch_vccnz Lx10_%=\n"
"Lxr10_%=:\n"
"v_add_f32 v184, v184, v186\n"
"v_add_f32 v185, v185, v187\n"
"v_mov_b32 v186, 0\n"
"v_mov_b32 v187, 0\n"
"s_waitcnt vmcnt(22)\n"
"v_dot2c_f32_bf16 v186, v104, v189\n"
"v_dot2c_f32_bf16 v187, v104, v104\n"
"v_dot2c_f32_bf16 v186, v105, v189\n"
"v_dot2c_f32_bf16 v187, v105, v105\n"
"v_dot2c_f32_bf16 v186, v106, v189\n"
"v_dot2c_f32_bf16 v187, v106, v106\n"
"v_dot2c_f32_bf16 v186, v107, v189\n"
"v_dot2c_f32_bf16 v187, v107, v107\n"
"v_dot2c_f32_bf16 v186, v108, v189\n"
"v_dot2c_f32_bf16 v187, v108, v108\n"
"v_dot2c_f32_bf16 v186, v109, v189\n"
"v_dot2c_f32_bf16 v187, v109, v109\n"
"v_dot2c_f32_bf16 v186, v110, v189\n"
"v_dot2c_f32_bf16 v187, v110, v110\n"
"v_dot2c_f32_bf16 v186, v111, v189\n"
"v_dot2c_f32_bf16 v187, v111, v111\n"
"s_nop 2\n"
"v_cmp_u_f32 vcc, v187, v187\n"
"s_waitcnt lgkmcnt(4)\n"
"v_mfma_f32_32x32x16_bf16 v[128:143], v[168:171], v[96:99], v[128:143]\n"
"ds_read_b128 v[164:167], v229 offset:6144\n"
"s_add_u32 m0, s83, 20480\n"
"s_add_u32 s87, s81, 20480\n"
"buffer_load_dwordx4 v232, s[48:51], s87 offen offset:0 sc1 lds\n"
"s_waitcnt lgkmcnt(4)\n"
"v_mfma_f32_32x32x16_bf16 v[128:143], v[172:175], v[100:103], v[128:143]\n"
"ds_read_b128 v[168:171], v230 offset:6144\n"
"buffer_load_dwordx4 v232, s[48:51], s87 offen offset:1024 sc1 lds\n"
"s_cbranch_vccnz Lx11_%=\n"
"Lxr11_%=:\n"
"v_add_f32 v184, v184, v186\n"
"v_add_f32 v185, v185, v187\n"
"v_mov_b32 v186, 0\n"
"v_mov_b32 v187, 0\n"
"s_waitcnt vmcnt(16)\n"
"v_dot2c_f32_bf16 v186, v112, v189\n"
"v_dot2c_f32_bf16 v187, v112, v112\n"
"v_dot2c_f32_bf16 v186, v113, v189\n"
"v_dot2c_f32_bf16 v187, v113, v113\n"
"v_dot2c_f32_bf16 v186, v114, v189\n"
"v_dot2c_f32_bf16 v187, v114, v114\n"
"v_dot2c_f32_bf16 v186, v115, v189\n"
"v_dot2c_f32_bf16 v187, v115, v115\n"
"v_dot2c_f32_bf16 v186, v116, v189\n"
"v_dot2c_f32_bf16 v187, v116, v116\n"
"v_dot2c_f32_bf16 v186, v117, v189\n"
"v_dot2c_f32_bf16 v187, v117, v117\n"
"v_dot2c_f32_bf16 v186, v118, v189\n"
"v_dot2c_f32_bf16 v187, v118, v118\n"
"v_dot2c_f32_bf16 v186, v119, v189\n"
"v_dot2c_f32_bf16 v187, v119, v119\n"
"s_nop 2\n"
"v_cmp_u_f32 vcc, v187, v187\n"
"s_waitcnt lgkmcnt(4)\n"
"v_mfma_f32_32x32x16_bf16 v[128:143], v[176:179], v[104:107], v[128:143]\n"
"ds_read_b128 v[172:175], v231 offset:6144\n"
"buffer_load_dwordx4 v232, s[48:51], s87 offen offset:2048 sc1 lds\n"
"s_waitcnt lgkmcnt(4)\n"
"v_mfma_f32_32x32x16_bf16 v[128:143], v[180:183], v[108:111], v[128:143]\n"
"ds_read_b128 v[176:179], v228 offset:7168\n"
"buffer_load_dwordx4 v232, s[48:51], s87 offen offset:3072 sc1 lds\n"
"s_cbranch_vccnz Lx12_%=\n"
"Lxr12_%=:\n"
"v_add_f32 v184, v184, v186\n"
"v_add_f32 v185, v185, v187\n"
"v_mov_b32 v186, 0\n"
"v_mov_b32 v187, 0\n"
"s_waitcnt vmcnt(10)\n"
"v_dot2c_f32_bf16 v186, v120, v189\n"
"v_dot2c_f32_bf16 v187, v120, v120\n"
"v_dot2c_f32_bf16 v186, v121, v189\n"
"v_dot2c_f32_bf16 v187, v121, v121\n"
"v_dot2c_f32_bf16 v186, v122, v189\n"
"v_dot2c_f32_bf16 v187, v122, v122\n"
"v_dot2c_f32_bf16 v186, v123, v189\n"
"v_dot2c_f32_bf16 v187, v123, v123\n"
"v_dot2c_f32_bf16 v186, v124, v189\n"
"v_dot2c_f32_bf16 v187, v124, v124\n"
"v_dot2c_f32_bf16 v186, v125, v189\n"
"v_dot2c_f32_bf16 v187, v125, v125\n"
"v_dot2c_f32_bf16 v186, v126, v189\n"
"v_dot2c_f32_bf16 v187, v126, v126\n"
"v_dot2c_f32_bf16 v186, v127, v189\n"
"v_dot2c_f32_bf16 v187, v127, v127\n"
"s_nop 2\n"
"v_cmp_u_f32 vcc, v187, v187\n"
"s_waitcnt lgkmcnt(4)\n"
"v_mfma_f32_32x32x16_bf16 v[128:143], v[160:163], v[112:115], v[128:143]\n"
"ds_read_b128 v[180:183], v229 offset:7168\n"
"s_add_u32 m0, s83, 24576\n"
"s_add_u32 s87, s81, 24576\n"
"buffer_load_dwordx4 v232, s[48:51], s87 offen offset:0 sc1 lds\n"
"s_waitcnt lgkmcnt(4)\n"
"v_mfma_f32_32x32x16_bf16 v[128:143], v[164:167], v[116:119], v[128:143]\n"
"ds_read_b128 v[160:163], v230 offset:7168\n"
"buffer_load_dwordx4 v232, s[48:51], s87 offen offset:1024 sc1 lds\n"
"s_cbranch_vccnz Lx13_%=\n"
"Lxr13_%=:\n"
"v_add_f32 v184, v184, v186\n"
"v_add_f32 v185, v185, v187\n"
"v_mov_b32 v186, 0\n"
"v_mov_b32 v187, 0\n"
"s_waitcnt lgkmcnt(4)\n"
"v_mfma_f32_32x32x16_bf16 v[128:143], v[168:171], v[120:123], v[128:143]\n"
"ds_read_b128 v[164:167], v231 offset:7168\n"
"buffer_load_dwordx4 v232, s[48:51], s87 offen offset:2048 sc1 lds\n"
"s_waitcnt lgkmcnt(4)\n"
"v_mfma_f32_32x32x16_bf16 v[128:143], v[172:175], v[124:127], v[128:143]\n"
"ds_read_b128 v[168:171], v228 offset:8192\n"
"buffer_load_dwordx4 v232, s[48:51], s87 offen offset:3072 sc1 lds\n"
"s_waitcnt lgkmcnt(3)\n"
"v_mfma_f32_32x32x16_bf16 v[144:159], v[176:179], v[16:19], 0\n"
"ds_read_b128 v[172:175], v229 offset:8192\n"
"v_add_u32 v234, 4096, v246\n"
"v_add_u32 v235, 4096, v247\n"
"v_add_u32 v236, 8192, v246\n"
"v_add_u32 v237, 8192, v247\n"
"v_add_u32 v238, 12288, v246\n"
"v_add_u32 v239, 12288, v247\n"
"v_mfma_f32_32x32x16_bf16 v[144:159], v[180:183], v[20:23], v[144:159]\n"
"ds_read_b128 v[176:179], v230 offset:8192\n"
"ds_read_b128 v[190:193], v248 offset:0\n"
"v_mov_b32 v186, v184\n"
"v_mov_b32 v187, v185\n"
"v_nop\n"
"v_permlane32_swap_b32 v184, v186\n"
"v_permlane32_swap_b32 v185, v187\n"
"s_waitcnt lgkmcnt(4)\n"
"v_mfma_f32_32x32x16_bf16 v[144:159], v[160:163], v[24:27], v[144:159]\n"
"ds_read_b128 v[180:183], v231 offset:8192\n"
"v_add_f32 v184, v184, v186\n"
"v_add_f32 v185, v185, v187\n"
"v_mul_f32 v184, 0x3b124925, v184\n"
"v_mul_f32 v185, 0x3b124925, v185\n"
"v_fma_f32 v185, -v184, v184, v185\n"
"v_mfma_f32_32x32x16_bf16 v[144:159], v[164:167], v[28:31], v[144:159]\n"
"ds_read_b128 v[160:163], v228 offset:9216\n"
"v_max_f32 v185, 0, v185\n"
"v_cvt_pk_bf16_f32 v185, v185, v185\n"
"v_lshlrev_b32 v185, 16, v185\n"
"v_add_f32 v185, 0x3727c5ac, v185\n"
"v_cvt_pk_bf16_f32 v185, v185, v185\n"
"s_waitcnt lgkmcnt(4)\n"
"v_mfma_f32_32x32x16_bf16 v[144:159], v[168:171], v[32:35], v[144:159]\n"
"ds_read_b128 v[164:167], v229 offset:9216\n"
"v_lshlrev_b32 v185, 16, v185\n"
"v_sqrt_f32 v185, v185\n"
"s_nop 0\n"
"v_cvt_pk_bf16_f32 v185, v185, v185\n"
"v_lshlrev_b32 v185, 16, v185\n"
"v_rcp_f32 v185, v185\n"
"v_mfma_f32_32x32x16_bf16 v[144:159], v[172:175], v[36:39], v[144:159]\n"
"ds_read_b128 v[168:171], v230 offset:9216\n"
"v_mul_f32 v188, 0xbfb8aa3b, v185\n"
"ds_read_b128 v[194:197], v248 offset:32\n"
"s_waitcnt lgkmcnt(5)\n"
"v_fmac_f32 v190, v128, v188\n"
"v_fmac_f32 v191, v129, v188\n"
"s_waitcnt lgkmcnt(4)\n"
"v_mfma_f32_32x32x16_bf16 v[144:159], v[176:179], v[40:43], v[144:159]\n"
"ds_read_b128 v[172:175], v231 offset:9216\n"
"v_fmac_f32 v192, v130, v188\n"
"v_fmac_f32 v193, v131, v188\n"
"v_exp_f32 v198, v190\n"
"v_exp_f32 v199, v191\n"
"v_mfma_f32_32x32x16_bf16 v[144:159], v[180:183], v[44:47], v[144:159]\n"
"ds_read_b128 v[176:179], v228 offset:10240\n"
"v_exp_f32 v200, v192\n"
"v_exp_f32 v201, v193\n"
"v_add_f32 v198, 1.0, v198\n"
"v_add_f32 v199, 1.0, v199\n"
"s_waitcnt lgkmcnt(4)\n"
"v_mfma_f32_32x32x16_bf16 v[144:159], v[160:163], v[48:51], v[144:159]\n"
"ds_read_b128 v[180:183], v229 offset:10240\n"
"v_add_f32 v200, 1.0, v200\n"
"v_add_f32 v201, 1.0, v201\n"
"v_rcp_f32 v198, v198\n"
"v_rcp_f32 v199, v199\n"
"v_mfma_f32_32x32x16_bf16 v[144:159], v[164:167], v[52:55], v[144:159]\n"
"ds_read_b128 v[160:163], v230 offset:10240\n"
"v_rcp_f32 v200, v200\n"
"v_rcp_f32 v201, v201\n"
"v_mul_f32 v198, v190, v198\n"
"s_waitcnt lgkmcnt(3)\n"
"v_mfma_f32_32x32x16_bf16 v[144:159], v[168:171], v[56:59], v[144:159]\n"
"ds_read_b128 v[164:167], v231 offset:10240\n"
"v_mul_f32 v199, v191, v199\n"
"v_mul_f32 v200, v192, v200\n"
"v_mul_f32 v201, v193, v201\n"
"v_cvt_pk_bf16_f32 v202, v198, v199\n"
"v_cvt_pk_bf16_f32 v203, v200, v201\n"
"ds_read_b128 v[190:193], v248 offset:64\n"
"v_mfma_f32_32x32x16_bf16 v[144:159], v[172:175], v[60:63], v[144:159]\n"
"ds_read_b128 v[168:171], v228 offset:11264\n"
"v_fmac_f32 v194, v132, v188\n"
"v_fmac_f32 v195, v133, v188\n"
"v_fmac_f32 v196, v134, v188\n"
"v_fmac_f32 v197, v135, v188\n"
"v_exp_f32 v198, v194\n"
"s_waitcnt lgkmcnt(4)\n"
"v_mfma_f32_32x32x16_bf16 v[144:159], v[176:179], v[64:67], v[144:159]\n"
"ds_read_b128 v[172:175], v229 offset:11264\n"
"v_exp_f32 v199, v195\n"
"v_exp_f32 v200, v196\n"
"v_exp_f32 v201, v197\n"
"v_mfma_f32_32x32x16_bf16 v[144:159], v[180:183], v[68:71], v[144:159]\n"
"ds_read_b128 v[176:179], v230 offset:11264\n"
"v_add_f32 v198, 1.0, v198\n"
"v_add_f32 v199, 1.0, v199\n"
"v_add_f32 v200, 1.0, v200\n"
"v_add_f32 v201, 1.0, v201\n"
"v_rcp_f32 v198, v198\n"
"s_waitcnt lgkmcnt(4)\n"
"v_mfma_f32_32x32x16_bf16 v[144:159], v[160:163], v[72:75], v[144:159]\n"
"ds_read_b128 v[180:183], v231 offset:11264\n"
"v_rcp_f32 v199, v199\n"
"v_rcp_f32 v200, v200\n"
"v_mfma_f32_32x32x16_bf16 v[144:159], v[164:167], v[76:79], v[144:159]\n"
"ds_read_b128 v[160:163], v228 offset:12288\n"
"v_rcp_f32 v201, v201\n"
"v_mul_f32 v198, v194, v198\n"
"v_mul_f32 v199, v195, v199\n"
"v_mul_f32 v200, v196, v200\n"
"v_mul_f32 v201, v197, v201\n"
"s_waitcnt lgkmcnt(3)\n"
"v_mfma_f32_32x32x16_bf16 v[144:159], v[168:171], v[80:83], v[144:159]\n"
"ds_read_b128 v[164:167], v229 offset:12288\n"
"v_cvt_pk_bf16_f32 v204, v198, v199\n"
"v_cvt_pk_bf16_f32 v205, v200, v201\n"
"s_add_u32 s88, s82, 0\n"
"buffer_store_dwordx4 v[202:205], v232, s[48:51], s88 offen offset:0 sc1\n"
"ds_read_b128 v[194:197], v248 offset:96\n"
"v_mfma_f32_32x32x16_bf16 v[144:159], v[172:175], v[84:87], v[144:159]\n"
"ds_read_b128 v[168:171], v230 offset:12288\n"
"v_fmac_f32 v190, v136, v188\n"
"v_fmac_f32 v191, v137, v188\n"
"v_fmac_f32 v192, v138, v188\n"
"v_fmac_f32 v193, v139, v188\n"
"v_exp_f32 v198, v190\n"
"s_waitcnt lgkmcnt(4)\n"
"v_mfma_f32_32x32x16_bf16 v[144:159], v[176:179], v[88:91], v[144:159]\n"
"ds_read_b128 v[172:175], v231 offset:12288\n"
"v_exp_f32 v199, v191\n"
"v_exp_f32 v200, v192\n"
"v_exp_f32 v201, v193\n"
"v_mfma_f32_32x32x16_bf16 v[144:159], v[180:183], v[92:95], v[144:159]\n"
"ds_read_b128 v[176:179], v228 offset:13312\n"
"v_add_f32 v198, 1.0, v198\n"
"v_add_f32 v199, 1.0, v199\n"
"v_add_f32 v200, 1.0, v200\n"
"v_add_f32 v201, 1.0, v201\n"
"v_rcp_f32 v198, v198\n"
"s_waitcnt lgkmcnt(4)\n"
"v_mfma_f32_32x32x16_bf16 v[144:159], v[160:163], v[96:99], v[144:159]\n"
"ds_read_b128 v[180:183], v229 offset:13312\n"
"v_rcp_f32 v199, v199\n"
"v_rcp_f32 v200, v200\n"
"v_rcp_f32 v201, v201\n"
"v_mfma_f32_32x32x16_bf16 v[144:159], v[164:167], v[100:103], v[144:159]\n"
"ds_read_b128 v[160:163], v230 offset:13312\n"
"v_mul_f32 v198, v190, v198\n"
"v_mul_f32 v199, v191, v199\n"
"v_mul_f32 v200, v192, v200\n"
"v_mul_f32 v201, v193, v201\n"
"v_cvt_pk_bf16_f32 v202, v198, v199\n"
"s_waitcnt lgkmcnt(3)\n"
"v_mfma_f32_32x32x16_bf16 v[144:159], v[168:171], v[104:107], v[144:159]\n"
"ds_read_b128 v[164:167], v231 offset:13312\n"
"v_cvt_pk_bf16_f32 v203, v200, v201\n"
"ds_read_b128 v[190:193], v248 offset:128\n"
"v_fmac_f32 v194, v140, v188\n"
"v_fmac_f32 v195, v141, v188\n"
"v_fmac_f32 v196, v142, v188\n"
"s_waitcnt vmcnt(1)\n"
"s_barrier\n"
"v_mfma_f32_32x32x16_bf16 v[144:159], v[172:175], v[108:111], v[144:159]\n"
"ds_read_b128 v[168:171], v228 offset:14336\n"
"v_fmac_f32 v197, v143, v188\n"
"v_exp_f32 v198, v194\n"
"v_exp_f32 v199, v195\n"
"v_exp_f32 v200, v196\n"
"s_waitcnt lgkmcnt(4)\n"
"v_mfma_f32_32x32x16_bf16 v[144:159], v[176:179], v[112:115], v[144:159]\n"
"ds_read_b128 v[172:175], v229 offset:14336\n"
"v_exp_f32 v201, v197\n"
"v_add_f32 v198, 1.0, v198\n"
"v_add_f32 v199, 1.0, v199\n"
"v_add_f32 v200, 1.0, v200\n"
"v_mfma_f32_32x32x16_bf16 v[144:159], v[180:183], v[116:119], v[144:159]\n"
"ds_read_b128 v[176:179], v230 offset:14336\n"
"v_add_f32 v201, 1.0, v201\n"
"v_rcp_f32 v198, v198\n"
"v_rcp_f32 v199, v199\n"
"v_rcp_f32 v200, v200\n"
"s_waitcnt lgkmcnt(4)\n"
"v_mfma_f32_32x32x16_bf16 v[144:159], v[160:163], v[120:123], v[144:159]\n"
"ds_read_b128 v[180:183], v231 offset:14336\n"
"v_rcp_f32 v201, v201\n"
"v_mul_f32 v198, v194, v198\n"
"v_mul_f32 v199, v195, v199\n"
"v_mfma_f32_32x32x16_bf16 v[144:159], v[164:167], v[124:127], v[144:159]\n"
"ds_read_b128 v[160:163], v228 offset:15360\n"
"v_mul_f32 v200, v196, v200\n"
"v_mul_f32 v201, v197, v201\n"
"v_cvt_pk_bf16_f32 v204, v198, v199\n"
"v_cvt_pk_bf16_f32 v205, v200, v201\n"
"buffer_store_dwordx4 v[202:205], v232, s[48:51], s88 offen offset:1024 sc1\n"
"s_waitcnt lgkmcnt(3)\n"
"v_mfma_f32_32x32x16_bf16 v[128:143], v[168:171], v[16:19], 0\n"
"ds_read_b128 v[164:167], v229 offset:15360\n"
"v_mfma_f32_32x32x16_bf16 v[128:143], v[172:175], v[20:23], v[128:143]\n"
"ds_read_b128 v[168:171], v230 offset:15360\n"
"ds_read_b128 v[194:197], v248 offset:160\n"
"v_fmac_f32 v190, v144, v188\n"
"v_fmac_f32 v191, v145, v188\n"
"v_fmac_f32 v192, v146, v188\n"
"v_fmac_f32 v193, v147, v188\n"
"s_waitcnt lgkmcnt(4)\n"
"v_mfma_f32_32x32x16_bf16 v[128:143], v[176:179], v[24:27], v[128:143]\n"
"ds_read_b128 v[172:175], v231 offset:15360\n"
"v_exp_f32 v198, v190\n"
"v_exp_f32 v199, v191\n"
"v_exp_f32 v200, v192\n"
"v_mfma_f32_32x32x16_bf16 v[128:143], v[180:183], v[28:31], v[128:143]\n"
"ds_read_b128 v[176:179], v228 offset:16384\n"
"v_exp_f32 v201, v193\n"
"v_add_f32 v198, 1.0, v198\n"
"v_add_f32 v199, 1.0, v199\n"
"s_waitcnt lgkmcnt(4)\n"
"v_mfma_f32_32x32x16_bf16 v[128:143], v[160:163], v[32:35], v[128:143]\n"
"ds_read_b128 v[180:183], v229 offset:16384\n"
"v_add_f32 v200, 1.0, v200\n"
"v_add_f32 v201, 1.0, v201\n"
"v_rcp_f32 v198, v198\n"
"v_rcp_f32 v199, v199\n"
"v_mfma_f32_32x32x16_bf16 v[128:143], v[164:167], v[36:39], v[128:143]\n"
"ds_read_b128 v[160:163], v230 offset:16384\n"
"v_rcp_f32 v200, v200\n"
"v_rcp_f32 v201, v201\n"
"s_waitcnt lgkmcnt(3)\n"
"v_mfma_f32_32x32x16_bf16 v[128:143], v[168:171], v[40:43], v[128:143]\n"
"ds_read_b128 v[164:167], v231 offset:16384\n"
"v_mul_f32 v198, v190, v198\n"
"v_mul_f32 v199, v191, v199\n"
"v_mul_f32 v200, v192, v200\n"
"v_mul_f32 v201, v193, v201\n"
"v_mfma_f32_32x32x16_bf16 v[128:143], v[172:175], v[44:47], v[128:143]\n"
"ds_read_b128 v[168:171], v228 offset:17408\n"
"v_cvt_pk_bf16_f32 v202, v198, v199\n"
"v_cvt_pk_bf16_f32 v203, v200, v201\n"
"ds_read_b128 v[190:193], v248 offset:192\n"
"v_fmac_f32 v194, v148, v188\n"
"s_waitcnt lgkmcnt(4)\n"
"v_mfma_f32_32x32x16_bf16 v[128:143], v[176:179], v[48:51], v[128:143]\n"
"ds_read_b128 v[172:175], v229 offset:17408\n"
"v_fmac_f32 v195, v149, v188\n"
"v_fmac_f32 v196, v150, v188\n"
"v_fmac_f32 v197, v151, v188\n"
"v_exp_f32 v198, v194\n"
"v_mfma_f32_32x32x16_bf16 v[128:143], v[180:183], v[52:55], v[128:143]\n"
"ds_read_b128 v[176:179], v230 offset:17408\n"
"v_exp_f32 v199, v195\n"
"v_exp_f32 v200, v196\n"
"v_exp_f32 v201, v197\n"
"s_waitcnt lgkmcnt(4)\n"
"v_mfma_f32_32x32x16_bf16 v[128:143], v[160:163], v[56:59], v[128:143]\n"
"ds_read_b128 v[180:183], v231 offset:17408\n"
"v_add_f32 v198, 1.0, v198\n"
"v_add_f32 v199, 1.0, v199\n"
"v_add_f32 v200, 1.0, v200\n"
"v_add_f32 v201, 1.0, v201\n"
"v_mfma_f32_32x32x16_bf16 v[128:143], v[164:167], v[60:63], v[128:143]\n"
"ds_read_b128 v[160:163], v228 offset:18432\n"
"v_rcp_f32 v198, v198\n"
"v_rcp_f32 v199, v199\n"
"v_rcp_f32 v200, v200\n"
"s_waitcnt lgkmcnt(3)\n"
"v_mfma_f32_32x32x16_bf16 v[128:143], v[168:171], v[64:67], v[128:143]\n"
"ds_read_b128 v[164:167], v229 offset:18432\n"
"v_rcp_f32 v201, v201\n"
"v_mul_f32 v198, v194, v198\n"
"v_mfma_f32_32x32x16_bf16 v[128:143], v[172:175], v[68:71], v[128:143]\n"
"ds_read_b128 v[168:171], v230 offset:18432\n"
"v_mul_f32 v199, v195, v199\n"
"v_mul_f32 v200, v196, v200\n"
"v_mul_f32 v201, v197, v201\n"
"v_cvt_pk_bf16_f32 v204, v198, v199\n"
"v_cvt_pk_bf16_f32 v205, v200, v201\n"
"s_waitcnt lgkmcnt(3)\n"
"v_mfma_f32_32x32x16_bf16 v[128:143], v[176:179], v[72:75], v[128:143]\n"
"ds_read_b128 v[172:175], v231 offset:18432\n"
"s_add_u32 s88, s82, 4096\n"
"buffer_store_dwordx4 v[202:205], v232, s[48:51], s88 offen offset:0 sc1\n"
"ds_read_b128 v[194:197], v248 offset:224\n"
"v_fmac_f32 v190, v152, v188\n"
"v_mfma_f32_32x32x16_bf16 v[128:143], v[180:183], v[76:79], v[128:143]\n"
"ds_read_b128 v[176:179], v228 offset:19456\n"
"v_fmac_f32 v191, v153, v188\n"
"v_fmac_f32 v192, v154, v188\n"
"v_fmac_f32 v193, v155, v188\n"
"v_exp_f32 v198, v190\n"
"s_waitcnt lgkmcnt(4)\n"
"v_mfma_f32_32x32x16_bf16 v[128:143], v[160:163], v[80:83], v[128:143]\n"
"ds_read_b128 v[180:183], v229 offset:19456\n"
"v_exp_f32 v199, v191\n"
"v_exp_f32 v200, v192\n"
"v_exp_f32 v201, v193\n"
"v_mfma_f32_32x32x16_bf16 v[128:143], v[164:167], v[84:87], v[128:143]\n"
"ds_read_b128 v[160:163], v230 offset:19456\n"
"v_add_f32 v198, 1.0, v198\n"
"v_add_f32 v199, 1.0, v199\n"
"v_add_f32 v200, 1.0, v200\n"
"v_add_f32 v201, 1.0, v201\n"
"s_waitcnt lgkmcnt(4)\n"
"v_mfma_f32_32x32x16_bf16 v[128:143], v[168:171], v[88:91], v[128:143]\n"
"ds_read_b128 v[164:167], v231 offset:19456\n"
"v_rcp_f32 v198, v198\n"
"v_rcp_f32 v199, v199\n"
"v_mfma_f32_32x32x16_bf16 v[128:143], v[172:175], v[92:95], v[128:143]\n"
"ds_read_b128 v[168:171], v228 offset:20480\n"
"v_rcp_f32 v200, v200\n"
"v_rcp_f32 v201, v201\n"
"v_mul_f32 v198, v190, v198\n"
"s_waitcnt lgkmcnt(3)\n"
"v_mfma_f32_32x32x16_bf16 v[128:143], v[176:179], v[96:99], v[128:143]\n"
"ds_read_b128 v[172:175], v229 offset:20480\n"
"v_mul_f32 v199, v191, v199\n"
"v_mul_f32 v200, v192, v200\n"
"v_mul_f32 v201, v193, v201\n"
"v_cvt_pk_bf16_f32 v202, v198, v199\n"
"v_cvt_pk_bf16_f32 v203, v200, v201\n"
"v_mfma_f32_32x32x16_bf16 v[128:143], v[180:183], v[100:103], v[128:143]\n"
"ds_read_b128 v[176:179], v230 offset:20480\n"
"ds_read_b128 v[190:193], v248 offset:256\n"
"v_fmac_f32 v194, v156, v188\n"
"v_fmac_f32 v195, v157, v188\n"
"v_fmac_f32 v196, v158, v188\n"
"s_waitcnt lgkmcnt(4)\n"
"v_mfma_f32_32x32x16_bf16 v[128:143], v[160:163], v[104:107], v[128:143]\n"
"ds_read_b128 v[180:183], v231 offset:20480\n"
"v_fmac_f32 v197, v159, v188\n"
"v_exp_f32 v198, v194\n"
"v_exp_f32 v199, v195\n"
"v_mfma_f32_32x32x16_bf16 v[128:143], v[164:167], v[108:111], v[128:143]\n"
"ds_read_b128 v[160:163], v228 offset:21504\n"
"v_exp_f32 v200, v196\n"
"v_exp_f32 v201, v197\n"
"v_add_f32 v198, 1.0, v198\n"
"s_waitcnt lgkmcnt(4)\n"
"v_mfma_f32_32x32x16_bf16 v[128:143], v[168:171], v[112:115], v[128:143]\n"
"ds_read_b128 v[164:167], v229 offset:21504\n"
"v_add_f32 v199, 1.0, v199\n"
"v_add_f32 v200, 1.0, v200\n"
"v_add_f32 v201, 1.0, v201\n"
"v_rcp_f32 v198, v198\n"
"v_mfma_f32_32x32x16_bf16 v[128:143], v[172:175], v[116:119], v[128:143]\n"
"ds_read_b128 v[168:171], v230 offset:21504\n"
"v_rcp_f32 v199, v199\n"
"v_rcp_f32 v200, v200\n"
"s_waitcnt lgkmcnt(3)\n"
"v_mfma_f32_32x32x16_bf16 v[128:143], v[176:179], v[120:123], v[128:143]\n"
"ds_read_b128 v[172:175], v231 offset:21504\n"
"v_rcp_f32 v201, v201\n"
"v_mul_f32 v198, v194, v198\n"
"v_mul_f32 v199, v195, v199\n"
"v_mul_f32 v200, v196, v200\n"
"v_mfma_f32_32x32x16_bf16 v[128:143], v[180:183], v[124:127], v[128:143]\n"
"ds_read_b128 v[176:179], v228 offset:22528\n"
"v_mul_f32 v201, v197, v201\n"
"v_cvt_pk_bf16_f32 v204, v198, v199\n"
"v_cvt_pk_bf16_f32 v205, v200, v201\n"
"buffer_store_dwordx4 v[202:205], v232, s[48:51], s88 offen offset:1024 sc1\n"
"s_waitcnt lgkmcnt(3)\n"
"v_mfma_f32_32x32x16_bf16 v[144:159], v[160:163], v[16:19], 0\n"
"ds_read_b128 v[180:183], v229 offset:22528\n"
"v_mfma_f32_32x32x16_bf16 v[144:159], v[164:167], v[20:23], v[144:159]\n"
"ds_read_b128 v[160:163], v230 offset:22528\n"
"ds_read_b128 v[194:197], v248 offset:288\n"
"s_nop 0\n"
"v_fmac_f32 v190, v128, v188\n"
"v_fmac_f32 v191, v129, v188\n"
"v_fmac_f32 v192, v130, v188\n"
"v_fmac_f32 v193, v131, v188\n"
"s_waitcnt lgkmcnt(4)\n"
"v_mfma_f32_32x32x16_bf16 v[144:159], v[168:171], v[24:27], v[144:159]\n"
"ds_read_b128 v[164:167], v231 offset:22528\n"
"v_exp_f32 v198, v190\n"
"v_exp_f32 v199, v191\n"
"v_exp_f32 v200, v192\n"
"v_mfma_f32_32x32x16_bf16 v[144:159], v[172:175], v[28:31], v[144:159]\n"
"ds_read_b128 v[168:171], v228 offset:23552\n"
"v_exp_f32 v201, v193\n"
"v_add_f32 v198, 1.0, v198\n"
"v_add_f32 v199, 1.0, v199\n"
"buffer_load_dwordx4 v[208:211], v246, s[48:51], s72 offen sc1\n"
"s_waitcnt lgkmcnt(4)\n"
"v_mfma_f32_32x32x16_bf16 v[144:159], v[176:179], v[32:35], v[144:159]\n"
"ds_read_b128 v[172:175], v229 offset:23552\n"
"v_add_f32 v200, 1.0, v200\n"
"v_add_f32 v201, 1.0, v201\n"
"v_rcp_f32 v198, v198\n"
"v_rcp_f32 v199, v199\n"
"v_mfma_f32_32x32x16_bf16 v[144:159], v[180:183], v[36:39], v[144:159]\n"
"ds_read_b128 v[176:179], v230 offset:23552\n"
"v_rcp_f32 v200, v200\n"
"v_rcp_f32 v201, v201\n"
"s_waitcnt lgkmcnt(3)\n"
"v_mfma_f32_32x32x16_bf16 v[144:159], v[160:163], v[40:43], v[144:159]\n"
"ds_read_b128 v[180:183], v231 offset:23552\n"
"v_mul_f32 v198, v190, v198\n"
"v_mul_f32 v199, v191, v199\n"
"v_mul_f32 v200, v192, v200\n"
"v_mul_f32 v201, v193, v201\n"
"buffer_load_dwordx4 v[212:215], v247, s[48:51], s72 offen sc1\n"
"v_mfma_f32_32x32x16_bf16 v[144:159], v[164:167], v[44:47], v[144:159]\n"
"ds_read_b128 v[160:163], v228 offset:24576\n"
"v_cvt_pk_bf16_f32 v202, v198, v199\n"
"v_cvt_pk_bf16_f32 v203, v200, v201\n"
"ds_read_b128 v[190:193], v248 offset:320\n"
"v_fmac_f32 v194, v132, v188\n"
"s_waitcnt lgkmcnt(4)\n"
"v_mfma_f32_32x32x16_bf16 v[144:159], v[168:171], v[48:51], v[144:159]\n"
"ds_read_b128 v[164:167], v229 offset:24576\n"
"v_fmac_f32 v195, v133, v188\n"
"v_fmac_f32 v196, v134, v188\n"
"v_fmac_f32 v197, v135, v188\n"
"v_exp_f32 v198, v194\n"
"v_mfma_f32_32x32x16_bf16 v[144:159], v[172:175], v[52:55], v[144:159]\n"
"ds_read_b128 v[168:171], v230 offset:24576\n"
"v_exp_f32 v199, v195\n"
"v_exp_f32 v200, v196\n"
"v_exp_f32 v201, v197\n"
"buffer_load_dwordx4 v[216:219], v246, s[48:51], s73 offen sc1\n"
"s_waitcnt lgkmcnt(4)\n"
"v_mfma_f32_32x32x16_bf16 v[144:159], v[176:179], v[56:59], v[144:159]\n"
"ds_read_b128 v[172:175], v231 offset:24576\n"
"v_add_f32 v198, 1.0, v198\n"
"v_add_f32 v199, 1.0, v199\n"
"v_add_f32 v200, 1.0, v200\n"
"v_add_f32 v201, 1.0, v201\n"
"v_mfma_f32_32x32x16_bf16 v[144:159], v[180:183], v[60:63], v[144:159]\n"
"ds_read_b128 v[176:179], v228 offset:25600\n"
"v_rcp_f32 v198, v198\n"
"v_rcp_f32 v199, v199\n"
"v_rcp_f32 v200, v200\n"
"s_waitcnt lgkmcnt(3)\n"
"v_mfma_f32_32x32x16_bf16 v[144:159], v[160:163], v[64:67], v[144:159]\n"
"ds_read_b128 v[180:183], v229 offset:25600\n"
"v_rcp_f32 v201, v201\n"
"v_mul_f32 v198, v194, v198\n"
"buffer_load_dwordx4 v[220:223], v247, s[48:51], s73 offen sc1\n"
"v_mfma_f32_32x32x16_bf16 v[144:159], v[164:167], v[68:71], v[144:159]\n"
"ds_read_b128 v[160:163], v230 offset:25600\n"
"v_mul_f32 v199, v195, v199\n"
"v_mul_f32 v200, v196, v200\n"
"v_mul_f32 v201, v197, v201\n"
"v_cvt_pk_bf16_f32 v204, v198, v199\n"
"v_cvt_pk_bf16_f32 v205, v200, v201\n"
"s_waitcnt lgkmcnt(3)\n"
"v_mfma_f32_32x32x16_bf16 v[144:159], v[168:171], v[72:75], v[144:159]\n"
"ds_read_b128 v[164:167], v231 offset:25600\n"
"s_add_u32 s88, s82, 8192\n"
"buffer_store_dwordx4 v[202:205], v232, s[48:51], s88 offen offset:0 sc1\n"
"ds_read_b128 v[194:197], v248 offset:352\n"
"v_fmac_f32 v190, v136, v188\n"
"v_mfma_f32_32x32x16_bf16 v[144:159], v[172:175], v[76:79], v[144:159]\n"
"ds_read_b128 v[168:171], v228 offset:26624\n"
"v_fmac_f32 v191, v137, v188\n"
"v_fmac_f32 v192, v138, v188\n"
"v_fmac_f32 v193, v139, v188\n"
"v_exp_f32 v198, v190\n"
"buffer_load_dwordx4 v[0:3], v246, s[48:51], s74 offen sc1\n"
"s_waitcnt lgkmcnt(4)\n"
"v_mfma_f32_32x32x16_bf16 v[144:159], v[176:179], v[80:83], v[144:159]\n"
"ds_read_b128 v[172:175], v229 offset:26624\n"
"v_exp_f32 v199, v191\n"
"v_exp_f32 v200, v192\n"
"v_exp_f32 v201, v193\n"
"v_mfma_f32_32x32x16_bf16 v[144:159], v[180:183], v[84:87], v[144:159]\n"
"ds_read_b128 v[176:179], v230 offset:26624\n"
"v_add_f32 v198, 1.0, v198\n"
"v_add_f32 v199, 1.0, v199\n"
"v_add_f32 v200, 1.0, v200\n"
"v_add_f32 v201, 1.0, v201\n"
"s_waitcnt lgkmcnt(4)\n"
"v_mfma_f32_32x32x16_bf16 v[144:159], v[160:163], v[88:91], v[144:159]\n"
"ds_read_b128 v[180:183], v231 offset:26624\n"
"v_rcp_f32 v198, v198\n"
"v_rcp_f32 v199, v199\n"
"buffer_load_dwordx4 v[4:7], v247, s[48:51], s74 offen sc1\n"
"v_mfma_f32_32x32x16_bf16 v[144:159], v[164:167], v[92:95], v[144:159]\n"
"ds_read_b128 v[160:163], v228 offset:27648\n"
"v_rcp_f32 v200, v200\n"
"v_rcp_f32 v201, v201\n"
"v_mul_f32 v198, v190, v198\n"
"s_waitcnt lgkmcnt(3)\n"
"v_mfma_f32_32x32x16_bf16 v[144:159], v[168:171], v[96:99], v[144:159]\n"
"ds_read_b128 v[164:167], v229 offset:27648\n"
"v_mul_f32 v199, v191, v199\n"
"v_mul_f32 v200, v192, v200\n"
"v_mul_f32 v201, v193, v201\n"
"v_cvt_pk_bf16_f32 v202, v198, v199\n"
"v_cvt_pk_bf16_f32 v203, v200, v201\n"
"v_mfma_f32_32x32x16_bf16 v[144:159], v[172:175], v[100:103], v[144:159]\n"
"ds_read_b128 v[168:171], v230 offset:27648\n"
"ds_read_b128 v[190:193], v248 offset:384\n"
"v_fmac_f32 v194, v140, v188\n"
"v_fmac_f32 v195, v141, v188\n"
"v_fmac_f32 v196, v142, v188\n"
"buffer_load_dwordx4 v[8:11], v246, s[48:51], s75 offen sc1\n"
"s_waitcnt lgkmcnt(4)\n"
"v_mfma_f32_32x32x16_bf16 v[144:159], v[176:179], v[104:107], v[144:159]\n"
"ds_read_b128 v[172:175], v231 offset:27648\n"
"v_fmac_f32 v197, v143, v188\n"
"v_exp_f32 v198, v194\n"
"v_exp_f32 v199, v195\n"
"v_mfma_f32_32x32x16_bf16 v[144:159], v[180:183], v[108:111], v[144:159]\n"
"v_exp_f32 v200, v196\n"
"v_exp_f32 v201, v197\n"
"v_add_f32 v198, 1.0, v198\n"
"s_waitcnt lgkmcnt(3)\n"
"v_mfma_f32_32x32x16_bf16 v[144:159], v[160:163], v[112:115], v[144:159]\n"
"v_add_f32 v199, 1.0, v199\n"
"v_add_f32 v200, 1.0, v200\n"
"v_add_f32 v201, 1.0, v201\n"
"v_rcp_f32 v198, v198\n"
"buffer_load_dwordx4 v[12:15], v247, s[48:51], s75 offen sc1\n"
"v_mfma_f32_32x32x16_bf16 v[144:159], v[164:167], v[116:119], v[144:159]\n"
"v_rcp_f32 v199, v199\n"
"v_rcp_f32 v200, v200\n"
"s_waitcnt lgkmcnt(0)\n"
"v_mfma_f32_32x32x16_bf16 v[144:159], v[168:171], v[120:123], v[144:159]\n"
"v_rcp_f32 v201, v201\n"
"v_mul_f32 v198, v194, v198\n"
"v_mul_f32 v199, v195, v199\n"
"v_mul_f32 v200, v196, v200\n"
"v_mfma_f32_32x32x16_bf16 v[144:159], v[172:175], v[124:127], v[144:159]\n"
"v_mul_f32 v201, v197, v201\n"
"v_cvt_pk_bf16_f32 v204, v198, v199\n"
"v_cvt_pk_bf16_f32 v205, v200, v201\n"
"buffer_store_dwordx4 v[202:205], v232, s[48:51], s88 offen offset:1024 sc1\n"
"s_waitcnt vmcnt(8)\n"
"v_max3_u32 v186, v208, v210, v212\n"
"v_max_u32 v186, v186, v214\n"
"v_cmp_eq_u32 vcc, -1, v186\n"
"s_nop 4\n"
"s_cbranch_vccnz Lh0_%=\n"
"Lhr0_%=:\n"
"v_mfma_f32_32x32x16_bf16 v[16:31], a[0:3], v[208:211], 0\n"
"buffer_load_dwordx4 v[80:83], v246, s[48:51], s76 offen sc1\n"
"v_mfma_f32_32x32x16_bf16 v[32:47], a[0:3], v[212:215], 0\n"
"buffer_load_dwordx4 v[84:87], v247, s[48:51], s76 offen sc1\n"
"v_mfma_f32_32x32x16_bf16 v[48:63], a[4:7], v[208:211], 0\n"
"buffer_load_dwordx4 v[88:91], v246, s[48:51], s77 offen sc1\n"
"ds_read_b128 v[194:197], v248 offset:416\n"
"v_fmac_f32 v190, v144, v188\n"
"v_fmac_f32 v191, v145, v188\n"
"v_fmac_f32 v192, v146, v188\n"
"v_fmac_f32 v193, v147, v188\n"
"s_waitcnt vmcnt(9)\n"
"v_max3_u32 v186, v216, v218, v220\n"
"v_max_u32 v186, v186, v222\n"
"v_cmp_eq_u32 vcc, -1, v186\n"
"v_mfma_f32_32x32x16_bf16 v[64:79], a[4:7], v[212:215], 0\n"
"buffer_load_dwordx4 v[92:95], v247, s[48:51], s77 offen sc1\n"
"v_exp_f32 v198, v190\n"
"v_exp_f32 v199, v191\n"
"v_exp_f32 v200, v192\n"
"s_cbranch_vccnz Lh1_%=\n"
"Lhr1_%=:\n"
"v_mfma_f32_32x32x16_bf16 v[16:31], a[8:11], v[216:219], v[16:31]\n"
"buffer_load_dwordx4 v[96:99], v246, s[48:51], s78 offen sc1\n"
"v_exp_f32 v201, v193\n"
"v_add_f32 v198, 1.0, v198\n"
"v_add_f32 v199, 1.0, v199\n"
"v_mfma_f32_32x32x16_bf16 v[32:47], a[8:11], v[220:223], v[32:47]\n"
"buffer_load_dwordx4 v[100:103], v247, s[48:51], s78 offen sc1\n"
"v_add_f32 v200, 1.0, v200\n"
"v_add_f32 v201, 1.0, v201\n"
"v_rcp_f32 v198, v198\n"
"v_rcp_f32 v199, v199\n"
"v_mfma_f32_32x32x16_bf16 v[48:63], a[12:15], v[216:219], v[48:63]\n"
"buffer_load_dwordx4 v[104:107], v234, s[48:51], s72 offen sc1\n"
"v_rcp_f32 v200, v200\n"
"v_rcp_f32 v201, v201\n"
"s_waitcnt vmcnt(10)\n"
"v_max3_u32 v186, v0, v2, v4\n"
"v_max_u32 v186, v186, v6\n"
"v_cmp_eq_u32 vcc, -1, v186\n"
"v_mfma_f32_32x32x16_bf16 v[64:79], a[12:15], v[220:223], v[64:79]\n"
"buffer_load_dwordx4 v[108:111], v235, s[48:51], s72 offen sc1\n"
"v_mul_f32 v198, v190, v198\n"
"v_mul_f32 v199, v191, v199\n"
"v_mul_f32 v200, v192, v200\n"
"v_mul_f32 v201, v193, v201\n"
"v_cvt_pk_bf16_f32 v202, v198, v199\n"
"s_cbranch_vccnz Lh2_%=\n"
"Lhr2_%=:\n"
"v_mfma_f32_32x32x16_bf16 v[16:31], a[16:19], v[0:3], v[16:31]\n"
"buffer_load_dwordx4 v[112:115], v234, s[48:51], s73 offen sc1\n"
"v_cvt_pk_bf16_f32 v203, v200, v201\n"
"ds_read_b128 v[190:193], v248 offset:448\n"
"s_waitcnt lgkmcnt(1)\n"
"v_fmac_f32 v194, v148, v188\n"
"v_fmac_f32 v195, v149, v188\n"
"v_mfma_f32_32x32x16_bf16 v[32:47], a[16:19], v[4:7], v[32:47]\n"
"buffer_load_dwordx4 v[116:119], v235, s[48:51], s73 offen sc1\n"
"v_fmac_f32 v196, v150, v188\n"
"v_fmac_f32 v197, v151, v188\n"
"v_exp_f32 v198, v194\n"
"v_exp_f32 v199, v195\n"
"v_mfma_f32_32x32x16_bf16 v[48:63], a[20:23], v[0:3], v[48:63]\n"
"buffer_load_dwordx4 v[120:123], v234, s[48:51], s74 offen sc1\n"
"v_exp_f32 v200, v196\n"
"v_exp_f32 v201, v197\n"
"s_waitcnt vmcnt(12)\n"
"v_max3_u32 v186, v8, v10, v12\n"
"v_max_u32 v186, v186, v14\n"
"v_cmp_eq_u32 vcc, -1, v186\n"
"v_mfma_f32_32x32x16_bf16 v[64:79], a[20:23], v[4:7], v[64:79]\n"
"buffer_load_dwordx4 v[124:127], v235, s[48:51], s74 offen sc1\n"
"v_add_f32 v198, 1.0, v198\n"
"v_add_f32 v199, 1.0, v199\n"
"v_add_f32 v200, 1.0, v200\n"
"v_add_f32 v201, 1.0, v201\n"
"v_rcp_f32 v198, v198\n"
"s_cbranch_vccnz Lh3_%=\n"
"Lhr3_%=:\n"
"v_mfma_f32_32x32x16_bf16 v[16:31], a[24:27], v[8:11], v[16:31]\n"
"buffer_load_dwordx4 v[160:163], v234, s[48:51], s75 offen sc1\n"
"v_rcp_f32 v199, v199\n"
"v_rcp_f32 v200, v200\n"
"v_mfma_f32_32x32x16_bf16 v[32:47], a[24:27], v[12:15], v[32:47]\n"
"buffer_load_dwordx4 v[164:167], v235, s[48:51], s75 offen sc1\n"
"v_rcp_f32 v201, v201\n"
"v_mul_f32 v198, v194, v198\n"
"v_mul_f32 v199, v195, v199\n"
"v_mul_f32 v200, v196, v200\n"
"v_mfma_f32_32x32x16_bf16 v[48:63], a[28:31], v[8:11], v[48:63]\n"
"buffer_load_dwordx4 v[168:171], v234, s[48:51], s76 offen sc1\n"
"v_mul_f32 v201, v197, v201\n"
"v_cvt_pk_bf16_f32 v204, v198, v199\n"
"v_cvt_pk_bf16_f32 v205, v200, v201\n"
"s_add_u32 s88, s82, 12288\n"
"s_waitcnt vmcnt(13)\n"
"v_max3_u32 v186, v80, v82, v84\n"
"v_max_u32 v186, v186, v86\n"
"v_cmp_eq_u32 vcc, -1, v186\n"
"v_mfma_f32_32x32x16_bf16 v[64:79], a[28:31], v[12:15], v[64:79]\n"
"buffer_load_dwordx4 v[172:175], v235, s[48:51], s76 offen sc1\n"
"buffer_store_dwordx4 v[202:205], v232, s[48:51], s88 offen offset:0 sc1\n"
"ds_read_b128 v[194:197], v248 offset:480\n"
"s_waitcnt lgkmcnt(1)\n"
"v_fmac_f32 v190, v152, v188\n"
"v_fmac_f32 v191, v153, v188\n"
"v_fmac_f32 v192, v154, v188\n"
"s_cbranch_vccnz Lh4_%=\n"
"Lhr4_%=:\n"
"v_mfma_f32_32x32x16_bf16 v[16:31], a[32:35], v[80:83], v[16:31]\n"
"buffer_load_dwordx4 v[176:179], v234, s[48:51], s77 offen sc1\n"
"v_fmac_f32 v193, v155, v188\n"
"v_exp_f32 v198, v190\n"
"v_exp_f32 v199, v191\n"
"v_mfma_f32_32x32x16_bf16 v[32:47], a[32:35], v[84:87], v[32:47]\n"
"buffer_load_dwordx4 v[180:183], v235, s[48:51], s77 offen sc1\n"
"v_exp_f32 v200, v192\n"
"v_exp_f32 v201, v193\n"
"v_add_f32 v198, 1.0, v198\n"
"v_mfma_f32_32x32x16_bf16 v[48:63], a[36:39], v[80:83], v[48:63]\n"
"buffer_load_dwordx4 v[128:131], v234, s[48:51], s78 offen sc1\n"
"v_add_f32 v199, 1.0, v199\n"
"v_add_f32 v200, 1.0, v200\n"
"v_add_f32 v201, 1.0, v201\n"
"v_rcp_f32 v198, v198\n"
"s_waitcnt vmcnt(16)\n"
"v_max3_u32 v186, v88, v90, v92\n"
"v_max_u32 v186, v186, v94\n"
"v_cmp_eq_u32 vcc, -1, v186\n"
"v_mfma_f32_32x32x16_bf16 v[64:79], a[36:39], v[84:87], v[64:79]\n"
"buffer_load_dwordx4 v[132:135], v235, s[48:51], s78 offen sc1\n"
"v_rcp_f32 v199, v199\n"
"v_rcp_f32 v200, v200\n"
"v_rcp_f32 v201, v201\n"
"s_cbranch_vccnz Lh5_%=\n"
"Lhr5_%=:\n"
"v_mfma_f32_32x32x16_bf16 v[16:31], a[40:43], v[88:91], v[16:31]\n"
"buffer_load_dwordx4 v[136:139], v236, s[48:51], s72 offen sc1\n"
"v_mul_f32 v198, v190, v198\n"
"v_mul_f32 v199, v191, v199\n"
"v_mul_f32 v200, v192, v200\n"
"v_mul_f32 v201, v193, v201\n"
"v_mfma_f32_32x32x16_bf16 v[32:47], a[40:43], v[92:95], v[32:47]\n"
"buffer_load_dwordx4 v[140:143], v237, s[48:51], s72 offen sc1\n"
"v_cvt_pk_bf16_f32 v202, v198, v199\n"
"v_cvt_pk_bf16_f32 v203, v200, v201\n"
"s_waitcnt lgkmcnt(0)\n"
"v_fmac_f32 v194, v156, v188\n"
"v_fmac_f32 v195, v157, v188\n"
"v_mfma_f32_32x32x16_bf16 v[48:63], a[44:47], v[88:91], v[48:63]\n"
"buffer_load_dwordx4 v[208:211], v236, s[48:51], s73 offen sc1\n"
"v_fmac_f32 v196, v158, v188\n"
"v_fmac_f32 v197, v159, v188\n"
"v_exp_f32 v198, v194\n"
"v_exp_f32 v199, v195\n"
"s_waitcnt vmcnt(18)\n"
"v_max3_u32 v186, v96, v98, v100\n"
"v_max_u32 v186, v186, v102\n"
"v_cmp_eq_u32 vcc, -1, v186\n"
"v_mfma_f32_32x32x16_bf16 v[64:79], a[44:47], v[92:95], v[64:79]\n"
"buffer_load_dwordx4 v[212:215], v237, s[48:51], s73 offen sc1\n"
"v_exp_f32 v200, v196\n"
"v_exp_f32 v201, v197\n"
"s_nop 0\n"
"s_cbranch_vccnz Lh6_%=\n"
"Lhr6_%=:\n"
"v_mfma_f32_32x32x16_bf16 v[16:31], a[48:51], v[96:99], v[16:31]\n"
"buffer_load_dwordx4 v[216:219], v236, s[48:51], s74 offen sc1\n"
"v_add_f32 v198, 1.0, v198\n"
"v_add_f32 v199, 1.0, v199\n"
"v_add_f32 v200, 1.0, v200\n"
"v_add_f32 v201, 1.0, v201\n"
"v_rcp_f32 v198, v198\n"
"v_mfma_f32_32x32x16_bf16 v[32:47], a[48:51], v[100:103], v[32:47]\n"
"buffer_load_dwordx4 v[220:223], v237, s[48:51], s74 offen sc1\n"
"v_rcp_f32 v199, v199\n"
"v_rcp_f32 v200, v200\n"
"v_mfma_f32_32x32x16_bf16 v[48:63], a[52:55], v[96:99], v[48:63]\n"
"v_rcp_f32 v201, v201\n"
"v_mul_f32 v198, v194, v198\n"
"v_mul_f32 v199, v195, v199\n"
"v_mul_f32 v200, v196, v200\n"
"s_waitcnt vmcnt(19)\n"
"v_max3_u32 v186, v104, v106, v108\n"
"v_max_u32 v186, v186, v110\n"
"v_cmp_eq_u32 vcc, -1, v186\n"
"v_mfma_f32_32x32x16_bf16 v[64:79], a[52:55], v[100:103], v[64:79]\n"
"v_mul_f32 v201, v197, v201\n"
"v_cvt_pk_bf16_f32 v204, v198, v199\n"
"v_cvt_pk_bf16_f32 v205, v200, v201\n"
"buffer_store_dwordx4 v[202:205], v232, s[48:51], s88 offen offset:1024 sc1\n"
"s_cbranch_vccnz Lh7_%=\n"
"Lhr7_%=:\n"
"v_mfma_f32_32x32x16_bf16 v[16:31], a[56:59], v[104:107], v[16:31]\n"
"buffer_load_dwordx4 v[0:3], v236, s[48:51], s75 offen sc1\n"
"v_mfma_f32_32x32x16_bf16 v[32:47], a[56:59], v[108:111], v[32:47]\n"
"buffer_load_dwordx4 v[4:7], v237, s[48:51], s75 offen sc1\n"
"v_mfma_f32_32x32x16_bf16 v[48:63], a[60:63], v[104:107], v[48:63]\n"
"s_waitcnt vmcnt(20)\n"
"v_max3_u32 v186, v112, v114, v116\n"
"v_max_u32 v186, v186, v118\n"
"v_cmp_eq_u32 vcc, -1, v186\n"
"v_mfma_f32_32x32x16_bf16 v[64:79], a[60:63], v[108:111], v[64:79]\n"
"s_nop 3\n"
"s_cbranch_vccnz Lh8_%=\n"
"Lhr8_%=:\n"
"v_mfma_f32_32x32x16_bf16 v[16:31], a[64:67], v[112:115], v[16:31]\n"
"buffer_load_dwordx4 v[8:11], v236, s[48:51], s76 offen sc1\n"
"v_mfma_f32_32x32x16_bf16 v[32:47], a[64:67], v[116:119], v[32:47]\n"
"buffer_load_dwordx4 v[12:15], v237, s[48:51], s76 offen sc1\n"
"buffer_load_dword v190, v249, s[48:51], s84 offen offset:0 sc1\n"
"buffer_load_dword v191, v249, s[48:51], s84 offen offset:128 sc1\n"
"v_mfma_f32_32x32x16_bf16 v[48:63], a[68:71], v[112:115], v[48:63]\n"
"s_waitcnt vmcnt(22)\n"
"v_max3_u32 v186, v120, v122, v124\n"
"v_max_u32 v186, v186, v126\n"
"v_cmp_eq_u32 vcc, -1, v186\n"
"v_mfma_f32_32x32x16_bf16 v[64:79], a[68:71], v[116:119], v[64:79]\n"
"s_nop 3\n"
"s_cbranch_vccnz Lh9_%=\n"
"Lhr9_%=:\n"
"v_mfma_f32_32x32x16_bf16 v[16:31], a[72:75], v[120:123], v[16:31]\n"
"buffer_load_dwordx4 v[80:83], v236, s[48:51], s77 offen sc1\n"
"v_mfma_f32_32x32x16_bf16 v[32:47], a[72:75], v[124:127], v[32:47]\n"
"buffer_load_dwordx4 v[84:87], v237, s[48:51], s77 offen sc1\n"
"buffer_load_dword v192, v249, s[48:51], s84 offen offset:512 sc1\n"
"buffer_load_dword v193, v249, s[48:51], s84 offen offset:640 sc1\n"
"v_mfma_f32_32x32x16_bf16 v[48:63], a[76:79], v[120:123], v[48:63]\n"
"s_waitcnt vmcnt(24)\n"
"v_max3_u32 v186, v160, v162, v164\n"
"v_max_u32 v186, v186, v166\n"
"v_cmp_eq_u32 vcc, -1, v186\n"
"v_mfma_f32_32x32x16_bf16 v[64:79], a[76:79], v[124:127], v[64:79]\n"
"s_nop 3\n"
"s_cbranch_vccnz Lh10_%=\n"
"Lhr10_%=:\n"
"v_mfma_f32_32x32x16_bf16 v[16:31], a[80:83], v[160:163], v[16:31]\n"
"buffer_load_dwordx4 v[88:91], v236, s[48:51], s78 offen sc1\n"
"v_mfma_f32_32x32x16_bf16 v[32:47], a[80:83], v[164:167], v[32:47]\n"
"buffer_load_dwordx4 v[92:95], v237, s[48:51], s78 offen sc1\n"
"buffer_load_dword v194, v249, s[48:51], s84 offen offset:1024 sc1\n"
"buffer_load_dword v195, v249, s[48:51], s84 offen offset:1152 sc1\n"
"v_mfma_f32_32x32x16_bf16 v[48:63], a[84:87], v[160:163], v[48:63]\n"
"s_waitcnt vmcnt(26)\n"
"v_max3_u32 v186, v168, v170, v172\n"
"v_max_u32 v186, v186, v174\n"
"v_cmp_eq_u32 vcc, -1, v186\n"
"v_mfma_f32_32x32x16_bf16 v[64:79], a[84:87], v[164:167], v[64:79]\n"
"s_nop 3\n"
"s_cbranch_vccnz Lh11_%=\n"
"Lhr11_%=:\n"
"v_mfma_f32_32x32x16_bf16 v[16:31], a[88:91], v[168:171], v[16:31]\n"
"buffer_load_dwordx4 v[96:99], v238, s[48:51], s72 offen sc1\n"
"v_mfma_f32_32x32x16_bf16 v[32:47], a[88:91], v[172:175], v[32:47]\n"
"buffer_load_dwordx4 v[100:103], v239, s[48:51], s72 offen sc1\n"
"buffer_load_dword v196, v249, s[48:51], s84 offen offset:1536 sc1\n"
"buffer_load_dword v197, v249, s[48:51], s84 offen offset:1664 sc1\n"
"v_mfma_f32_32x32x16_bf16 v[48:63], a[92:95], v[168:171], v[48:63]\n"
"s_waitcnt vmcnt(27)\n"
"v_max3_u32 v186, v176, v178, v180\n"
"v_max_u32 v186, v186, v182\n"
"v_cmp_eq_u32 vcc, -1, v186\n"
"v_mfma_f32_32x32x16_bf16 v[64:79], a[92:95], v[172:175], v[64:79]\n"
"s_nop 3\n"
"s_cbranch_vccnz Lh12_%=\n"
"Lhr12_%=:\n"
"v_mfma_f32_32x32x16_bf16 v[16:31], a[96:99], v[176:179], v[16:31]\n"
"buffer_load_dwordx4 v[104:107], v238, s[48:51], s73 offen sc1\n"
"s_barrier\n"
"v_mfma_f32_32x32x16_bf16 v[32:47], a[96:99], v[180:183], v[32:47]\n"
"buffer_load_dwordx4 v[108:111], v239, s[48:51], s73 offen sc1\n"
"buffer_load_dwordx2 v[198:199], v250, s[48:51], s89 offen sc1\n"
"v_mfma_f32_32x32x16_bf16 v[48:63], a[100:103], v[176:179], v[48:63]\n"
"s_waitcnt vmcnt(28)\n"
"v_max3_u32 v186, v128, v130, v132\n"
"v_max_u32 v186, v186, v134\n"
"v_cmp_eq_u32 vcc, -1, v186\n"
"v_mfma_f32_32x32x16_bf16 v[64:79], a[100:103], v[180:183], v[64:79]\n"
"s_nop 3\n"
"s_cbranch_vccnz Lh13_%=\n"
"Lhr13_%=:\n"
"v_mfma_f32_32x32x16_bf16 v[16:31], a[104:107], v[128:131], v[16:31]\n"
"buffer_load_dwordx4 v[112:115], v238, s[48:51], s74 offen sc1\n"
"v_mfma_f32_32x32x16_bf16 v[32:47], a[104:107], v[132:135], v[32:47]\n"
"buffer_load_dwordx4 v[116:119], v239, s[48:51], s74 offen sc1\n"
"v_mfma_f32_32x32x16_bf16 v[48:63], a[108:111], v[128:131], v[48:63]\n"
"s_waitcnt vmcnt(28)\n"
"v_max3_u32 v186, v136, v138, v140\n"
"v_max_u32 v186, v186, v142\n"
"v_cmp_eq_u32 vcc, -1, v186\n"
"v_mfma_f32_32x32x16_bf16 v[64:79], a[108:111], v[132:135], v[64:79]\n"
"s_nop 3\n"
"s_cbranch_vccnz Lh14_%=\n"
"Lhr14_%=:\n"
"v_mfma_f32_32x32x16_bf16 v[16:31], a[112:115], v[136:139], v[16:31]\n"
"buffer_load_dwordx4 v[120:123], v238, s[48:51], s75 offen sc1\n"
"v_mfma_f32_32x32x16_bf16 v[32:47], a[112:115], v[140:143], v[32:47]\n"
"buffer_load_dwordx4 v[124:127], v239, s[48:51], s75 offen sc1\n"
"v_mfma_f32_32x32x16_bf16 v[48:63], a[116:119], v[136:139], v[48:63]\n"
"s_waitcnt vmcnt(28)\n"
"v_max3_u32 v186, v208, v210, v212\n"
"v_max_u32 v186, v186, v214\n"
"v_cmp_eq_u32 vcc, -1, v186\n"
"v_mfma_f32_32x32x16_bf16 v[64:79], a[116:119], v[140:143], v[64:79]\n"
"s_nop 3\n"
"s_cbranch_vccnz Lh15_%=\n"
"Lhr15_%=:\n"
"v_mfma_f32_32x32x16_bf16 v[16:31], a[120:123], v[208:211], v[16:31]\n"
"buffer_load_dwordx4 v[160:163], v238, s[48:51], s76 offen sc1\n"
"v_mfma_f32_32x32x16_bf16 v[32:47], a[120:123], v[212:215], v[32:47]\n"
"buffer_load_dwordx4 v[164:167], v239, s[48:51], s76 offen sc1\n"
"v_mfma_f32_32x32x16_bf16 v[48:63], a[124:127], v[208:211], v[48:63]\n"
"s_waitcnt vmcnt(28)\n"
"v_max3_u32 v186, v216, v218, v220\n"
"v_max_u32 v186, v186, v222\n"
"v_cmp_eq_u32 vcc, -1, v186\n"
"v_mfma_f32_32x32x16_bf16 v[64:79], a[124:127], v[212:215], v[64:79]\n"
"s_nop 3\n"
"s_cbranch_vccnz Lh16_%=\n"
"Lhr16_%=:\n"
"v_mfma_f32_32x32x16_bf16 v[16:31], a[128:131], v[216:219], v[16:31]\n"
"buffer_load_dwordx4 v[168:171], v238, s[48:51], s77 offen sc1\n"
"v_mfma_f32_32x32x16_bf16 v[32:47], a[128:131], v[220:223], v[32:47]\n"
"buffer_load_dwordx4 v[172:175], v239, s[48:51], s77 offen sc1\n"
"v_mfma_f32_32x32x16_bf16 v[48:63], a[132:135], v[216:219], v[48:63]\n"
"s_waitcnt vmcnt(27)\n"
"v_max3_u32 v186, v0, v2, v4\n"
"v_max_u32 v186, v186, v6\n"
"v_cmp_eq_u32 vcc, -1, v186\n"
"v_mfma_f32_32x32x16_bf16 v[64:79], a[132:135], v[220:223], v[64:79]\n"
"s_nop 3\n"
"s_cbranch_vccnz Lh17_%=\n"
"Lhr17_%=:\n"
"v_mfma_f32_32x32x16_bf16 v[16:31], a[136:139], v[0:3], v[16:31]\n"
"buffer_load_dwordx4 v[176:179], v238, s[48:51], s78 offen sc1\n"
"v_mfma_f32_32x32x16_bf16 v[32:47], a[136:139], v[4:7], v[32:47]\n"
"buffer_load_dwordx4 v[180:183], v239, s[48:51], s78 offen sc1\n"
"v_mfma_f32_32x32x16_bf16 v[48:63], a[140:143], v[0:3], v[48:63]\n"
"s_waitcnt vmcnt(27)\n"
"v_max3_u32 v186, v8, v10, v12\n"
"v_max_u32 v186, v186, v14\n"
"v_cmp_eq_u32 vcc, -1, v186\n"
"v_mfma_f32_32x32x16_bf16 v[64:79], a[140:143], v[4:7], v[64:79]\n"
"s_nop 3\n"
"s_cbranch_vccnz Lh18_%=\n"
"Lhr18_%=:\n"
"v_mfma_f32_32x32x16_bf16 v[16:31], a[144:147], v[8:11], v[16:31]\n"
"v_mfma_f32_32x32x16_bf16 v[32:47], a[144:147], v[12:15], v[32:47]\n"
"v_mfma_f32_32x32x16_bf16 v[48:63], a[148:151], v[8:11], v[48:63]\n"
"s_waitcnt vmcnt(23)\n"
"v_max3_u32 v186, v80, v82, v84\n"
"v_max_u32 v186, v186, v86\n"
"v_cmp_eq_u32 vcc, -1, v186\n"
"v_mfma_f32_32x32x16_bf16 v[64:79], a[148:151], v[12:15], v[64:79]\n"
"s_nop 3\n"
"s_cbranch_vccnz Lh19_%=\n"
"Lhr19_%=:\n"
"v_mfma_f32_32x32x16_bf16 v[16:31], a[152:155], v[80:83], v[16:31]\n"
"s_waitcnt vmcnt(10)\n"
"v_cmp_ne_u32 vcc, s39, v199\n"
"s_and_b32 s0, s43, 2\n"
"s_lshl_b32 s0, s0, 6\n"
"v_add_u32 v244, s0, v248\n"
"v_mfma_f32_32x32x16_bf16 v[32:47], a[152:155], v[84:87], v[32:47]\n"
"s_nop 0\n"
"s_cbranch_vccnz Lgb_%=\n"
"Lgbr_%=:\n"
"ds_write_b32 v233, v198 offset:43008\n"
"v_mfma_f32_32x32x16_bf16 v[48:63], a[156:159], v[80:83], v[48:63]\n"
"v_max3_u32 v186, v88, v90, v92\n"
"v_max_u32 v186, v186, v94\n"
"v_cmp_eq_u32 vcc, -1, v186\n"
"v_mfma_f32_32x32x16_bf16 v[64:79], a[156:159], v[84:87], v[64:79]\n"
"s_nop 3\n"
"s_cbranch_vccnz Lh20_%=\n"
"Lhr20_%=:\n"
"v_mfma_f32_32x32x16_bf16 v[32:47], a[160:163], v[92:95], v[32:47]\n"
"ds_read_b128 v[144:147], v244 offset:512\n"
"ds_read_b128 v[148:151], v244 offset:544\n"
"v_mfma_f32_32x32x16_bf16 v[48:63], a[164:167], v[88:91], v[48:63]\n"
"ds_read_b128 v[152:155], v244 offset:576\n"
"ds_read_b128 v[156:159], v244 offset:608\n"
"v_max3_u32 v186, v96, v98, v100\n"
"v_max_u32 v186, v186, v102\n"
"v_cmp_eq_u32 vcc, -1, v186\n"
"v_mfma_f32_32x32x16_bf16 v[64:79], a[164:167], v[92:95], v[64:79]\n"
"s_nop 3\n"
"s_cbranch_vccnz Lh21_%=\n"
"Lhr21_%=:\n"
"v_mfma_f32_32x32x16_bf16 v[32:47], a[168:171], v[100:103], v[32:47]\n"
"v_lshlrev_b32 v208, 16, v190\n"
"v_and_b32 v209, 0xffff0000, v190\n"
"v_mfma_f32_32x32x16_bf16 v[48:63], a[172:175], v[96:99], v[48:63]\n"
"v_lshlrev_b32 v210, 16, v191\n"
"v_and_b32 v211, 0xffff0000, v191\n"
"v_max3_u32 v186, v104, v106, v108\n"
"v_max_u32 v186, v186, v110\n"
"v_cmp_eq_u32 vcc, -1, v186\n"
"v_mfma_f32_32x32x16_bf16 v[64:79], a[172:175], v[100:103], v[64:79]\n"
"v_lshlrev_b32 v212, 16, v192\n"
"v_and_b32 v213, 0xffff0000, v192\n"
"s_nop 1\n"
"s_cbranch_vccnz Lh22_%=\n"
"Lhr22_%=:\n"
"v_mfma_f32_32x32x16_bf16 v[32:47], a[176:179], v[108:111], v[32:47]\n"
"v_lshlrev_b32 v214, 16, v193\n"
"v_and_b32 v215, 0xffff0000, v193\n"
"v_mfma_f32_32x32x16_bf16 v[48:63], a[180:183], v[104:107], v[48:63]\n"
"v_lshlrev_b32 v216, 16, v194\n"
"v_and_b32 v217, 0xffff0000, v194\n"
"s_waitcnt vmcnt(8)\n"
"v_max3_u32 v186, v112, v114, v116\n"
"v_max_u32 v186, v186, v118\n"
"v_cmp_eq_u32 vcc, -1, v186\n"
"v_mfma_f32_32x32x16_bf16 v[64:79], a[180:183], v[108:111], v[64:79]\n"
"v_lshlrev_b32 v218, 16, v195\n"
"v_and_b32 v219, 0xffff0000, v195\n"
"s_nop 1\n"
"s_cbranch_vccnz Lh23_%=\n"
"Lhr23_%=:\n"
"v_mfma_f32_32x32x16_bf16 v[32:47], a[184:187], v[116:119], v[32:47]\n"
"v_lshlrev_b32 v220, 16, v196\n"
"v_and_b32 v221, 0xffff0000, v196\n"
"v_mfma_f32_32x32x16_bf16 v[48:63], a[188:191], v[112:115], v[48:63]\n"
"v_lshlrev_b32 v222, 16, v197\n"
"v_and_b32 v223, 0xffff0000, v197\n"
"s_waitcnt vmcnt(6)\n"
"v_max3_u32 v186, v120, v122, v124\n"
"v_max_u32 v186, v186, v126\n"
"v_cmp_eq_u32 vcc, -1, v186\n"
"v_mfma_f32_32x32x16_bf16 v[64:79], a[188:191], v[116:119], v[64:79]\n"
"v_xor_b32 v13, 16384, v252\n"
"v_xor_b32 v14, 32768, v252\n"
"s_nop 1\n"
"s_cbranch_vccnz Lh24_%=\n"
"Lhr24_%=:\n"
"v_mfma_f32_32x32x16_bf16 v[32:47], a[192:195], v[124:127], v[32:47]\n"
"v_xor_b32 v15, 49152, v252\n"
"v_add_u32 v13, 53248, v13\n"
"v_mfma_f32_32x32x16_bf16 v[48:63], a[196:199], v[120:123], v[48:63]\n"
"v_add_u32 v14, 57344, v14\n"
"v_add_u32 v15, 61440, v15\n"
"s_waitcnt vmcnt(4)\n"
"v_max3_u32 v186, v160, v162, v164\n"
"v_max_u32 v186, v186, v166\n"
"v_cmp_eq_u32 vcc, -1, v186\n"
"v_mfma_f32_32x32x16_bf16 v[64:79], a[196:199], v[124:127], v[64:79]\n"
"s_nop 3\n"
"s_cbranch_vccnz Lh25_%=\n"
"Lhr25_%=:\n"
"v_mfma_f32_32x32x16_bf16 v[32:47], a[200:203], v[164:167], v[32:47]\n"
"v_mfma_f32_32x32x16_bf16 v[48:63], a[204:207], v[160:163], v[48:63]\n"
"s_waitcnt vmcnt(2)\n"
"v_max3_u32 v186, v168, v170, v172\n"
"v_max_u32 v186, v186, v174\n"
"v_cmp_eq_u32 vcc, -1, v186\n"
"v_mfma_f32_32x32x16_bf16 v[64:79], a[204:207], v[164:167], v[64:79]\n"
"s_nop 3\n"
"s_cbranch_vccnz Lh26_%=\n"
"Lhr26_%=:\n"
"v_mfma_f32_32x32x16_bf16 v[32:47], a[208:211], v[172:175], v[32:47]\n"
"v_mfma_f32_32x32x16_bf16 v[48:63], a[212:215], v[168:171], v[48:63]\n"
"s_waitcnt vmcnt(0)\n"
"v_max3_u32 v186, v176, v178, v180\n"
"v_max_u32 v186, v186, v182\n"
"v_cmp_eq_u32 vcc, -1, v186\n"
"v_mfma_f32_32x32x16_bf16 v[64:79], a[212:215], v[172:175], v[64:79]\n"
"s_nop 3\n"
"s_cbranch_vccnz Lh27_%=\n"
"Lhr27_%=:\n"
"v_mfma_f32_32x32x16_bf16 v[32:47], a[216:219], v[180:183], v[32:47]\n"
"v_mfma_f32_32x32x16_bf16 v[48:63], a[220:223], v[176:179], v[48:63]\n"
"v_mfma_f32_32x32x16_bf16 v[64:79], a[220:223], v[180:183], v[64:79]\n"
"v_mfma_f32_32x32x16_bf16 v[16:31], a[160:163], v[88:91], v[16:31]\n"
"v_mfma_f32_32x32x16_bf16 v[16:31], a[168:171], v[96:99], v[16:31]\n"
"s_nop 7\n"
"ds_write_b128 v13, v[32:35] offset:0\n"
"ds_write_b128 v13, v[36:39] offset:1024\n"
"v_mfma_f32_32x32x16_bf16 v[16:31], a[176:179], v[104:107], v[16:31]\n"
"ds_write_b128 v13, v[40:43] offset:2048\n"
"ds_write_b128 v13, v[44:47] offset:3072\n"
"v_mfma_f32_32x32x16_bf16 v[16:31], a[184:187], v[112:115], v[16:31]\n"
"ds_write_b128 v14, v[48:51] offset:0\n"
"ds_write_b128 v14, v[52:55] offset:1024\n"
"v_mfma_f32_32x32x16_bf16 v[16:31], a[192:195], v[120:123], v[16:31]\n"
"ds_write_b128 v14, v[56:59] offset:2048\n"
"ds_write_b128 v14, v[60:63] offset:3072\n"
"v_mfma_f32_32x32x16_bf16 v[16:31], a[200:203], v[160:163], v[16:31]\n"
"ds_write_b128 v15, v[64:67] offset:0\n"
"ds_write_b128 v15, v[68:71] offset:1024\n"
"v_mfma_f32_32x32x16_bf16 v[16:31], a[208:211], v[168:171], v[16:31]\n"
"ds_write_b128 v15, v[72:75] offset:2048\n"
"ds_write_b128 v15, v[76:79] offset:3072\n"
"v_mfma_f32_32x32x16_bf16 v[16:31], a[216:219], v[176:179], v[16:31]\n"
"s_waitcnt lgkmcnt(0)\n"
"s_barrier\n"
"ds_read_b128 v[32:35], v253 offset:4096\n"
"ds_read_b128 v[36:39], v253 offset:8192\n"
"ds_read_b128 v[40:43], v253 offset:12288\n"
"ds_read_b128 v[44:47], v253 offset:5120\n"
"ds_read_b128 v[48:51], v253 offset:9216\n"
"ds_read_b128 v[52:55], v253 offset:13312\n"
"ds_read_b128 v[56:59], v253 offset:6144\n"
"ds_read_b128 v[60:63], v253 offset:10240\n"
"ds_read_b128 v[64:67], v253 offset:14336\n"
"ds_read_b128 v[68:71], v253 offset:7168\n"
"ds_read_b128 v[72:75], v253 offset:11264\n"
"ds_read_b128 v[76:79], v253 offset:15360\n"
"s_waitcnt lgkmcnt(11)\n"
"v_pk_add_f32 v[16:17], v[16:17], v[32:33]\n"
"s_waitcnt lgkmcnt(9)\n"
"v_pk_add_f32 v[36:37], v[36:37], v[40:41]\n"
"v_pk_add_f32 v[18:19], v[18:19], v[34:35]\n"
"v_pk_add_f32 v[38:39], v[38:39], v[42:43]\n"
"v_pk_add_f32 v[16:17], v[16:17], v[36:37]\n"
"v_pk_add_f32 v[18:19], v[18:19], v[38:39]\n"
"v_pk_fma_f32 v[208:209], v[144:145], v[16:17], v[208:209]\n"
"v_pk_fma_f32 v[210:211], v[146:147], v[18:19], v[210:211]\n"
"v_cvt_pk_bf16_f32 v112, v208, v209\n"
"v_cvt_pk_bf16_f32 v113, v210, v211\n"
"s_waitcnt lgkmcnt(8)\n"
"v_pk_add_f32 v[20:21], v[20:21], v[44:45]\n"
"s_waitcnt lgkmcnt(6)\n"
"v_pk_add_f32 v[48:49], v[48:49], v[52:53]\n"
"v_pk_add_f32 v[22:23], v[22:23], v[46:47]\n"
"v_pk_add_f32 v[50:51], v[50:51], v[54:55]\n"
"v_pk_add_f32 v[20:21], v[20:21], v[48:49]\n"
"v_pk_add_f32 v[22:23], v[22:23], v[50:51]\n"
"v_pk_fma_f32 v[212:213], v[148:149], v[20:21], v[212:213]\n"
"v_pk_fma_f32 v[214:215], v[150:151], v[22:23], v[214:215]\n"
"v_cvt_pk_bf16_f32 v114, v212, v213\n"
"v_cvt_pk_bf16_f32 v115, v214, v215\n"
"s_nop 0\n"
"v_permlane32_swap_b32 v112, v114\n"
"v_permlane32_swap_b32 v113, v115\n"
"buffer_store_dwordx4 v[112:115], v251, s[52:55], 0 offen offset:0\n"
"s_waitcnt lgkmcnt(5)\n"
"v_pk_add_f32 v[24:25], v[24:25], v[56:57]\n"
"s_waitcnt lgkmcnt(3)\n"
"v_pk_add_f32 v[60:61], v[60:61], v[64:65]\n"
"v_pk_add_f32 v[26:27], v[26:27], v[58:59]\n"
"v_pk_add_f32 v[62:63], v[62:63], v[66:67]\n"
"v_pk_add_f32 v[24:25], v[24:25], v[60:61]\n"
"v_pk_add_f32 v[26:27], v[26:27], v[62:63]\n"
"v_pk_fma_f32 v[216:217], v[152:153], v[24:25], v[216:217]\n"
"v_pk_fma_f32 v[218:219], v[154:155], v[26:27], v[218:219]\n"
"v_cvt_pk_bf16_f32 v116, v216, v217\n"
"v_cvt_pk_bf16_f32 v117, v218, v219\n"
"s_waitcnt lgkmcnt(2)\n"
"v_pk_add_f32 v[28:29], v[28:29], v[68:69]\n"
"s_waitcnt lgkmcnt(0)\n"
"v_pk_add_f32 v[72:73], v[72:73], v[76:77]\n"
"v_pk_add_f32 v[30:31], v[30:31], v[70:71]\n"
"v_pk_add_f32 v[74:75], v[74:75], v[78:79]\n"
"v_pk_add_f32 v[28:29], v[28:29], v[72:73]\n"
"v_pk_add_f32 v[30:31], v[30:31], v[74:75]\n"
"v_pk_fma_f32 v[220:221], v[156:157], v[28:29], v[220:221]\n"
"v_pk_fma_f32 v[222:223], v[158:159], v[30:31], v[222:223]\n"
"v_cvt_pk_bf16_f32 v118, v220, v221\n"
"v_cvt_pk_bf16_f32 v119, v222, v223\n"
"s_nop 0\n"
"v_permlane32_swap_b32 v116, v118\n"
"v_permlane32_swap_b32 v117, v119\n"
"buffer_store_dwordx4 v[116:119], v251, s[52:55], 0 offen offset:32\n"
"s_endpgm\n"
"Lsw_%=:\n"
"s_sub_u32 s86, s86, 1\n"
"s_cbranch_scc1 Lswr_%=\n"
"buffer_load_dwordx4 v[176:179], v208, s[48:51], s30 offen offset:0 sc1\n"
"buffer_load_dwordx4 v[192:195], v209, s[48:51], s34 offen offset:0 sc1\n"
"buffer_load_dwordx4 v[180:183], v208, s[48:51], s30 offen offset:16 sc1\n"
"buffer_load_dwordx4 v[196:199], v209, s[48:51], s35 offen offset:0 sc1\n"
"buffer_load_dwordx4 v[184:187], v208, s[48:51], s31 offen offset:0 sc1\n"
"buffer_load_dwordx4 v[200:203], v209, s[48:51], s34 offen offset:1024 sc1\n"
"buffer_load_dwordx4 v[188:191], v208, s[48:51], s31 offen offset:16 sc1\n"
"buffer_load_dwordx4 v[204:207], v209, s[48:51], s35 offen offset:1024 sc1\n"
"s_waitcnt vmcnt(0)\n"
"v_xor_b32 v66, s39, v177\n"
"v_xor_b32 v67, s39, v179\n"
"v_bitop3_b32 v66, v66, v193, s39 bitop3:0xf6\n"
"v_bitop3_b32 v67, v67, v195, s39 bitop3:0xf6\n"
"v_bitop3_b32 v66, v66, v181, s39 bitop3:0xf6\n"
"v_bitop3_b32 v67, v67, v183, s39 bitop3:0xf6\n"
"v_bitop3_b32 v66, v66, v197, s39 bitop3:0xf6\n"
"v_bitop3_b32 v67, v67, v199, s39 bitop3:0xf6\n"
"v_bitop3_b32 v66, v66, v185, s39 bitop3:0xf6\n"
"v_bitop3_b32 v67, v67, v187, s39 bitop3:0xf6\n"
"v_bitop3_b32 v66, v66, v201, s39 bitop3:0xf6\n"
"v_bitop3_b32 v67, v67, v203, s39 bitop3:0xf6\n"
"v_bitop3_b32 v66, v66, v189, s39 bitop3:0xf6\n"
"v_bitop3_b32 v67, v67, v191, s39 bitop3:0xf6\n"
"v_bitop3_b32 v66, v66, v205, s39 bitop3:0xf6\n"
"v_bitop3_b32 v67, v67, v207, s39 bitop3:0xf6\n"
"v_or_b32 v66, v66, v67\n"
"v_cmp_ne_u32 vcc, 0, v66\n"
"s_nop 4\n"
"s_cbranch_vccnz Lsw_%=\n"
"s_branch Lswr_%=\n"
"Lk0_%=:\n"
"v_cvt_pk_bf16_f32 v80, v144, v145\n"
"v_cvt_pk_bf16_f32 v81, v146, v147\n"
"ds_write_b64 v215, v[80:81] offset:0\n"
"v_cvt_pk_bf16_f32 v84, v160, v161\n"
"v_cvt_pk_bf16_f32 v85, v162, v163\n"
"ds_write_b64 v215, v[84:85] offset:4608\n"
"s_branch Lkd_%=\n"
"Lk1_%=:\n"
"v_cvt_pk_bf16_f32 v80, v144, v144\n"
"v_cvt_pk_bf16_f32 v81, v145, v146\n"
"ds_write_b64 v215, v[80:81] offset:512\n"
"v_cvt_pk_bf16_f32 v84, v160, v160\n"
"v_cvt_pk_bf16_f32 v85, v161, v162\n"
"ds_write_b64 v215, v[84:85] offset:5120\n"
"s_branch Lkd_%=\n"
"Lk2_%=:\n"
"s_mov_b32 vcc_lo, 0\n"
"s_mov_b32 vcc_hi, -1\n"
"v_cndmask_b32 v90, v144, v145, vcc\n"
"v_cvt_pk_bf16_f32 v80, v144, v144\n"
"v_cvt_pk_bf16_f32 v81, v90, v145\n"
"ds_write_b64 v215, v[80:81] offset:1024\n"
"v_cndmask_b32 v90, v146, v147, vcc\n"
"v_cvt_pk_bf16_f32 v82, v146, v146\n"
"v_cvt_pk_bf16_f32 v83, v90, v147\n"
"ds_write_b64 v215, v[82:83] offset:1536\n"
"v_cndmask_b32 v90, v160, v161, vcc\n"
"v_cvt_pk_bf16_f32 v84, v160, v160\n"
"v_cvt_pk_bf16_f32 v85, v90, v161\n"
"ds_write_b64 v215, v[84:85] offset:5632\n"
"v_cndmask_b32 v90, v162, v163, vcc\n"
"v_cvt_pk_bf16_f32 v86, v162, v162\n"
"v_cvt_pk_bf16_f32 v87, v90, v163\n"
"ds_write_b64 v215, v[86:87] offset:6144\n"
"s_branch Lkd_%=\n"
"Lsm_%=:\n"
"s_sub_u32 s86, s86, 1\n"
"s_cbranch_scc1 Lsmr_%=\n"
"s_mov_b64 exec, 0xff\n"
"buffer_load_dwordx4 v[220:223], v216, s[48:51], 0 offen sc1\n"
"s_mov_b64 exec, -1\n"
"s_waitcnt vmcnt(0)\n"
"v_xor_b32 v10, s39, v221\n"
"v_bitop3_b32 v10, v10, v223, s39 bitop3:0xf6\n"
"v_cmp_ne_u32 vcc, 0, v10\n"
"s_nop 4\n"
"s_cbranch_vccnz Lsm_%=\n"
"s_branch Lsmr_%=\n"
"Lga_%=:\n"
"s_sub_u32 s86, s86, 1\n"
"s_cbranch_scc1 Lgar_%=\n"
"s_mov_b64 exec, 1\n"
"buffer_load_dwordx4 v[220:223], v3, s[48:51], s41 offen sc1\n"
"s_mov_b64 exec, -1\n"
"s_waitcnt vmcnt(0)\n"
"v_xor_b32 v10, s39, v221\n"
"v_bitop3_b32 v10, v10, v223, s39 bitop3:0xf6\n"
"v_cmp_ne_u32 vcc, 0, v10\n"
"s_nop 4\n"
"s_cbranch_vccnz Lga_%=\n"
"s_branch Lgar_%=\n"
"Lfl_%=:\n"
"s_sub_u32 s86, s86, 1\n"
"s_cbranch_scc1 Lflr_%=\n"
"buffer_load_dword v15, v2, s[48:51], s3 offen sc1\n"
"s_waitcnt vmcnt(0)\n"
"v_cmp_ne_u32 vcc, s39, v15\n"
"s_nop 4\n"
"s_cbranch_vccnz Lfl_%=\n"
"s_branch Lflr_%=\n"
"Lx0_%=:\n"
"s_nop 4\n"
"v_max3_u32 v255, v16, v17, v18\n"
"v_max3_u32 v255, v255, v19, v20\n"
"v_max3_u32 v255, v255, v21, v22\n"
"v_max_u32 v255, v255, v23\n"
"v_cmp_eq_u32 vcc, -1, v255\n"
"s_nop 4\n"
"s_cbranch_vccz Lxr0_%=\n"
"s_sub_u32 s86, s86, 1\n"
"s_cbranch_scc1 Lxr0_%=\n"
"global_load_dword v16, v233, s[4:5] offset:-4096 sc1\n"
"global_load_dword v17, v233, s[4:5] offset:-3840 sc1\n"
"global_load_dword v18, v233, s[4:5] offset:-3584 sc1\n"
"global_load_dword v19, v233, s[4:5] offset:-3328 sc1\n"
"global_load_dword v20, v233, s[4:5] offset:-3072 sc1\n"
"global_load_dword v21, v233, s[4:5] offset:-2816 sc1\n"
"global_load_dword v22, v233, s[4:5] offset:-2560 sc1\n"
"global_load_dword v23, v233, s[4:5] offset:-2304 sc1\n"
"global_load_dword v24, v233, s[4:5] offset:-2048 sc1\n"
"global_load_dword v25, v233, s[4:5] offset:-1792 sc1\n"
"global_load_dword v26, v233, s[4:5] offset:-1536 sc1\n"
"global_load_dword v27, v233, s[4:5] offset:-1280 sc1\n"
"global_load_dword v28, v233, s[4:5] offset:-1024 sc1\n"
"global_load_dword v29, v233, s[4:5] offset:-768 sc1\n"
"global_load_dword v30, v233, s[4:5] offset:-512 sc1\n"
"global_load_dword v31, v233, s[4:5] offset:-256 sc1\n"
"global_load_dword v32, v233, s[4:5] offset:0 sc1\n"
"global_load_dword v33, v233, s[4:5] offset:256 sc1\n"
"global_load_dword v34, v233, s[4:5] offset:512 sc1\n"
"global_load_dword v35, v233, s[4:5] offset:768 sc1\n"
"global_load_dword v36, v233, s[4:5] offset:1024 sc1\n"
"global_load_dword v37, v233, s[4:5] offset:1280 sc1\n"
"global_load_dword v38, v233, s[4:5] offset:1536 sc1\n"
"global_load_dword v39, v233, s[4:5] offset:1792 sc1\n"
"global_load_dword v40, v233, s[4:5] offset:2048 sc1\n"
"global_load_dword v41, v233, s[4:5] offset:2304 sc1\n"
"global_load_dword v42, v233, s[4:5] offset:2560 sc1\n"
"global_load_dword v43, v233, s[4:5] offset:2816 sc1\n"
"global_load_dword v44, v233, s[4:5] offset:3072 sc1\n"
"global_load_dword v45, v233, s[4:5] offset:3328 sc1\n"
"global_load_dword v46, v233, s[4:5] offset:3584 sc1\n"
"global_load_dword v47, v233, s[4:5] offset:3840 sc1\n"
"global_load_dword v48, v233, s[6:7] offset:-4096 sc1\n"
"global_load_dword v49, v233, s[6:7] offset:-3840 sc1\n"
"global_load_dword v50, v233, s[6:7] offset:-3584 sc1\n"
"global_load_dword v51, v233, s[6:7] offset:-3328 sc1\n"
"global_load_dword v52, v233, s[6:7] offset:-3072 sc1\n"
"global_load_dword v53, v233, s[6:7] offset:-2816 sc1\n"
"global_load_dword v54, v233, s[6:7] offset:-2560 sc1\n"
"global_load_dword v55, v233, s[6:7] offset:-2304 sc1\n"
"global_load_dword v56, v233, s[6:7] offset:-2048 sc1\n"
"global_load_dword v57, v233, s[6:7] offset:-1792 sc1\n"
"global_load_dword v58, v233, s[6:7] offset:-1536 sc1\n"
"global_load_dword v59, v233, s[6:7] offset:-1280 sc1\n"
"global_load_dword v60, v233, s[6:7] offset:-1024 sc1\n"
"global_load_dword v61, v233, s[6:7] offset:-768 sc1\n"
"global_load_dword v62, v233, s[6:7] offset:-512 sc1\n"
"global_load_dword v63, v233, s[6:7] offset:-256 sc1\n"
"global_load_dword v64, v233, s[6:7] offset:0 sc1\n"
"global_load_dword v65, v233, s[6:7] offset:256 sc1\n"
"global_load_dword v66, v233, s[6:7] offset:512 sc1\n"
"global_load_dword v67, v233, s[6:7] offset:768 sc1\n"
"global_load_dword v68, v233, s[6:7] offset:1024 sc1\n"
"global_load_dword v69, v233, s[6:7] offset:1280 sc1\n"
"global_load_dword v70, v233, s[6:7] offset:1536 sc1\n"
"global_load_dword v71, v233, s[6:7] offset:1792 sc1\n"
"v_mov_b32 v186, 0\n"
"v_mov_b32 v187, 0\n"
"s_waitcnt vmcnt(48)\n"
"v_dot2c_f32_bf16 v186, v16, v189\n"
"v_dot2c_f32_bf16 v187, v16, v16\n"
"v_dot2c_f32_bf16 v186, v17, v189\n"
"v_dot2c_f32_bf16 v187, v17, v17\n"
"v_dot2c_f32_bf16 v186, v18, v189\n"
"v_dot2c_f32_bf16 v187, v18, v18\n"
"v_dot2c_f32_bf16 v186, v19, v189\n"
"v_dot2c_f32_bf16 v187, v19, v19\n"
"v_dot2c_f32_bf16 v186, v20, v189\n"
"v_dot2c_f32_bf16 v187, v20, v20\n"
"v_dot2c_f32_bf16 v186, v21, v189\n"
"v_dot2c_f32_bf16 v187, v21, v21\n"
"v_dot2c_f32_bf16 v186, v22, v189\n"
"v_dot2c_f32_bf16 v187, v22, v22\n"
"v_dot2c_f32_bf16 v186, v23, v189\n"
"v_dot2c_f32_bf16 v187, v23, v23\n"
"s_nop 2\n"
"v_cmp_u_f32 vcc, v187, v187\n"
"s_nop 4\n"
"s_cbranch_vccnz Lx0_%=\n"
"s_nop 4\n"
"s_branch Lxr0_%=\n"
"Lx1_%=:\n"
"s_nop 4\n"
"v_max3_u32 v255, v24, v25, v26\n"
"v_max3_u32 v255, v255, v27, v28\n"
"v_max3_u32 v255, v255, v29, v30\n"
"v_max_u32 v255, v255, v31\n"
"v_cmp_eq_u32 vcc, -1, v255\n"
"s_nop 4\n"
"s_cbranch_vccz Lxr1_%=\n"
"s_sub_u32 s86, s86, 1\n"
"s_cbranch_scc1 Lxr1_%=\n"
"global_load_dword v24, v233, s[4:5] offset:-2048 sc1\n"
"global_load_dword v25, v233, s[4:5] offset:-1792 sc1\n"
"global_load_dword v26, v233, s[4:5] offset:-1536 sc1\n"
"global_load_dword v27, v233, s[4:5] offset:-1280 sc1\n"
"global_load_dword v28, v233, s[4:5] offset:-1024 sc1\n"
"global_load_dword v29, v233, s[4:5] offset:-768 sc1\n"
"global_load_dword v30, v233, s[4:5] offset:-512 sc1\n"
"global_load_dword v31, v233, s[4:5] offset:-256 sc1\n"
"global_load_dword v32, v233, s[4:5] offset:0 sc1\n"
"global_load_dword v33, v233, s[4:5] offset:256 sc1\n"
"global_load_dword v34, v233, s[4:5] offset:512 sc1\n"
"global_load_dword v35, v233, s[4:5] offset:768 sc1\n"
"global_load_dword v36, v233, s[4:5] offset:1024 sc1\n"
"global_load_dword v37, v233, s[4:5] offset:1280 sc1\n"
"global_load_dword v38, v233, s[4:5] offset:1536 sc1\n"
"global_load_dword v39, v233, s[4:5] offset:1792 sc1\n"
"global_load_dword v40, v233, s[4:5] offset:2048 sc1\n"
"global_load_dword v41, v233, s[4:5] offset:2304 sc1\n"
"global_load_dword v42, v233, s[4:5] offset:2560 sc1\n"
"global_load_dword v43, v233, s[4:5] offset:2816 sc1\n"
"global_load_dword v44, v233, s[4:5] offset:3072 sc1\n"
"global_load_dword v45, v233, s[4:5] offset:3328 sc1\n"
"global_load_dword v46, v233, s[4:5] offset:3584 sc1\n"
"global_load_dword v47, v233, s[4:5] offset:3840 sc1\n"
"global_load_dword v48, v233, s[6:7] offset:-4096 sc1\n"
"global_load_dword v49, v233, s[6:7] offset:-3840 sc1\n"
"global_load_dword v50, v233, s[6:7] offset:-3584 sc1\n"
"global_load_dword v51, v233, s[6:7] offset:-3328 sc1\n"
"global_load_dword v52, v233, s[6:7] offset:-3072 sc1\n"
"global_load_dword v53, v233, s[6:7] offset:-2816 sc1\n"
"global_load_dword v54, v233, s[6:7] offset:-2560 sc1\n"
"global_load_dword v55, v233, s[6:7] offset:-2304 sc1\n"
"global_load_dword v56, v233, s[6:7] offset:-2048 sc1\n"
"global_load_dword v57, v233, s[6:7] offset:-1792 sc1\n"
"global_load_dword v58, v233, s[6:7] offset:-1536 sc1\n"
"global_load_dword v59, v233, s[6:7] offset:-1280 sc1\n"
"global_load_dword v60, v233, s[6:7] offset:-1024 sc1\n"
"global_load_dword v61, v233, s[6:7] offset:-768 sc1\n"
"global_load_dword v62, v233, s[6:7] offset:-512 sc1\n"
"global_load_dword v63, v233, s[6:7] offset:-256 sc1\n"
"global_load_dword v64, v233, s[6:7] offset:0 sc1\n"
"global_load_dword v65, v233, s[6:7] offset:256 sc1\n"
"global_load_dword v66, v233, s[6:7] offset:512 sc1\n"
"global_load_dword v67, v233, s[6:7] offset:768 sc1\n"
"global_load_dword v68, v233, s[6:7] offset:1024 sc1\n"
"global_load_dword v69, v233, s[6:7] offset:1280 sc1\n"
"global_load_dword v70, v233, s[6:7] offset:1536 sc1\n"
"global_load_dword v71, v233, s[6:7] offset:1792 sc1\n"
"global_load_dword v72, v233, s[6:7] offset:2048 sc1\n"
"global_load_dword v73, v233, s[6:7] offset:2304 sc1\n"
"global_load_dword v74, v233, s[6:7] offset:2560 sc1\n"
"global_load_dword v75, v233, s[6:7] offset:2816 sc1\n"
"global_load_dword v76, v233, s[6:7] offset:3072 sc1\n"
"global_load_dword v77, v233, s[6:7] offset:3328 sc1\n"
"global_load_dword v78, v233, s[6:7] offset:3584 sc1\n"
"global_load_dword v79, v233, s[6:7] offset:3840 sc1\n"
"v_mov_b32 v186, 0\n"
"v_mov_b32 v187, 0\n"
"s_waitcnt vmcnt(48)\n"
"v_dot2c_f32_bf16 v186, v24, v189\n"
"v_dot2c_f32_bf16 v187, v24, v24\n"
"v_dot2c_f32_bf16 v186, v25, v189\n"
"v_dot2c_f32_bf16 v187, v25, v25\n"
"v_dot2c_f32_bf16 v186, v26, v189\n"
"v_dot2c_f32_bf16 v187, v26, v26\n"
"v_dot2c_f32_bf16 v186, v27, v189\n"
"v_dot2c_f32_bf16 v187, v27, v27\n"
"v_dot2c_f32_bf16 v186, v28, v189\n"
"v_dot2c_f32_bf16 v187, v28, v28\n"
"v_dot2c_f32_bf16 v186, v29, v189\n"
"v_dot2c_f32_bf16 v187, v29, v29\n"
"v_dot2c_f32_bf16 v186, v30, v189\n"
"v_dot2c_f32_bf16 v187, v30, v30\n"
"v_dot2c_f32_bf16 v186, v31, v189\n"
"v_dot2c_f32_bf16 v187, v31, v31\n"
"s_nop 2\n"
"v_cmp_u_f32 vcc, v187, v187\n"
"s_nop 4\n"
"s_cbranch_vccnz Lx1_%=\n"
"s_nop 4\n"
"s_branch Lxr1_%=\n"
"Lx2_%=:\n"
"s_nop 4\n"
"v_max3_u32 v255, v32, v33, v34\n"
"v_max3_u32 v255, v255, v35, v36\n"
"v_max3_u32 v255, v255, v37, v38\n"
"v_max_u32 v255, v255, v39\n"
"v_cmp_eq_u32 vcc, -1, v255\n"
"s_nop 4\n"
"s_cbranch_vccz Lxr2_%=\n"
"s_sub_u32 s86, s86, 1\n"
"s_cbranch_scc1 Lxr2_%=\n"
"global_load_dword v32, v233, s[4:5] offset:0 sc1\n"
"global_load_dword v33, v233, s[4:5] offset:256 sc1\n"
"global_load_dword v34, v233, s[4:5] offset:512 sc1\n"
"global_load_dword v35, v233, s[4:5] offset:768 sc1\n"
"global_load_dword v36, v233, s[4:5] offset:1024 sc1\n"
"global_load_dword v37, v233, s[4:5] offset:1280 sc1\n"
"global_load_dword v38, v233, s[4:5] offset:1536 sc1\n"
"global_load_dword v39, v233, s[4:5] offset:1792 sc1\n"
"global_load_dword v40, v233, s[4:5] offset:2048 sc1\n"
"global_load_dword v41, v233, s[4:5] offset:2304 sc1\n"
"global_load_dword v42, v233, s[4:5] offset:2560 sc1\n"
"global_load_dword v43, v233, s[4:5] offset:2816 sc1\n"
"global_load_dword v44, v233, s[4:5] offset:3072 sc1\n"
"global_load_dword v45, v233, s[4:5] offset:3328 sc1\n"
"global_load_dword v46, v233, s[4:5] offset:3584 sc1\n"
"global_load_dword v47, v233, s[4:5] offset:3840 sc1\n"
"global_load_dword v48, v233, s[6:7] offset:-4096 sc1\n"
"global_load_dword v49, v233, s[6:7] offset:-3840 sc1\n"
"global_load_dword v50, v233, s[6:7] offset:-3584 sc1\n"
"global_load_dword v51, v233, s[6:7] offset:-3328 sc1\n"
"global_load_dword v52, v233, s[6:7] offset:-3072 sc1\n"
"global_load_dword v53, v233, s[6:7] offset:-2816 sc1\n"
"global_load_dword v54, v233, s[6:7] offset:-2560 sc1\n"
"global_load_dword v55, v233, s[6:7] offset:-2304 sc1\n"
"global_load_dword v56, v233, s[6:7] offset:-2048 sc1\n"
"global_load_dword v57, v233, s[6:7] offset:-1792 sc1\n"
"global_load_dword v58, v233, s[6:7] offset:-1536 sc1\n"
"global_load_dword v59, v233, s[6:7] offset:-1280 sc1\n"
"global_load_dword v60, v233, s[6:7] offset:-1024 sc1\n"
"global_load_dword v61, v233, s[6:7] offset:-768 sc1\n"
"global_load_dword v62, v233, s[6:7] offset:-512 sc1\n"
"global_load_dword v63, v233, s[6:7] offset:-256 sc1\n"
"global_load_dword v64, v233, s[6:7] offset:0 sc1\n"
"global_load_dword v65, v233, s[6:7] offset:256 sc1\n"
"global_load_dword v66, v233, s[6:7] offset:512 sc1\n"
"global_load_dword v67, v233, s[6:7] offset:768 sc1\n"
"global_load_dword v68, v233, s[6:7] offset:1024 sc1\n"
"global_load_dword v69, v233, s[6:7] offset:1280 sc1\n"
"global_load_dword v70, v233, s[6:7] offset:1536 sc1\n"
"global_load_dword v71, v233, s[6:7] offset:1792 sc1\n"
"global_load_dword v72, v233, s[6:7] offset:2048 sc1\n"
"global_load_dword v73, v233, s[6:7] offset:2304 sc1\n"
"global_load_dword v74, v233, s[6:7] offset:2560 sc1\n"
"global_load_dword v75, v233, s[6:7] offset:2816 sc1\n"
"global_load_dword v76, v233, s[6:7] offset:3072 sc1\n"
"global_load_dword v77, v233, s[6:7] offset:3328 sc1\n"
"global_load_dword v78, v233, s[6:7] offset:3584 sc1\n"
"global_load_dword v79, v233, s[6:7] offset:3840 sc1\n"
"global_load_dword v80, v233, s[8:9] offset:-4096 sc1\n"
"global_load_dword v81, v233, s[8:9] offset:-3840 sc1\n"
"global_load_dword v82, v233, s[8:9] offset:-3584 sc1\n"
"global_load_dword v83, v233, s[8:9] offset:-3328 sc1\n"
"global_load_dword v84, v233, s[8:9] offset:-3072 sc1\n"
"global_load_dword v85, v233, s[8:9] offset:-2816 sc1\n"
"global_load_dword v86, v233, s[8:9] offset:-2560 sc1\n"
"global_load_dword v87, v233, s[8:9] offset:-2304 sc1\n"
"v_mov_b32 v186, 0\n"
"v_mov_b32 v187, 0\n"
"s_waitcnt vmcnt(48)\n"
"v_dot2c_f32_bf16 v186, v32, v189\n"
"v_dot2c_f32_bf16 v187, v32, v32\n"
"v_dot2c_f32_bf16 v186, v33, v189\n"
"v_dot2c_f32_bf16 v187, v33, v33\n"
"v_dot2c_f32_bf16 v186, v34, v189\n"
"v_dot2c_f32_bf16 v187, v34, v34\n"
"v_dot2c_f32_bf16 v186, v35, v189\n"
"v_dot2c_f32_bf16 v187, v35, v35\n"
"v_dot2c_f32_bf16 v186, v36, v189\n"
"v_dot2c_f32_bf16 v187, v36, v36\n"
"v_dot2c_f32_bf16 v186, v37, v189\n"
"v_dot2c_f32_bf16 v187, v37, v37\n"
"v_dot2c_f32_bf16 v186, v38, v189\n"
"v_dot2c_f32_bf16 v187, v38, v38\n"
"v_dot2c_f32_bf16 v186, v39, v189\n"
"v_dot2c_f32_bf16 v187, v39, v39\n"
"s_nop 2\n"
"v_cmp_u_f32 vcc, v187, v187\n"
"s_nop 4\n"
"s_cbranch_vccnz Lx2_%=\n"
"s_nop 4\n"
"s_branch Lxr2_%=\n"
"Lx3_%=:\n"
"s_nop 4\n"
"v_max3_u32 v255, v40, v41, v42\n"
"v_max3_u32 v255, v255, v43, v44\n"
"v_max3_u32 v255, v255, v45, v46\n"
"v_max_u32 v255, v255, v47\n"
"v_cmp_eq_u32 vcc, -1, v255\n"
"s_nop 4\n"
"s_cbranch_vccz Lxr3_%=\n"
"s_sub_u32 s86, s86, 1\n"
"s_cbranch_scc1 Lxr3_%=\n"
"global_load_dword v40, v233, s[4:5] offset:2048 sc1\n"
"global_load_dword v41, v233, s[4:5] offset:2304 sc1\n"
"global_load_dword v42, v233, s[4:5] offset:2560 sc1\n"
"global_load_dword v43, v233, s[4:5] offset:2816 sc1\n"
"global_load_dword v44, v233, s[4:5] offset:3072 sc1\n"
"global_load_dword v45, v233, s[4:5] offset:3328 sc1\n"
"global_load_dword v46, v233, s[4:5] offset:3584 sc1\n"
"global_load_dword v47, v233, s[4:5] offset:3840 sc1\n"
"global_load_dword v48, v233, s[6:7] offset:-4096 sc1\n"
"global_load_dword v49, v233, s[6:7] offset:-3840 sc1\n"
"global_load_dword v50, v233, s[6:7] offset:-3584 sc1\n"
"global_load_dword v51, v233, s[6:7] offset:-3328 sc1\n"
"global_load_dword v52, v233, s[6:7] offset:-3072 sc1\n"
"global_load_dword v53, v233, s[6:7] offset:-2816 sc1\n"
"global_load_dword v54, v233, s[6:7] offset:-2560 sc1\n"
"global_load_dword v55, v233, s[6:7] offset:-2304 sc1\n"
"global_load_dword v56, v233, s[6:7] offset:-2048 sc1\n"
"global_load_dword v57, v233, s[6:7] offset:-1792 sc1\n"
"global_load_dword v58, v233, s[6:7] offset:-1536 sc1\n"
"global_load_dword v59, v233, s[6:7] offset:-1280 sc1\n"
"global_load_dword v60, v233, s[6:7] offset:-1024 sc1\n"
"global_load_dword v61, v233, s[6:7] offset:-768 sc1\n"
"global_load_dword v62, v233, s[6:7] offset:-512 sc1\n"
"global_load_dword v63, v233, s[6:7] offset:-256 sc1\n"
"global_load_dword v64, v233, s[6:7] offset:0 sc1\n"
"global_load_dword v65, v233, s[6:7] offset:256 sc1\n"
"global_load_dword v66, v233, s[6:7] offset:512 sc1\n"
"global_load_dword v67, v233, s[6:7] offset:768 sc1\n"
"global_load_dword v68, v233, s[6:7] offset:1024 sc1\n"
"global_load_dword v69, v233, s[6:7] offset:1280 sc1\n"
"global_load_dword v70, v233, s[6:7] offset:1536 sc1\n"
"global_load_dword v71, v233, s[6:7] offset:1792 sc1\n"
"global_load_dword v72, v233, s[6:7] offset:2048 sc1\n"
"global_load_dword v73, v233, s[6:7] offset:2304 sc1\n"
"global_load_dword v74, v233, s[6:7] offset:2560 sc1\n"
"global_load_dword v75, v233, s[6:7] offset:2816 sc1\n"
"global_load_dword v76, v233, s[6:7] offset:3072 sc1\n"
"global_load_dword v77, v233, s[6:7] offset:3328 sc1\n"
"global_load_dword v78, v233, s[6:7] offset:3584 sc1\n"
"global_load_dword v79, v233, s[6:7] offset:3840 sc1\n"
"global_load_dword v80, v233, s[8:9] offset:-4096 sc1\n"
"global_load_dword v81, v233, s[8:9] offset:-3840 sc1\n"
"global_load_dword v82, v233, s[8:9] offset:-3584 sc1\n"
"global_load_dword v83, v233, s[8:9] offset:-3328 sc1\n"
"global_load_dword v84, v233, s[8:9] offset:-3072 sc1\n"
"global_load_dword v85, v233, s[8:9] offset:-2816 sc1\n"
"global_load_dword v86, v233, s[8:9] offset:-2560 sc1\n"
"global_load_dword v87, v233, s[8:9] offset:-2304 sc1\n"
"global_load_dword v88, v233, s[8:9] offset:-2048 sc1\n"
"global_load_dword v89, v233, s[8:9] offset:-1792 sc1\n"
"global_load_dword v90, v233, s[8:9] offset:-1536 sc1\n"
"global_load_dword v91, v233, s[8:9] offset:-1280 sc1\n"
"global_load_dword v92, v233, s[8:9] offset:-1024 sc1\n"
"global_load_dword v93, v233, s[8:9] offset:-768 sc1\n"
"global_load_dword v94, v233, s[8:9] offset:-512 sc1\n"
"global_load_dword v95, v233, s[8:9] offset:-256 sc1\n"
"v_mov_b32 v186, 0\n"
"v_mov_b32 v187, 0\n"
"s_waitcnt vmcnt(48)\n"
"v_dot2c_f32_bf16 v186, v40, v189\n"
"v_dot2c_f32_bf16 v187, v40, v40\n"
"v_dot2c_f32_bf16 v186, v41, v189\n"
"v_dot2c_f32_bf16 v187, v41, v41\n"
"v_dot2c_f32_bf16 v186, v42, v189\n"
"v_dot2c_f32_bf16 v187, v42, v42\n"
"v_dot2c_f32_bf16 v186, v43, v189\n"
"v_dot2c_f32_bf16 v187, v43, v43\n"
"v_dot2c_f32_bf16 v186, v44, v189\n"
"v_dot2c_f32_bf16 v187, v44, v44\n"
"v_dot2c_f32_bf16 v186, v45, v189\n"
"v_dot2c_f32_bf16 v187, v45, v45\n"
"v_dot2c_f32_bf16 v186, v46, v189\n"
"v_dot2c_f32_bf16 v187, v46, v46\n"
"v_dot2c_f32_bf16 v186, v47, v189\n"
"v_dot2c_f32_bf16 v187, v47, v47\n"
"s_nop 2\n"
"v_cmp_u_f32 vcc, v187, v187\n"
"s_nop 4\n"
"s_cbranch_vccnz Lx3_%=\n"
"s_nop 4\n"
"s_branch Lxr3_%=\n"
"Lx4_%=:\n"
"s_nop 4\n"
"v_max3_u32 v255, v48, v49, v50\n"
"v_max3_u32 v255, v255, v51, v52\n"
"v_max3_u32 v255, v255, v53, v54\n"
"v_max_u32 v255, v255, v55\n"
"v_cmp_eq_u32 vcc, -1, v255\n"
"s_nop 4\n"
"s_cbranch_vccz Lxr4_%=\n"
"s_sub_u32 s86, s86, 1\n"
"s_cbranch_scc1 Lxr4_%=\n"
"global_load_dword v48, v233, s[6:7] offset:-4096 sc1\n"
"global_load_dword v49, v233, s[6:7] offset:-3840 sc1\n"
"global_load_dword v50, v233, s[6:7] offset:-3584 sc1\n"
"global_load_dword v51, v233, s[6:7] offset:-3328 sc1\n"
"global_load_dword v52, v233, s[6:7] offset:-3072 sc1\n"
"global_load_dword v53, v233, s[6:7] offset:-2816 sc1\n"
"global_load_dword v54, v233, s[6:7] offset:-2560 sc1\n"
"global_load_dword v55, v233, s[6:7] offset:-2304 sc1\n"
"global_load_dword v56, v233, s[6:7] offset:-2048 sc1\n"
"global_load_dword v57, v233, s[6:7] offset:-1792 sc1\n"
"global_load_dword v58, v233, s[6:7] offset:-1536 sc1\n"
"global_load_dword v59, v233, s[6:7] offset:-1280 sc1\n"
"global_load_dword v60, v233, s[6:7] offset:-1024 sc1\n"
"global_load_dword v61, v233, s[6:7] offset:-768 sc1\n"
"global_load_dword v62, v233, s[6:7] offset:-512 sc1\n"
"global_load_dword v63, v233, s[6:7] offset:-256 sc1\n"
"global_load_dword v64, v233, s[6:7] offset:0 sc1\n"
"global_load_dword v65, v233, s[6:7] offset:256 sc1\n"
"global_load_dword v66, v233, s[6:7] offset:512 sc1\n"
"global_load_dword v67, v233, s[6:7] offset:768 sc1\n"
"global_load_dword v68, v233, s[6:7] offset:1024 sc1\n"
"global_load_dword v69, v233, s[6:7] offset:1280 sc1\n"
"global_load_dword v70, v233, s[6:7] offset:1536 sc1\n"
"global_load_dword v71, v233, s[6:7] offset:1792 sc1\n"
"global_load_dword v72, v233, s[6:7] offset:2048 sc1\n"
"global_load_dword v73, v233, s[6:7] offset:2304 sc1\n"
"global_load_dword v74, v233, s[6:7] offset:2560 sc1\n"
"global_load_dword v75, v233, s[6:7] offset:2816 sc1\n"
"global_load_dword v76, v233, s[6:7] offset:3072 sc1\n"
"global_load_dword v77, v233, s[6:7] offset:3328 sc1\n"
"global_load_dword v78, v233, s[6:7] offset:3584 sc1\n"
"global_load_dword v79, v233, s[6:7] offset:3840 sc1\n"
"global_load_dword v80, v233, s[8:9] offset:-4096 sc1\n"
"global_load_dword v81, v233, s[8:9] offset:-3840 sc1\n"
"global_load_dword v82, v233, s[8:9] offset:-3584 sc1\n"
"global_load_dword v83, v233, s[8:9] offset:-3328 sc1\n"
"global_load_dword v84, v233, s[8:9] offset:-3072 sc1\n"
"global_load_dword v85, v233, s[8:9] offset:-2816 sc1\n"
"global_load_dword v86, v233, s[8:9] offset:-2560 sc1\n"
"global_load_dword v87, v233, s[8:9] offset:-2304 sc1\n"
"global_load_dword v88, v233, s[8:9] offset:-2048 sc1\n"
"global_load_dword v89, v233, s[8:9] offset:-1792 sc1\n"
"global_load_dword v90, v233, s[8:9] offset:-1536 sc1\n"
"global_load_dword v91, v233, s[8:9] offset:-1280 sc1\n"
"global_load_dword v92, v233, s[8:9] offset:-1024 sc1\n"
"global_load_dword v93, v233, s[8:9] offset:-768 sc1\n"
"global_load_dword v94, v233, s[8:9] offset:-512 sc1\n"
"global_load_dword v95, v233, s[8:9] offset:-256 sc1\n"
"global_load_dword v96, v233, s[8:9] offset:0 sc1\n"
"global_load_dword v97, v233, s[8:9] offset:256 sc1\n"
"global_load_dword v98, v233, s[8:9] offset:512 sc1\n"
"global_load_dword v99, v233, s[8:9] offset:768 sc1\n"
"global_load_dword v100, v233, s[8:9] offset:1024 sc1\n"
"global_load_dword v101, v233, s[8:9] offset:1280 sc1\n"
"global_load_dword v102, v233, s[8:9] offset:1536 sc1\n"
"global_load_dword v103, v233, s[8:9] offset:1792 sc1\n"
"v_mov_b32 v186, 0\n"
"v_mov_b32 v187, 0\n"
"s_waitcnt vmcnt(48)\n"
"v_dot2c_f32_bf16 v186, v48, v189\n"
"v_dot2c_f32_bf16 v187, v48, v48\n"
"v_dot2c_f32_bf16 v186, v49, v189\n"
"v_dot2c_f32_bf16 v187, v49, v49\n"
"v_dot2c_f32_bf16 v186, v50, v189\n"
"v_dot2c_f32_bf16 v187, v50, v50\n"
"v_dot2c_f32_bf16 v186, v51, v189\n"
"v_dot2c_f32_bf16 v187, v51, v51\n"
"v_dot2c_f32_bf16 v186, v52, v189\n"
"v_dot2c_f32_bf16 v187, v52, v52\n"
"v_dot2c_f32_bf16 v186, v53, v189\n"
"v_dot2c_f32_bf16 v187, v53, v53\n"
"v_dot2c_f32_bf16 v186, v54, v189\n"
"v_dot2c_f32_bf16 v187, v54, v54\n"
"v_dot2c_f32_bf16 v186, v55, v189\n"
"v_dot2c_f32_bf16 v187, v55, v55\n"
"s_nop 2\n"
"v_cmp_u_f32 vcc, v187, v187\n"
"s_nop 4\n"
"s_cbranch_vccnz Lx4_%=\n"
"s_nop 4\n"
"s_branch Lxr4_%=\n"
"Lx5_%=:\n"
"s_nop 4\n"
"v_max3_u32 v255, v56, v57, v58\n"
"v_max3_u32 v255, v255, v59, v60\n"
"v_max3_u32 v255, v255, v61, v62\n"
"v_max_u32 v255, v255, v63\n"
"v_cmp_eq_u32 vcc, -1, v255\n"
"s_nop 4\n"
"s_cbranch_vccz Lxr5_%=\n"
"s_sub_u32 s86, s86, 1\n"
"s_cbranch_scc1 Lxr5_%=\n"
"global_load_dword v56, v233, s[6:7] offset:-2048 sc1\n"
"global_load_dword v57, v233, s[6:7] offset:-1792 sc1\n"
"global_load_dword v58, v233, s[6:7] offset:-1536 sc1\n"
"global_load_dword v59, v233, s[6:7] offset:-1280 sc1\n"
"global_load_dword v60, v233, s[6:7] offset:-1024 sc1\n"
"global_load_dword v61, v233, s[6:7] offset:-768 sc1\n"
"global_load_dword v62, v233, s[6:7] offset:-512 sc1\n"
"global_load_dword v63, v233, s[6:7] offset:-256 sc1\n"
"global_load_dword v64, v233, s[6:7] offset:0 sc1\n"
"global_load_dword v65, v233, s[6:7] offset:256 sc1\n"
"global_load_dword v66, v233, s[6:7] offset:512 sc1\n"
"global_load_dword v67, v233, s[6:7] offset:768 sc1\n"
"global_load_dword v68, v233, s[6:7] offset:1024 sc1\n"
"global_load_dword v69, v233, s[6:7] offset:1280 sc1\n"
"global_load_dword v70, v233, s[6:7] offset:1536 sc1\n"
"global_load_dword v71, v233, s[6:7] offset:1792 sc1\n"
"global_load_dword v72, v233, s[6:7] offset:2048 sc1\n"
"global_load_dword v73, v233, s[6:7] offset:2304 sc1\n"
"global_load_dword v74, v233, s[6:7] offset:2560 sc1\n"
"global_load_dword v75, v233, s[6:7] offset:2816 sc1\n"
"global_load_dword v76, v233, s[6:7] offset:3072 sc1\n"
"global_load_dword v77, v233, s[6:7] offset:3328 sc1\n"
"global_load_dword v78, v233, s[6:7] offset:3584 sc1\n"
"global_load_dword v79, v233, s[6:7] offset:3840 sc1\n"
"global_load_dword v80, v233, s[8:9] offset:-4096 sc1\n"
"global_load_dword v81, v233, s[8:9] offset:-3840 sc1\n"
"global_load_dword v82, v233, s[8:9] offset:-3584 sc1\n"
"global_load_dword v83, v233, s[8:9] offset:-3328 sc1\n"
"global_load_dword v84, v233, s[8:9] offset:-3072 sc1\n"
"global_load_dword v85, v233, s[8:9] offset:-2816 sc1\n"
"global_load_dword v86, v233, s[8:9] offset:-2560 sc1\n"
"global_load_dword v87, v233, s[8:9] offset:-2304 sc1\n"
"global_load_dword v88, v233, s[8:9] offset:-2048 sc1\n"
"global_load_dword v89, v233, s[8:9] offset:-1792 sc1\n"
"global_load_dword v90, v233, s[8:9] offset:-1536 sc1\n"
"global_load_dword v91, v233, s[8:9] offset:-1280 sc1\n"
"global_load_dword v92, v233, s[8:9] offset:-1024 sc1\n"
"global_load_dword v93, v233, s[8:9] offset:-768 sc1\n"
"global_load_dword v94, v233, s[8:9] offset:-512 sc1\n"
"global_load_dword v95, v233, s[8:9] offset:-256 sc1\n"
"global_load_dword v96, v233, s[8:9] offset:0 sc1\n"
"global_load_dword v97, v233, s[8:9] offset:256 sc1\n"
"global_load_dword v98, v233, s[8:9] offset:512 sc1\n"
"global_load_dword v99, v233, s[8:9] offset:768 sc1\n"
"global_load_dword v100, v233, s[8:9] offset:1024 sc1\n"
"global_load_dword v101, v233, s[8:9] offset:1280 sc1\n"
"global_load_dword v102, v233, s[8:9] offset:1536 sc1\n"
"global_load_dword v103, v233, s[8:9] offset:1792 sc1\n"
"global_load_dword v104, v233, s[8:9] offset:2048 sc1\n"
"global_load_dword v105, v233, s[8:9] offset:2304 sc1\n"
"global_load_dword v106, v233, s[8:9] offset:2560 sc1\n"
"global_load_dword v107, v233, s[8:9] offset:2816 sc1\n"
"global_load_dword v108, v233, s[8:9] offset:3072 sc1\n"
"global_load_dword v109, v233, s[8:9] offset:3328 sc1\n"
"global_load_dword v110, v233, s[8:9] offset:3584 sc1\n"
"global_load_dword v111, v233, s[8:9] offset:3840 sc1\n"
"v_mov_b32 v186, 0\n"
"v_mov_b32 v187, 0\n"
"s_waitcnt vmcnt(48)\n"
"v_dot2c_f32_bf16 v186, v56, v189\n"
"v_dot2c_f32_bf16 v187, v56, v56\n"
"v_dot2c_f32_bf16 v186, v57, v189\n"
"v_dot2c_f32_bf16 v187, v57, v57\n"
"v_dot2c_f32_bf16 v186, v58, v189\n"
"v_dot2c_f32_bf16 v187, v58, v58\n"
"v_dot2c_f32_bf16 v186, v59, v189\n"
"v_dot2c_f32_bf16 v187, v59, v59\n"
"v_dot2c_f32_bf16 v186, v60, v189\n"
"v_dot2c_f32_bf16 v187, v60, v60\n"
"v_dot2c_f32_bf16 v186, v61, v189\n"
"v_dot2c_f32_bf16 v187, v61, v61\n"
"v_dot2c_f32_bf16 v186, v62, v189\n"
"v_dot2c_f32_bf16 v187, v62, v62\n"
"v_dot2c_f32_bf16 v186, v63, v189\n"
"v_dot2c_f32_bf16 v187, v63, v63\n"
"s_nop 2\n"
"v_cmp_u_f32 vcc, v187, v187\n"
"s_nop 4\n"
"s_cbranch_vccnz Lx5_%=\n"
"s_nop 4\n"
"s_branch Lxr5_%=\n"
"Lx6_%=:\n"
"s_nop 4\n"
"v_max3_u32 v255, v64, v65, v66\n"
"v_max3_u32 v255, v255, v67, v68\n"
"v_max3_u32 v255, v255, v69, v70\n"
"v_max_u32 v255, v255, v71\n"
"v_cmp_eq_u32 vcc, -1, v255\n"
"s_nop 4\n"
"s_cbranch_vccz Lxr6_%=\n"
"s_sub_u32 s86, s86, 1\n"
"s_cbranch_scc1 Lxr6_%=\n"
"global_load_dword v64, v233, s[6:7] offset:0 sc1\n"
"global_load_dword v65, v233, s[6:7] offset:256 sc1\n"
"global_load_dword v66, v233, s[6:7] offset:512 sc1\n"
"global_load_dword v67, v233, s[6:7] offset:768 sc1\n"
"global_load_dword v68, v233, s[6:7] offset:1024 sc1\n"
"global_load_dword v69, v233, s[6:7] offset:1280 sc1\n"
"global_load_dword v70, v233, s[6:7] offset:1536 sc1\n"
"global_load_dword v71, v233, s[6:7] offset:1792 sc1\n"
"global_load_dword v72, v233, s[6:7] offset:2048 sc1\n"
"global_load_dword v73, v233, s[6:7] offset:2304 sc1\n"
"global_load_dword v74, v233, s[6:7] offset:2560 sc1\n"
"global_load_dword v75, v233, s[6:7] offset:2816 sc1\n"
"global_load_dword v76, v233, s[6:7] offset:3072 sc1\n"
"global_load_dword v77, v233, s[6:7] offset:3328 sc1\n"
"global_load_dword v78, v233, s[6:7] offset:3584 sc1\n"
"global_load_dword v79, v233, s[6:7] offset:3840 sc1\n"
"global_load_dword v80, v233, s[8:9] offset:-4096 sc1\n"
"global_load_dword v81, v233, s[8:9] offset:-3840 sc1\n"
"global_load_dword v82, v233, s[8:9] offset:-3584 sc1\n"
"global_load_dword v83, v233, s[8:9] offset:-3328 sc1\n"
"global_load_dword v84, v233, s[8:9] offset:-3072 sc1\n"
"global_load_dword v85, v233, s[8:9] offset:-2816 sc1\n"
"global_load_dword v86, v233, s[8:9] offset:-2560 sc1\n"
"global_load_dword v87, v233, s[8:9] offset:-2304 sc1\n"
"global_load_dword v88, v233, s[8:9] offset:-2048 sc1\n"
"global_load_dword v89, v233, s[8:9] offset:-1792 sc1\n"
"global_load_dword v90, v233, s[8:9] offset:-1536 sc1\n"
"global_load_dword v91, v233, s[8:9] offset:-1280 sc1\n"
"global_load_dword v92, v233, s[8:9] offset:-1024 sc1\n"
"global_load_dword v93, v233, s[8:9] offset:-768 sc1\n"
"global_load_dword v94, v233, s[8:9] offset:-512 sc1\n"
"global_load_dword v95, v233, s[8:9] offset:-256 sc1\n"
"global_load_dword v96, v233, s[8:9] offset:0 sc1\n"
"global_load_dword v97, v233, s[8:9] offset:256 sc1\n"
"global_load_dword v98, v233, s[8:9] offset:512 sc1\n"
"global_load_dword v99, v233, s[8:9] offset:768 sc1\n"
"global_load_dword v100, v233, s[8:9] offset:1024 sc1\n"
"global_load_dword v101, v233, s[8:9] offset:1280 sc1\n"
"global_load_dword v102, v233, s[8:9] offset:1536 sc1\n"
"global_load_dword v103, v233, s[8:9] offset:1792 sc1\n"
"global_load_dword v104, v233, s[8:9] offset:2048 sc1\n"
"global_load_dword v105, v233, s[8:9] offset:2304 sc1\n"
"global_load_dword v106, v233, s[8:9] offset:2560 sc1\n"
"global_load_dword v107, v233, s[8:9] offset:2816 sc1\n"
"global_load_dword v108, v233, s[8:9] offset:3072 sc1\n"
"global_load_dword v109, v233, s[8:9] offset:3328 sc1\n"
"global_load_dword v110, v233, s[8:9] offset:3584 sc1\n"
"global_load_dword v111, v233, s[8:9] offset:3840 sc1\n"
"global_load_dword v112, v233, s[10:11] offset:-4096 sc1\n"
"global_load_dword v113, v233, s[10:11] offset:-3840 sc1\n"
"global_load_dword v114, v233, s[10:11] offset:-3584 sc1\n"
"global_load_dword v115, v233, s[10:11] offset:-3328 sc1\n"
"global_load_dword v116, v233, s[10:11] offset:-3072 sc1\n"
"global_load_dword v117, v233, s[10:11] offset:-2816 sc1\n"
"global_load_dword v118, v233, s[10:11] offset:-2560 sc1\n"
"global_load_dword v119, v233, s[10:11] offset:-2304 sc1\n"
"v_mov_b32 v186, 0\n"
"v_mov_b32 v187, 0\n"
"s_waitcnt vmcnt(48)\n"
"v_dot2c_f32_bf16 v186, v64, v189\n"
"v_dot2c_f32_bf16 v187, v64, v64\n"
"v_dot2c_f32_bf16 v186, v65, v189\n"
"v_dot2c_f32_bf16 v187, v65, v65\n"
"v_dot2c_f32_bf16 v186, v66, v189\n"
"v_dot2c_f32_bf16 v187, v66, v66\n"
"v_dot2c_f32_bf16 v186, v67, v189\n"
"v_dot2c_f32_bf16 v187, v67, v67\n"
"v_dot2c_f32_bf16 v186, v68, v189\n"
"v_dot2c_f32_bf16 v187, v68, v68\n"
"v_dot2c_f32_bf16 v186, v69, v189\n"
"v_dot2c_f32_bf16 v187, v69, v69\n"
"v_dot2c_f32_bf16 v186, v70, v189\n"
"v_dot2c_f32_bf16 v187, v70, v70\n"
"v_dot2c_f32_bf16 v186, v71, v189\n"
"v_dot2c_f32_bf16 v187, v71, v71\n"
"s_nop 2\n"
"v_cmp_u_f32 vcc, v187, v187\n"
"s_nop 4\n"
"s_cbranch_vccnz Lx6_%=\n"
"s_nop 4\n"
"s_branch Lxr6_%=\n"
"Lx7_%=:\n"
"s_nop 4\n"
"v_max3_u32 v255, v72, v73, v74\n"
"v_max3_u32 v255, v255, v75, v76\n"
"v_max3_u32 v255, v255, v77, v78\n"
"v_max_u32 v255, v255, v79\n"
"v_cmp_eq_u32 vcc, -1, v255\n"
"s_nop 4\n"
"s_cbranch_vccz Lxr7_%=\n"
"s_sub_u32 s86, s86, 1\n"
"s_cbranch_scc1 Lxr7_%=\n"
"global_load_dword v72, v233, s[6:7] offset:2048 sc1\n"
"global_load_dword v73, v233, s[6:7] offset:2304 sc1\n"
"global_load_dword v74, v233, s[6:7] offset:2560 sc1\n"
"global_load_dword v75, v233, s[6:7] offset:2816 sc1\n"
"global_load_dword v76, v233, s[6:7] offset:3072 sc1\n"
"global_load_dword v77, v233, s[6:7] offset:3328 sc1\n"
"global_load_dword v78, v233, s[6:7] offset:3584 sc1\n"
"global_load_dword v79, v233, s[6:7] offset:3840 sc1\n"
"global_load_dword v80, v233, s[8:9] offset:-4096 sc1\n"
"global_load_dword v81, v233, s[8:9] offset:-3840 sc1\n"
"global_load_dword v82, v233, s[8:9] offset:-3584 sc1\n"
"global_load_dword v83, v233, s[8:9] offset:-3328 sc1\n"
"global_load_dword v84, v233, s[8:9] offset:-3072 sc1\n"
"global_load_dword v85, v233, s[8:9] offset:-2816 sc1\n"
"global_load_dword v86, v233, s[8:9] offset:-2560 sc1\n"
"global_load_dword v87, v233, s[8:9] offset:-2304 sc1\n"
"global_load_dword v88, v233, s[8:9] offset:-2048 sc1\n"
"global_load_dword v89, v233, s[8:9] offset:-1792 sc1\n"
"global_load_dword v90, v233, s[8:9] offset:-1536 sc1\n"
"global_load_dword v91, v233, s[8:9] offset:-1280 sc1\n"
"global_load_dword v92, v233, s[8:9] offset:-1024 sc1\n"
"global_load_dword v93, v233, s[8:9] offset:-768 sc1\n"
"global_load_dword v94, v233, s[8:9] offset:-512 sc1\n"
"global_load_dword v95, v233, s[8:9] offset:-256 sc1\n"
"global_load_dword v96, v233, s[8:9] offset:0 sc1\n"
"global_load_dword v97, v233, s[8:9] offset:256 sc1\n"
"global_load_dword v98, v233, s[8:9] offset:512 sc1\n"
"global_load_dword v99, v233, s[8:9] offset:768 sc1\n"
"global_load_dword v100, v233, s[8:9] offset:1024 sc1\n"
"global_load_dword v101, v233, s[8:9] offset:1280 sc1\n"
"global_load_dword v102, v233, s[8:9] offset:1536 sc1\n"
"global_load_dword v103, v233, s[8:9] offset:1792 sc1\n"
"global_load_dword v104, v233, s[8:9] offset:2048 sc1\n"
"global_load_dword v105, v233, s[8:9] offset:2304 sc1\n"
"global_load_dword v106, v233, s[8:9] offset:2560 sc1\n"
"global_load_dword v107, v233, s[8:9] offset:2816 sc1\n"
"global_load_dword v108, v233, s[8:9] offset:3072 sc1\n"
"global_load_dword v109, v233, s[8:9] offset:3328 sc1\n"
"global_load_dword v110, v233, s[8:9] offset:3584 sc1\n"
"global_load_dword v111, v233, s[8:9] offset:3840 sc1\n"
"global_load_dword v112, v233, s[10:11] offset:-4096 sc1\n"
"global_load_dword v113, v233, s[10:11] offset:-3840 sc1\n"
"global_load_dword v114, v233, s[10:11] offset:-3584 sc1\n"
"global_load_dword v115, v233, s[10:11] offset:-3328 sc1\n"
"global_load_dword v116, v233, s[10:11] offset:-3072 sc1\n"
"global_load_dword v117, v233, s[10:11] offset:-2816 sc1\n"
"global_load_dword v118, v233, s[10:11] offset:-2560 sc1\n"
"global_load_dword v119, v233, s[10:11] offset:-2304 sc1\n"
"global_load_dword v120, v233, s[10:11] offset:-2048 sc1\n"
"global_load_dword v121, v233, s[10:11] offset:-1792 sc1\n"
"global_load_dword v122, v233, s[10:11] offset:-1536 sc1\n"
"global_load_dword v123, v233, s[10:11] offset:-1280 sc1\n"
"global_load_dword v124, v233, s[10:11] offset:-1024 sc1\n"
"global_load_dword v125, v233, s[10:11] offset:-768 sc1\n"
"global_load_dword v126, v233, s[10:11] offset:-512 sc1\n"
"global_load_dword v127, v233, s[10:11] offset:-256 sc1\n"
"v_mov_b32 v186, 0\n"
"v_mov_b32 v187, 0\n"
"s_waitcnt vmcnt(48)\n"
"v_dot2c_f32_bf16 v186, v72, v189\n"
"v_dot2c_f32_bf16 v187, v72, v72\n"
"v_dot2c_f32_bf16 v186, v73, v189\n"
"v_dot2c_f32_bf16 v187, v73, v73\n"
"v_dot2c_f32_bf16 v186, v74, v189\n"
"v_dot2c_f32_bf16 v187, v74, v74\n"
"v_dot2c_f32_bf16 v186, v75, v189\n"
"v_dot2c_f32_bf16 v187, v75, v75\n"
"v_dot2c_f32_bf16 v186, v76, v189\n"
"v_dot2c_f32_bf16 v187, v76, v76\n"
"v_dot2c_f32_bf16 v186, v77, v189\n"
"v_dot2c_f32_bf16 v187, v77, v77\n"
"v_dot2c_f32_bf16 v186, v78, v189\n"
"v_dot2c_f32_bf16 v187, v78, v78\n"
"v_dot2c_f32_bf16 v186, v79, v189\n"
"v_dot2c_f32_bf16 v187, v79, v79\n"
"s_nop 2\n"
"v_cmp_u_f32 vcc, v187, v187\n"
"s_nop 4\n"
"s_cbranch_vccnz Lx7_%=\n"
"s_nop 4\n"
"s_branch Lxr7_%=\n"
"Lx8_%=:\n"
"s_nop 4\n"
"v_max3_u32 v255, v80, v81, v82\n"
"v_max3_u32 v255, v255, v83, v84\n"
"v_max3_u32 v255, v255, v85, v86\n"
"v_max_u32 v255, v255, v87\n"
"v_cmp_eq_u32 vcc, -1, v255\n"
"s_nop 4\n"
"s_cbranch_vccz Lxr8_%=\n"
"s_sub_u32 s86, s86, 1\n"
"s_cbranch_scc1 Lxr8_%=\n"
"global_load_dword v80, v233, s[8:9] offset:-4096 sc1\n"
"global_load_dword v81, v233, s[8:9] offset:-3840 sc1\n"
"global_load_dword v82, v233, s[8:9] offset:-3584 sc1\n"
"global_load_dword v83, v233, s[8:9] offset:-3328 sc1\n"
"global_load_dword v84, v233, s[8:9] offset:-3072 sc1\n"
"global_load_dword v85, v233, s[8:9] offset:-2816 sc1\n"
"global_load_dword v86, v233, s[8:9] offset:-2560 sc1\n"
"global_load_dword v87, v233, s[8:9] offset:-2304 sc1\n"
"global_load_dword v88, v233, s[8:9] offset:-2048 sc1\n"
"global_load_dword v89, v233, s[8:9] offset:-1792 sc1\n"
"global_load_dword v90, v233, s[8:9] offset:-1536 sc1\n"
"global_load_dword v91, v233, s[8:9] offset:-1280 sc1\n"
"global_load_dword v92, v233, s[8:9] offset:-1024 sc1\n"
"global_load_dword v93, v233, s[8:9] offset:-768 sc1\n"
"global_load_dword v94, v233, s[8:9] offset:-512 sc1\n"
"global_load_dword v95, v233, s[8:9] offset:-256 sc1\n"
"global_load_dword v96, v233, s[8:9] offset:0 sc1\n"
"global_load_dword v97, v233, s[8:9] offset:256 sc1\n"
"global_load_dword v98, v233, s[8:9] offset:512 sc1\n"
"global_load_dword v99, v233, s[8:9] offset:768 sc1\n"
"global_load_dword v100, v233, s[8:9] offset:1024 sc1\n"
"global_load_dword v101, v233, s[8:9] offset:1280 sc1\n"
"global_load_dword v102, v233, s[8:9] offset:1536 sc1\n"
"global_load_dword v103, v233, s[8:9] offset:1792 sc1\n"
"global_load_dword v104, v233, s[8:9] offset:2048 sc1\n"
"global_load_dword v105, v233, s[8:9] offset:2304 sc1\n"
"global_load_dword v106, v233, s[8:9] offset:2560 sc1\n"
"global_load_dword v107, v233, s[8:9] offset:2816 sc1\n"
"global_load_dword v108, v233, s[8:9] offset:3072 sc1\n"
"global_load_dword v109, v233, s[8:9] offset:3328 sc1\n"
"global_load_dword v110, v233, s[8:9] offset:3584 sc1\n"
"global_load_dword v111, v233, s[8:9] offset:3840 sc1\n"
"global_load_dword v112, v233, s[10:11] offset:-4096 sc1\n"
"global_load_dword v113, v233, s[10:11] offset:-3840 sc1\n"
"global_load_dword v114, v233, s[10:11] offset:-3584 sc1\n"
"global_load_dword v115, v233, s[10:11] offset:-3328 sc1\n"
"global_load_dword v116, v233, s[10:11] offset:-3072 sc1\n"
"global_load_dword v117, v233, s[10:11] offset:-2816 sc1\n"
"global_load_dword v118, v233, s[10:11] offset:-2560 sc1\n"
"global_load_dword v119, v233, s[10:11] offset:-2304 sc1\n"
"global_load_dword v120, v233, s[10:11] offset:-2048 sc1\n"
"global_load_dword v121, v233, s[10:11] offset:-1792 sc1\n"
"global_load_dword v122, v233, s[10:11] offset:-1536 sc1\n"
"global_load_dword v123, v233, s[10:11] offset:-1280 sc1\n"
"global_load_dword v124, v233, s[10:11] offset:-1024 sc1\n"
"global_load_dword v125, v233, s[10:11] offset:-768 sc1\n"
"global_load_dword v126, v233, s[10:11] offset:-512 sc1\n"
"global_load_dword v127, v233, s[10:11] offset:-256 sc1\n"
"v_mov_b32 v186, 0\n"
"v_mov_b32 v187, 0\n"
"s_waitcnt vmcnt(0)\n"
"v_dot2c_f32_bf16 v186, v80, v189\n"
"v_dot2c_f32_bf16 v187, v80, v80\n"
"v_dot2c_f32_bf16 v186, v81, v189\n"
"v_dot2c_f32_bf16 v187, v81, v81\n"
"v_dot2c_f32_bf16 v186, v82, v189\n"
"v_dot2c_f32_bf16 v187, v82, v82\n"
"v_dot2c_f32_bf16 v186, v83, v189\n"
"v_dot2c_f32_bf16 v187, v83, v83\n"
"v_dot2c_f32_bf16 v186, v84, v189\n"
"v_dot2c_f32_bf16 v187, v84, v84\n"
"v_dot2c_f32_bf16 v186, v85, v189\n"
"v_dot2c_f32_bf16 v187, v85, v85\n"
"v_dot2c_f32_bf16 v186, v86, v189\n"
"v_dot2c_f32_bf16 v187, v86, v86\n"
"v_dot2c_f32_bf16 v186, v87, v189\n"
"v_dot2c_f32_bf16 v187, v87, v87\n"
"s_nop 2\n"
"v_cmp_u_f32 vcc, v187, v187\n"
"s_nop 4\n"
"s_cbranch_vccnz Lx8_%=\n"
"s_nop 4\n"
"s_branch Lxr8_%=\n"
"Lx9_%=:\n"
"s_nop 4\n"
"v_max3_u32 v255, v88, v89, v90\n"
"v_max3_u32 v255, v255, v91, v92\n"
"v_max3_u32 v255, v255, v93, v94\n"
"v_max_u32 v255, v255, v95\n"
"v_cmp_eq_u32 vcc, -1, v255\n"
"s_nop 4\n"
"s_cbranch_vccz Lxr9_%=\n"
"s_sub_u32 s86, s86, 1\n"
"s_cbranch_scc1 Lxr9_%=\n"
"global_load_dword v88, v233, s[8:9] offset:-2048 sc1\n"
"global_load_dword v89, v233, s[8:9] offset:-1792 sc1\n"
"global_load_dword v90, v233, s[8:9] offset:-1536 sc1\n"
"global_load_dword v91, v233, s[8:9] offset:-1280 sc1\n"
"global_load_dword v92, v233, s[8:9] offset:-1024 sc1\n"
"global_load_dword v93, v233, s[8:9] offset:-768 sc1\n"
"global_load_dword v94, v233, s[8:9] offset:-512 sc1\n"
"global_load_dword v95, v233, s[8:9] offset:-256 sc1\n"
"global_load_dword v96, v233, s[8:9] offset:0 sc1\n"
"global_load_dword v97, v233, s[8:9] offset:256 sc1\n"
"global_load_dword v98, v233, s[8:9] offset:512 sc1\n"
"global_load_dword v99, v233, s[8:9] offset:768 sc1\n"
"global_load_dword v100, v233, s[8:9] offset:1024 sc1\n"
"global_load_dword v101, v233, s[8:9] offset:1280 sc1\n"
"global_load_dword v102, v233, s[8:9] offset:1536 sc1\n"
"global_load_dword v103, v233, s[8:9] offset:1792 sc1\n"
"global_load_dword v104, v233, s[8:9] offset:2048 sc1\n"
"global_load_dword v105, v233, s[8:9] offset:2304 sc1\n"
"global_load_dword v106, v233, s[8:9] offset:2560 sc1\n"
"global_load_dword v107, v233, s[8:9] offset:2816 sc1\n"
"global_load_dword v108, v233, s[8:9] offset:3072 sc1\n"
"global_load_dword v109, v233, s[8:9] offset:3328 sc1\n"
"global_load_dword v110, v233, s[8:9] offset:3584 sc1\n"
"global_load_dword v111, v233, s[8:9] offset:3840 sc1\n"
"global_load_dword v112, v233, s[10:11] offset:-4096 sc1\n"
"global_load_dword v113, v233, s[10:11] offset:-3840 sc1\n"
"global_load_dword v114, v233, s[10:11] offset:-3584 sc1\n"
"global_load_dword v115, v233, s[10:11] offset:-3328 sc1\n"
"global_load_dword v116, v233, s[10:11] offset:-3072 sc1\n"
"global_load_dword v117, v233, s[10:11] offset:-2816 sc1\n"
"global_load_dword v118, v233, s[10:11] offset:-2560 sc1\n"
"global_load_dword v119, v233, s[10:11] offset:-2304 sc1\n"
"global_load_dword v120, v233, s[10:11] offset:-2048 sc1\n"
"global_load_dword v121, v233, s[10:11] offset:-1792 sc1\n"
"global_load_dword v122, v233, s[10:11] offset:-1536 sc1\n"
"global_load_dword v123, v233, s[10:11] offset:-1280 sc1\n"
"global_load_dword v124, v233, s[10:11] offset:-1024 sc1\n"
"global_load_dword v125, v233, s[10:11] offset:-768 sc1\n"
"global_load_dword v126, v233, s[10:11] offset:-512 sc1\n"
"global_load_dword v127, v233, s[10:11] offset:-256 sc1\n"
"v_mov_b32 v186, 0\n"
"v_mov_b32 v187, 0\n"
"s_waitcnt vmcnt(0)\n"
"v_dot2c_f32_bf16 v186, v88, v189\n"
"v_dot2c_f32_bf16 v187, v88, v88\n"
"v_dot2c_f32_bf16 v186, v89, v189\n"
"v_dot2c_f32_bf16 v187, v89, v89\n"
"v_dot2c_f32_bf16 v186, v90, v189\n"
"v_dot2c_f32_bf16 v187, v90, v90\n"
"v_dot2c_f32_bf16 v186, v91, v189\n"
"v_dot2c_f32_bf16 v187, v91, v91\n"
"v_dot2c_f32_bf16 v186, v92, v189\n"
"v_dot2c_f32_bf16 v187, v92, v92\n"
"v_dot2c_f32_bf16 v186, v93, v189\n"
"v_dot2c_f32_bf16 v187, v93, v93\n"
"v_dot2c_f32_bf16 v186, v94, v189\n"
"v_dot2c_f32_bf16 v187, v94, v94\n"
"v_dot2c_f32_bf16 v186, v95, v189\n"
"v_dot2c_f32_bf16 v187, v95, v95\n"
"s_nop 2\n"
"v_cmp_u_f32 vcc, v187, v187\n"
"s_nop 4\n"
"s_cbranch_vccnz Lx9_%=\n"
"s_nop 4\n"
"s_branch Lxr9_%=\n"
"Lx10_%=:\n"
"s_nop 4\n"
"v_max3_u32 v255, v96, v97, v98\n"
"v_max3_u32 v255, v255, v99, v100\n"
"v_max3_u32 v255, v255, v101, v102\n"
"v_max_u32 v255, v255, v103\n"
"v_cmp_eq_u32 vcc, -1, v255\n"
"s_nop 4\n"
"s_cbranch_vccz Lxr10_%=\n"
"s_sub_u32 s86, s86, 1\n"
"s_cbranch_scc1 Lxr10_%=\n"
"global_load_dword v96, v233, s[8:9] offset:0 sc1\n"
"global_load_dword v97, v233, s[8:9] offset:256 sc1\n"
"global_load_dword v98, v233, s[8:9] offset:512 sc1\n"
"global_load_dword v99, v233, s[8:9] offset:768 sc1\n"
"global_load_dword v100, v233, s[8:9] offset:1024 sc1\n"
"global_load_dword v101, v233, s[8:9] offset:1280 sc1\n"
"global_load_dword v102, v233, s[8:9] offset:1536 sc1\n"
"global_load_dword v103, v233, s[8:9] offset:1792 sc1\n"
"global_load_dword v104, v233, s[8:9] offset:2048 sc1\n"
"global_load_dword v105, v233, s[8:9] offset:2304 sc1\n"
"global_load_dword v106, v233, s[8:9] offset:2560 sc1\n"
"global_load_dword v107, v233, s[8:9] offset:2816 sc1\n"
"global_load_dword v108, v233, s[8:9] offset:3072 sc1\n"
"global_load_dword v109, v233, s[8:9] offset:3328 sc1\n"
"global_load_dword v110, v233, s[8:9] offset:3584 sc1\n"
"global_load_dword v111, v233, s[8:9] offset:3840 sc1\n"
"global_load_dword v112, v233, s[10:11] offset:-4096 sc1\n"
"global_load_dword v113, v233, s[10:11] offset:-3840 sc1\n"
"global_load_dword v114, v233, s[10:11] offset:-3584 sc1\n"
"global_load_dword v115, v233, s[10:11] offset:-3328 sc1\n"
"global_load_dword v116, v233, s[10:11] offset:-3072 sc1\n"
"global_load_dword v117, v233, s[10:11] offset:-2816 sc1\n"
"global_load_dword v118, v233, s[10:11] offset:-2560 sc1\n"
"global_load_dword v119, v233, s[10:11] offset:-2304 sc1\n"
"global_load_dword v120, v233, s[10:11] offset:-2048 sc1\n"
"global_load_dword v121, v233, s[10:11] offset:-1792 sc1\n"
"global_load_dword v122, v233, s[10:11] offset:-1536 sc1\n"
"global_load_dword v123, v233, s[10:11] offset:-1280 sc1\n"
"global_load_dword v124, v233, s[10:11] offset:-1024 sc1\n"
"global_load_dword v125, v233, s[10:11] offset:-768 sc1\n"
"global_load_dword v126, v233, s[10:11] offset:-512 sc1\n"
"global_load_dword v127, v233, s[10:11] offset:-256 sc1\n"
"v_mov_b32 v186, 0\n"
"v_mov_b32 v187, 0\n"
"s_waitcnt vmcnt(0)\n"
"v_dot2c_f32_bf16 v186, v96, v189\n"
"v_dot2c_f32_bf16 v187, v96, v96\n"
"v_dot2c_f32_bf16 v186, v97, v189\n"
"v_dot2c_f32_bf16 v187, v97, v97\n"
"v_dot2c_f32_bf16 v186, v98, v189\n"
"v_dot2c_f32_bf16 v187, v98, v98\n"
"v_dot2c_f32_bf16 v186, v99, v189\n"
"v_dot2c_f32_bf16 v187, v99, v99\n"
"v_dot2c_f32_bf16 v186, v100, v189\n"
"v_dot2c_f32_bf16 v187, v100, v100\n"
"v_dot2c_f32_bf16 v186, v101, v189\n"
"v_dot2c_f32_bf16 v187, v101, v101\n"
"v_dot2c_f32_bf16 v186, v102, v189\n"
"v_dot2c_f32_bf16 v187, v102, v102\n"
"v_dot2c_f32_bf16 v186, v103, v189\n"
"v_dot2c_f32_bf16 v187, v103, v103\n"
"s_nop 2\n"
"v_cmp_u_f32 vcc, v187, v187\n"
"s_nop 4\n"
"s_cbranch_vccnz Lx10_%=\n"
"s_nop 4\n"
"s_branch Lxr10_%=\n"
"Lx11_%=:\n"
"s_nop 4\n"
"v_max3_u32 v255, v104, v105, v106\n"
"v_max3_u32 v255, v255, v107, v108\n"
"v_max3_u32 v255, v255, v109, v110\n"
"v_max_u32 v255, v255, v111\n"
"v_cmp_eq_u32 vcc, -1, v255\n"
"s_nop 4\n"
"s_cbranch_vccz Lxr11_%=\n"
"s_sub_u32 s86, s86, 1\n"
"s_cbranch_scc1 Lxr11_%=\n"
"global_load_dword v104, v233, s[8:9] offset:2048 sc1\n"
"global_load_dword v105, v233, s[8:9] offset:2304 sc1\n"
"global_load_dword v106, v233, s[8:9] offset:2560 sc1\n"
"global_load_dword v107, v233, s[8:9] offset:2816 sc1\n"
"global_load_dword v108, v233, s[8:9] offset:3072 sc1\n"
"global_load_dword v109, v233, s[8:9] offset:3328 sc1\n"
"global_load_dword v110, v233, s[8:9] offset:3584 sc1\n"
"global_load_dword v111, v233, s[8:9] offset:3840 sc1\n"
"global_load_dword v112, v233, s[10:11] offset:-4096 sc1\n"
"global_load_dword v113, v233, s[10:11] offset:-3840 sc1\n"
"global_load_dword v114, v233, s[10:11] offset:-3584 sc1\n"
"global_load_dword v115, v233, s[10:11] offset:-3328 sc1\n"
"global_load_dword v116, v233, s[10:11] offset:-3072 sc1\n"
"global_load_dword v117, v233, s[10:11] offset:-2816 sc1\n"
"global_load_dword v118, v233, s[10:11] offset:-2560 sc1\n"
"global_load_dword v119, v233, s[10:11] offset:-2304 sc1\n"
"global_load_dword v120, v233, s[10:11] offset:-2048 sc1\n"
"global_load_dword v121, v233, s[10:11] offset:-1792 sc1\n"
"global_load_dword v122, v233, s[10:11] offset:-1536 sc1\n"
"global_load_dword v123, v233, s[10:11] offset:-1280 sc1\n"
"global_load_dword v124, v233, s[10:11] offset:-1024 sc1\n"
"global_load_dword v125, v233, s[10:11] offset:-768 sc1\n"
"global_load_dword v126, v233, s[10:11] offset:-512 sc1\n"
"global_load_dword v127, v233, s[10:11] offset:-256 sc1\n"
"v_mov_b32 v186, 0\n"
"v_mov_b32 v187, 0\n"
"s_waitcnt vmcnt(0)\n"
"v_dot2c_f32_bf16 v186, v104, v189\n"
"v_dot2c_f32_bf16 v187, v104, v104\n"
"v_dot2c_f32_bf16 v186, v105, v189\n"
"v_dot2c_f32_bf16 v187, v105, v105\n"
"v_dot2c_f32_bf16 v186, v106, v189\n"
"v_dot2c_f32_bf16 v187, v106, v106\n"
"v_dot2c_f32_bf16 v186, v107, v189\n"
"v_dot2c_f32_bf16 v187, v107, v107\n"
"v_dot2c_f32_bf16 v186, v108, v189\n"
"v_dot2c_f32_bf16 v187, v108, v108\n"
"v_dot2c_f32_bf16 v186, v109, v189\n"
"v_dot2c_f32_bf16 v187, v109, v109\n"
"v_dot2c_f32_bf16 v186, v110, v189\n"
"v_dot2c_f32_bf16 v187, v110, v110\n"
"v_dot2c_f32_bf16 v186, v111, v189\n"
"v_dot2c_f32_bf16 v187, v111, v111\n"
"s_nop 2\n"
"v_cmp_u_f32 vcc, v187, v187\n"
"s_nop 4\n"
"s_cbranch_vccnz Lx11_%=\n"
"s_nop 4\n"
"s_branch Lxr11_%=\n"
"Lx12_%=:\n"
"s_nop 4\n"
"v_max3_u32 v255, v112, v113, v114\n"
"v_max3_u32 v255, v255, v115, v116\n"
"v_max3_u32 v255, v255, v117, v118\n"
"v_max_u32 v255, v255, v119\n"
"v_cmp_eq_u32 vcc, -1, v255\n"
"s_nop 4\n"
"s_cbranch_vccz Lxr12_%=\n"
"s_sub_u32 s86, s86, 1\n"
"s_cbranch_scc1 Lxr12_%=\n"
"global_load_dword v112, v233, s[10:11] offset:-4096 sc1\n"
"global_load_dword v113, v233, s[10:11] offset:-3840 sc1\n"
"global_load_dword v114, v233, s[10:11] offset:-3584 sc1\n"
"global_load_dword v115, v233, s[10:11] offset:-3328 sc1\n"
"global_load_dword v116, v233, s[10:11] offset:-3072 sc1\n"
"global_load_dword v117, v233, s[10:11] offset:-2816 sc1\n"
"global_load_dword v118, v233, s[10:11] offset:-2560 sc1\n"
"global_load_dword v119, v233, s[10:11] offset:-2304 sc1\n"
"global_load_dword v120, v233, s[10:11] offset:-2048 sc1\n"
"global_load_dword v121, v233, s[10:11] offset:-1792 sc1\n"
"global_load_dword v122, v233, s[10:11] offset:-1536 sc1\n"
"global_load_dword v123, v233, s[10:11] offset:-1280 sc1\n"
"global_load_dword v124, v233, s[10:11] offset:-1024 sc1\n"
"global_load_dword v125, v233, s[10:11] offset:-768 sc1\n"
"global_load_dword v126, v233, s[10:11] offset:-512 sc1\n"
"global_load_dword v127, v233, s[10:11] offset:-256 sc1\n"
"v_mov_b32 v186, 0\n"
"v_mov_b32 v187, 0\n"
"s_waitcnt vmcnt(0)\n"
"v_dot2c_f32_bf16 v186, v112, v189\n"
"v_dot2c_f32_bf16 v187, v112, v112\n"
"v_dot2c_f32_bf16 v186, v113, v189\n"
"v_dot2c_f32_bf16 v187, v113, v113\n"
"v_dot2c_f32_bf16 v186, v114, v189\n"
"v_dot2c_f32_bf16 v187, v114, v114\n"
"v_dot2c_f32_bf16 v186, v115, v189\n"
"v_dot2c_f32_bf16 v187, v115, v115\n"
"v_dot2c_f32_bf16 v186, v116, v189\n"
"v_dot2c_f32_bf16 v187, v116, v116\n"
"v_dot2c_f32_bf16 v186, v117, v189\n"
"v_dot2c_f32_bf16 v187, v117, v117\n"
"v_dot2c_f32_bf16 v186, v118, v189\n"
"v_dot2c_f32_bf16 v187, v118, v118\n"
"v_dot2c_f32_bf16 v186, v119, v189\n"
"v_dot2c_f32_bf16 v187, v119, v119\n"
"s_nop 2\n"
"v_cmp_u_f32 vcc, v187, v187\n"
"s_nop 4\n"
"s_cbranch_vccnz Lx12_%=\n"
"s_nop 4\n"
"s_branch Lxr12_%=\n"
"Lx13_%=:\n"
"s_nop 4\n"
"v_max3_u32 v255, v120, v121, v122\n"
"v_max3_u32 v255, v255, v123, v124\n"
"v_max3_u32 v255, v255, v125, v126\n"
"v_max_u32 v255, v255, v127\n"
"v_cmp_eq_u32 vcc, -1, v255\n"
"s_nop 4\n"
"s_cbranch_vccz Lxr13_%=\n"
"s_sub_u32 s86, s86, 1\n"
"s_cbranch_scc1 Lxr13_%=\n"
"global_load_dword v120, v233, s[10:11] offset:-2048 sc1\n"
"global_load_dword v121, v233, s[10:11] offset:-1792 sc1\n"
"global_load_dword v122, v233, s[10:11] offset:-1536 sc1\n"
"global_load_dword v123, v233, s[10:11] offset:-1280 sc1\n"
"global_load_dword v124, v233, s[10:11] offset:-1024 sc1\n"
"global_load_dword v125, v233, s[10:11] offset:-768 sc1\n"
"global_load_dword v126, v233, s[10:11] offset:-512 sc1\n"
"global_load_dword v127, v233, s[10:11] offset:-256 sc1\n"
"v_mov_b32 v186, 0\n"
"v_mov_b32 v187, 0\n"
"s_waitcnt vmcnt(0)\n"
"v_dot2c_f32_bf16 v186, v120, v189\n"
"v_dot2c_f32_bf16 v187, v120, v120\n"
"v_dot2c_f32_bf16 v186, v121, v189\n"
"v_dot2c_f32_bf16 v187, v121, v121\n"
"v_dot2c_f32_bf16 v186, v122, v189\n"
"v_dot2c_f32_bf16 v187, v122, v122\n"
"v_dot2c_f32_bf16 v186, v123, v189\n"
"v_dot2c_f32_bf16 v187, v123, v123\n"
"v_dot2c_f32_bf16 v186, v124, v189\n"
"v_dot2c_f32_bf16 v187, v124, v124\n"
"v_dot2c_f32_bf16 v186, v125, v189\n"
"v_dot2c_f32_bf16 v187, v125, v125\n"
"v_dot2c_f32_bf16 v186, v126, v189\n"
"v_dot2c_f32_bf16 v187, v126, v126\n"
"v_dot2c_f32_bf16 v186, v127, v189\n"
"v_dot2c_f32_bf16 v187, v127, v127\n"
"s_nop 2\n"
"v_cmp_u_f32 vcc, v187, v187\n"
"s_nop 4\n"
"s_cbranch_vccnz Lx13_%=\n"
"s_nop 4\n"
"s_branch Lxr13_%=\n"
"Lh0_%=:\n"
"s_sub_u32 s86, s86, 1\n"
"s_cbranch_scc1 Lhr0_%=\n"
"buffer_load_dwordx4 v[208:211], v246, s[48:51], s72 offen sc1\n"
"buffer_load_dwordx4 v[212:215], v247, s[48:51], s72 offen sc1\n"
"buffer_load_dwordx4 v[216:219], v246, s[48:51], s73 offen sc1\n"
"buffer_load_dwordx4 v[220:223], v247, s[48:51], s73 offen sc1\n"
"buffer_load_dwordx4 v[0:3], v246, s[48:51], s74 offen sc1\n"
"buffer_load_dwordx4 v[4:7], v247, s[48:51], s74 offen sc1\n"
"buffer_load_dwordx4 v[8:11], v246, s[48:51], s75 offen sc1\n"
"buffer_load_dwordx4 v[12:15], v247, s[48:51], s75 offen sc1\n"
"s_waitcnt vmcnt(0)\n"
"v_max3_u32 v186, v208, v210, v212\n"
"v_max_u32 v186, v186, v214\n"
"v_cmp_eq_u32 vcc, -1, v186\n"
"s_nop 4\n"
"s_cbranch_vccnz Lh0_%=\n"
"s_nop 4\n"
"s_branch Lhr0_%=\n"
"Lgb_%=:\n"
"s_sub_u32 s86, s86, 1\n"
"s_cbranch_scc1 Lgbr_%=\n"
"buffer_load_dwordx2 v[198:199], v250, s[48:51], s89 offen sc1\n"
"s_waitcnt vmcnt(0)\n"
"v_cmp_ne_u32 vcc, s39, v199\n"
"s_nop 4\n"
"s_cbranch_vccnz Lgb_%=\n"
"s_branch Lgbr_%=\n"
"Lh1_%=:\n"
"s_sub_u32 s86, s86, 1\n"
"s_cbranch_scc1 Lhr1_%=\n"
"buffer_load_dwordx4 v[216:219], v246, s[48:51], s73 offen sc1\n"
"buffer_load_dwordx4 v[220:223], v247, s[48:51], s73 offen sc1\n"
"buffer_load_dwordx4 v[0:3], v246, s[48:51], s74 offen sc1\n"
"buffer_load_dwordx4 v[4:7], v247, s[48:51], s74 offen sc1\n"
"buffer_load_dwordx4 v[8:11], v246, s[48:51], s75 offen sc1\n"
"buffer_load_dwordx4 v[12:15], v247, s[48:51], s75 offen sc1\n"
"buffer_load_dwordx4 v[80:83], v246, s[48:51], s76 offen sc1\n"
"buffer_load_dwordx4 v[84:87], v247, s[48:51], s76 offen sc1\n"
"buffer_load_dwordx4 v[88:91], v246, s[48:51], s77 offen sc1\n"
"buffer_load_dwordx4 v[92:95], v247, s[48:51], s77 offen sc1\n"
"s_waitcnt vmcnt(0)\n"
"v_max3_u32 v186, v216, v218, v220\n"
"v_max_u32 v186, v186, v222\n"
"v_cmp_eq_u32 vcc, -1, v186\n"
"s_nop 4\n"
"s_cbranch_vccnz Lh1_%=\n"
"s_nop 4\n"
"s_branch Lhr1_%=\n"
"Lh2_%=:\n"
"s_sub_u32 s86, s86, 1\n"
"s_cbranch_scc1 Lhr2_%=\n"
"buffer_load_dwordx4 v[0:3], v246, s[48:51], s74 offen sc1\n"
"buffer_load_dwordx4 v[4:7], v247, s[48:51], s74 offen sc1\n"
"buffer_load_dwordx4 v[8:11], v246, s[48:51], s75 offen sc1\n"
"buffer_load_dwordx4 v[12:15], v247, s[48:51], s75 offen sc1\n"
"buffer_load_dwordx4 v[80:83], v246, s[48:51], s76 offen sc1\n"
"buffer_load_dwordx4 v[84:87], v247, s[48:51], s76 offen sc1\n"
"buffer_load_dwordx4 v[88:91], v246, s[48:51], s77 offen sc1\n"
"buffer_load_dwordx4 v[92:95], v247, s[48:51], s77 offen sc1\n"
"buffer_load_dwordx4 v[96:99], v246, s[48:51], s78 offen sc1\n"
"buffer_load_dwordx4 v[100:103], v247, s[48:51], s78 offen sc1\n"
"buffer_load_dwordx4 v[104:107], v234, s[48:51], s72 offen sc1\n"
"buffer_load_dwordx4 v[108:111], v235, s[48:51], s72 offen sc1\n"
"s_waitcnt vmcnt(0)\n"
"v_max3_u32 v186, v0, v2, v4\n"
"v_max_u32 v186, v186, v6\n"
"v_cmp_eq_u32 vcc, -1, v186\n"
"s_nop 4\n"
"s_cbranch_vccnz Lh2_%=\n"
"s_nop 4\n"
"s_branch Lhr2_%=\n"
"Lh3_%=:\n"
"s_sub_u32 s86, s86, 1\n"
"s_cbranch_scc1 Lhr3_%=\n"
"buffer_load_dwordx4 v[8:11], v246, s[48:51], s75 offen sc1\n"
"buffer_load_dwordx4 v[12:15], v247, s[48:51], s75 offen sc1\n"
"buffer_load_dwordx4 v[80:83], v246, s[48:51], s76 offen sc1\n"
"buffer_load_dwordx4 v[84:87], v247, s[48:51], s76 offen sc1\n"
"buffer_load_dwordx4 v[88:91], v246, s[48:51], s77 offen sc1\n"
"buffer_load_dwordx4 v[92:95], v247, s[48:51], s77 offen sc1\n"
"buffer_load_dwordx4 v[96:99], v246, s[48:51], s78 offen sc1\n"
"buffer_load_dwordx4 v[100:103], v247, s[48:51], s78 offen sc1\n"
"buffer_load_dwordx4 v[104:107], v234, s[48:51], s72 offen sc1\n"
"buffer_load_dwordx4 v[108:111], v235, s[48:51], s72 offen sc1\n"
"buffer_load_dwordx4 v[112:115], v234, s[48:51], s73 offen sc1\n"
"buffer_load_dwordx4 v[116:119], v235, s[48:51], s73 offen sc1\n"
"buffer_load_dwordx4 v[120:123], v234, s[48:51], s74 offen sc1\n"
"buffer_load_dwordx4 v[124:127], v235, s[48:51], s74 offen sc1\n"
"s_waitcnt vmcnt(0)\n"
"v_max3_u32 v186, v8, v10, v12\n"
"v_max_u32 v186, v186, v14\n"
"v_cmp_eq_u32 vcc, -1, v186\n"
"s_nop 4\n"
"s_cbranch_vccnz Lh3_%=\n"
"s_nop 4\n"
"s_branch Lhr3_%=\n"
"Lh4_%=:\n"
"s_sub_u32 s86, s86, 1\n"
"s_cbranch_scc1 Lhr4_%=\n"
"buffer_load_dwordx4 v[80:83], v246, s[48:51], s76 offen sc1\n"
"buffer_load_dwordx4 v[84:87], v247, s[48:51], s76 offen sc1\n"
"buffer_load_dwordx4 v[88:91], v246, s[48:51], s77 offen sc1\n"
"buffer_load_dwordx4 v[92:95], v247, s[48:51], s77 offen sc1\n"
"buffer_load_dwordx4 v[96:99], v246, s[48:51], s78 offen sc1\n"
"buffer_load_dwordx4 v[100:103], v247, s[48:51], s78 offen sc1\n"
"buffer_load_dwordx4 v[104:107], v234, s[48:51], s72 offen sc1\n"
"buffer_load_dwordx4 v[108:111], v235, s[48:51], s72 offen sc1\n"
"buffer_load_dwordx4 v[112:115], v234, s[48:51], s73 offen sc1\n"
"buffer_load_dwordx4 v[116:119], v235, s[48:51], s73 offen sc1\n"
"buffer_load_dwordx4 v[120:123], v234, s[48:51], s74 offen sc1\n"
"buffer_load_dwordx4 v[124:127], v235, s[48:51], s74 offen sc1\n"
"buffer_load_dwordx4 v[160:163], v234, s[48:51], s75 offen sc1\n"
"buffer_load_dwordx4 v[164:167], v235, s[48:51], s75 offen sc1\n"
"buffer_load_dwordx4 v[168:171], v234, s[48:51], s76 offen sc1\n"
"buffer_load_dwordx4 v[172:175], v235, s[48:51], s76 offen sc1\n"
"s_waitcnt vmcnt(0)\n"
"v_max3_u32 v186, v80, v82, v84\n"
"v_max_u32 v186, v186, v86\n"
"v_cmp_eq_u32 vcc, -1, v186\n"
"s_nop 4\n"
"s_cbranch_vccnz Lh4_%=\n"
"s_nop 4\n"
"s_branch Lhr4_%=\n"
"Lh5_%=:\n"
"s_sub_u32 s86, s86, 1\n"
"s_cbranch_scc1 Lhr5_%=\n"
"buffer_load_dwordx4 v[88:91], v246, s[48:51], s77 offen sc1\n"
"buffer_load_dwordx4 v[92:95], v247, s[48:51], s77 offen sc1\n"
"buffer_load_dwordx4 v[96:99], v246, s[48:51], s78 offen sc1\n"
"buffer_load_dwordx4 v[100:103], v247, s[48:51], s78 offen sc1\n"
"buffer_load_dwordx4 v[104:107], v234, s[48:51], s72 offen sc1\n"
"buffer_load_dwordx4 v[108:111], v235, s[48:51], s72 offen sc1\n"
"buffer_load_dwordx4 v[112:115], v234, s[48:51], s73 offen sc1\n"
"buffer_load_dwordx4 v[116:119], v235, s[48:51], s73 offen sc1\n"
"buffer_load_dwordx4 v[120:123], v234, s[48:51], s74 offen sc1\n"
"buffer_load_dwordx4 v[124:127], v235, s[48:51], s74 offen sc1\n"
"buffer_load_dwordx4 v[160:163], v234, s[48:51], s75 offen sc1\n"
"buffer_load_dwordx4 v[164:167], v235, s[48:51], s75 offen sc1\n"
"buffer_load_dwordx4 v[168:171], v234, s[48:51], s76 offen sc1\n"
"buffer_load_dwordx4 v[172:175], v235, s[48:51], s76 offen sc1\n"
"buffer_load_dwordx4 v[176:179], v234, s[48:51], s77 offen sc1\n"
"buffer_load_dwordx4 v[180:183], v235, s[48:51], s77 offen sc1\n"
"buffer_load_dwordx4 v[128:131], v234, s[48:51], s78 offen sc1\n"
"buffer_load_dwordx4 v[132:135], v235, s[48:51], s78 offen sc1\n"
"s_waitcnt vmcnt(0)\n"
"v_max3_u32 v186, v88, v90, v92\n"
"v_max_u32 v186, v186, v94\n"
"v_cmp_eq_u32 vcc, -1, v186\n"
"s_nop 4\n"
"s_cbranch_vccnz Lh5_%=\n"
"s_nop 4\n"
"s_branch Lhr5_%=\n"
"Lh6_%=:\n"
"s_sub_u32 s86, s86, 1\n"
"s_cbranch_scc1 Lhr6_%=\n"
"buffer_load_dwordx4 v[96:99], v246, s[48:51], s78 offen sc1\n"
"buffer_load_dwordx4 v[100:103], v247, s[48:51], s78 offen sc1\n"
"buffer_load_dwordx4 v[104:107], v234, s[48:51], s72 offen sc1\n"
"buffer_load_dwordx4 v[108:111], v235, s[48:51], s72 offen sc1\n"
"buffer_load_dwordx4 v[112:115], v234, s[48:51], s73 offen sc1\n"
"buffer_load_dwordx4 v[116:119], v235, s[48:51], s73 offen sc1\n"
"buffer_load_dwordx4 v[120:123], v234, s[48:51], s74 offen sc1\n"
"buffer_load_dwordx4 v[124:127], v235, s[48:51], s74 offen sc1\n"
"buffer_load_dwordx4 v[160:163], v234, s[48:51], s75 offen sc1\n"
"buffer_load_dwordx4 v[164:167], v235, s[48:51], s75 offen sc1\n"
"buffer_load_dwordx4 v[168:171], v234, s[48:51], s76 offen sc1\n"
"buffer_load_dwordx4 v[172:175], v235, s[48:51], s76 offen sc1\n"
"buffer_load_dwordx4 v[176:179], v234, s[48:51], s77 offen sc1\n"
"buffer_load_dwordx4 v[180:183], v235, s[48:51], s77 offen sc1\n"
"buffer_load_dwordx4 v[128:131], v234, s[48:51], s78 offen sc1\n"
"buffer_load_dwordx4 v[132:135], v235, s[48:51], s78 offen sc1\n"
"buffer_load_dwordx4 v[136:139], v236, s[48:51], s72 offen sc1\n"
"buffer_load_dwordx4 v[140:143], v237, s[48:51], s72 offen sc1\n"
"buffer_load_dwordx4 v[208:211], v236, s[48:51], s73 offen sc1\n"
"buffer_load_dwordx4 v[212:215], v237, s[48:51], s73 offen sc1\n"
"s_waitcnt vmcnt(0)\n"
"v_max3_u32 v186, v96, v98, v100\n"
"v_max_u32 v186, v186, v102\n"
"v_cmp_eq_u32 vcc, -1, v186\n"
"s_nop 4\n"
"s_cbranch_vccnz Lh6_%=\n"
"s_nop 4\n"
"s_branch Lhr6_%=\n"
"Lh7_%=:\n"
"s_sub_u32 s86, s86, 1\n"
"s_cbranch_scc1 Lhr7_%=\n"
"buffer_load_dwordx4 v[104:107], v234, s[48:51], s72 offen sc1\n"
"buffer_load_dwordx4 v[108:111], v235, s[48:51], s72 offen sc1\n"
"buffer_load_dwordx4 v[112:115], v234, s[48:51], s73 offen sc1\n"
"buffer_load_dwordx4 v[116:119], v235, s[48:51], s73 offen sc1\n"
"buffer_load_dwordx4 v[120:123], v234, s[48:51], s74 offen sc1\n"
"buffer_load_dwordx4 v[124:127], v235, s[48:51], s74 offen sc1\n"
"buffer_load_dwordx4 v[160:163], v234, s[48:51], s75 offen sc1\n"
"buffer_load_dwordx4 v[164:167], v235, s[48:51], s75 offen sc1\n"
"buffer_load_dwordx4 v[168:171], v234, s[48:51], s76 offen sc1\n"
"buffer_load_dwordx4 v[172:175], v235, s[48:51], s76 offen sc1\n"
"buffer_load_dwordx4 v[176:179], v234, s[48:51], s77 offen sc1\n"
"buffer_load_dwordx4 v[180:183], v235, s[48:51], s77 offen sc1\n"
"buffer_load_dwordx4 v[128:131], v234, s[48:51], s78 offen sc1\n"
"buffer_load_dwordx4 v[132:135], v235, s[48:51], s78 offen sc1\n"
"buffer_load_dwordx4 v[136:139], v236, s[48:51], s72 offen sc1\n"
"buffer_load_dwordx4 v[140:143], v237, s[48:51], s72 offen sc1\n"
"buffer_load_dwordx4 v[208:211], v236, s[48:51], s73 offen sc1\n"
"buffer_load_dwordx4 v[212:215], v237, s[48:51], s73 offen sc1\n"
"buffer_load_dwordx4 v[216:219], v236, s[48:51], s74 offen sc1\n"
"buffer_load_dwordx4 v[220:223], v237, s[48:51], s74 offen sc1\n"
"s_waitcnt vmcnt(0)\n"
"v_max3_u32 v186, v104, v106, v108\n"
"v_max_u32 v186, v186, v110\n"
"v_cmp_eq_u32 vcc, -1, v186\n"
"s_nop 4\n"
"s_cbranch_vccnz Lh7_%=\n"
"s_nop 4\n"
"s_branch Lhr7_%=\n"
"Lh8_%=:\n"
"s_sub_u32 s86, s86, 1\n"
"s_cbranch_scc1 Lhr8_%=\n"
"buffer_load_dwordx4 v[112:115], v234, s[48:51], s73 offen sc1\n"
"buffer_load_dwordx4 v[116:119], v235, s[48:51], s73 offen sc1\n"
"buffer_load_dwordx4 v[120:123], v234, s[48:51], s74 offen sc1\n"
"buffer_load_dwordx4 v[124:127], v235, s[48:51], s74 offen sc1\n"
"buffer_load_dwordx4 v[160:163], v234, s[48:51], s75 offen sc1\n"
"buffer_load_dwordx4 v[164:167], v235, s[48:51], s75 offen sc1\n"
"buffer_load_dwordx4 v[168:171], v234, s[48:51], s76 offen sc1\n"
"buffer_load_dwordx4 v[172:175], v235, s[48:51], s76 offen sc1\n"
"buffer_load_dwordx4 v[176:179], v234, s[48:51], s77 offen sc1\n"
"buffer_load_dwordx4 v[180:183], v235, s[48:51], s77 offen sc1\n"
"buffer_load_dwordx4 v[128:131], v234, s[48:51], s78 offen sc1\n"
"buffer_load_dwordx4 v[132:135], v235, s[48:51], s78 offen sc1\n"
"buffer_load_dwordx4 v[136:139], v236, s[48:51], s72 offen sc1\n"
"buffer_load_dwordx4 v[140:143], v237, s[48:51], s72 offen sc1\n"
"buffer_load_dwordx4 v[208:211], v236, s[48:51], s73 offen sc1\n"
"buffer_load_dwordx4 v[212:215], v237, s[48:51], s73 offen sc1\n"
"buffer_load_dwordx4 v[216:219], v236, s[48:51], s74 offen sc1\n"
"buffer_load_dwordx4 v[220:223], v237, s[48:51], s74 offen sc1\n"
"buffer_load_dwordx4 v[0:3], v236, s[48:51], s75 offen sc1\n"
"buffer_load_dwordx4 v[4:7], v237, s[48:51], s75 offen sc1\n"
"s_waitcnt vmcnt(0)\n"
"v_max3_u32 v186, v112, v114, v116\n"
"v_max_u32 v186, v186, v118\n"
"v_cmp_eq_u32 vcc, -1, v186\n"
"s_nop 4\n"
"s_cbranch_vccnz Lh8_%=\n"
"s_nop 4\n"
"s_branch Lhr8_%=\n"
"Lh9_%=:\n"
"s_sub_u32 s86, s86, 1\n"
"s_cbranch_scc1 Lhr9_%=\n"
"buffer_load_dwordx4 v[120:123], v234, s[48:51], s74 offen sc1\n"
"buffer_load_dwordx4 v[124:127], v235, s[48:51], s74 offen sc1\n"
"buffer_load_dwordx4 v[160:163], v234, s[48:51], s75 offen sc1\n"
"buffer_load_dwordx4 v[164:167], v235, s[48:51], s75 offen sc1\n"
"buffer_load_dwordx4 v[168:171], v234, s[48:51], s76 offen sc1\n"
"buffer_load_dwordx4 v[172:175], v235, s[48:51], s76 offen sc1\n"
"buffer_load_dwordx4 v[176:179], v234, s[48:51], s77 offen sc1\n"
"buffer_load_dwordx4 v[180:183], v235, s[48:51], s77 offen sc1\n"
"buffer_load_dwordx4 v[128:131], v234, s[48:51], s78 offen sc1\n"
"buffer_load_dwordx4 v[132:135], v235, s[48:51], s78 offen sc1\n"
"buffer_load_dwordx4 v[136:139], v236, s[48:51], s72 offen sc1\n"
"buffer_load_dwordx4 v[140:143], v237, s[48:51], s72 offen sc1\n"
"buffer_load_dwordx4 v[208:211], v236, s[48:51], s73 offen sc1\n"
"buffer_load_dwordx4 v[212:215], v237, s[48:51], s73 offen sc1\n"
"buffer_load_dwordx4 v[216:219], v236, s[48:51], s74 offen sc1\n"
"buffer_load_dwordx4 v[220:223], v237, s[48:51], s74 offen sc1\n"
"buffer_load_dwordx4 v[0:3], v236, s[48:51], s75 offen sc1\n"
"buffer_load_dwordx4 v[4:7], v237, s[48:51], s75 offen sc1\n"
"buffer_load_dwordx4 v[8:11], v236, s[48:51], s76 offen sc1\n"
"buffer_load_dwordx4 v[12:15], v237, s[48:51], s76 offen sc1\n"
"s_waitcnt vmcnt(0)\n"
"v_max3_u32 v186, v120, v122, v124\n"
"v_max_u32 v186, v186, v126\n"
"v_cmp_eq_u32 vcc, -1, v186\n"
"s_nop 4\n"
"s_cbranch_vccnz Lh9_%=\n"
"s_nop 4\n"
"s_branch Lhr9_%=\n"
"Lh10_%=:\n"
"s_sub_u32 s86, s86, 1\n"
"s_cbranch_scc1 Lhr10_%=\n"
"buffer_load_dwordx4 v[160:163], v234, s[48:51], s75 offen sc1\n"
"buffer_load_dwordx4 v[164:167], v235, s[48:51], s75 offen sc1\n"
"buffer_load_dwordx4 v[168:171], v234, s[48:51], s76 offen sc1\n"
"buffer_load_dwordx4 v[172:175], v235, s[48:51], s76 offen sc1\n"
"buffer_load_dwordx4 v[176:179], v234, s[48:51], s77 offen sc1\n"
"buffer_load_dwordx4 v[180:183], v235, s[48:51], s77 offen sc1\n"
"buffer_load_dwordx4 v[128:131], v234, s[48:51], s78 offen sc1\n"
"buffer_load_dwordx4 v[132:135], v235, s[48:51], s78 offen sc1\n"
"buffer_load_dwordx4 v[136:139], v236, s[48:51], s72 offen sc1\n"
"buffer_load_dwordx4 v[140:143], v237, s[48:51], s72 offen sc1\n"
"buffer_load_dwordx4 v[208:211], v236, s[48:51], s73 offen sc1\n"
"buffer_load_dwordx4 v[212:215], v237, s[48:51], s73 offen sc1\n"
"buffer_load_dwordx4 v[216:219], v236, s[48:51], s74 offen sc1\n"
"buffer_load_dwordx4 v[220:223], v237, s[48:51], s74 offen sc1\n"
"buffer_load_dwordx4 v[0:3], v236, s[48:51], s75 offen sc1\n"
"buffer_load_dwordx4 v[4:7], v237, s[48:51], s75 offen sc1\n"
"buffer_load_dwordx4 v[8:11], v236, s[48:51], s76 offen sc1\n"
"buffer_load_dwordx4 v[12:15], v237, s[48:51], s76 offen sc1\n"
"buffer_load_dwordx4 v[80:83], v236, s[48:51], s77 offen sc1\n"
"buffer_load_dwordx4 v[84:87], v237, s[48:51], s77 offen sc1\n"
"s_waitcnt vmcnt(0)\n"
"v_max3_u32 v186, v160, v162, v164\n"
"v_max_u32 v186, v186, v166\n"
"v_cmp_eq_u32 vcc, -1, v186\n"
"s_nop 4\n"
"s_cbranch_vccnz Lh10_%=\n"
"s_nop 4\n"
"s_branch Lhr10_%=\n"
"Lh11_%=:\n"
"s_sub_u32 s86, s86, 1\n"
"s_cbranch_scc1 Lhr11_%=\n"
"buffer_load_dwordx4 v[168:171], v234, s[48:51], s76 offen sc1\n"
"buffer_load_dwordx4 v[172:175], v235, s[48:51], s76 offen sc1\n"
"buffer_load_dwordx4 v[176:179], v234, s[48:51], s77 offen sc1\n"
"buffer_load_dwordx4 v[180:183], v235, s[48:51], s77 offen sc1\n"
"buffer_load_dwordx4 v[128:131], v234, s[48:51], s78 offen sc1\n"
"buffer_load_dwordx4 v[132:135], v235, s[48:51], s78 offen sc1\n"
"buffer_load_dwordx4 v[136:139], v236, s[48:51], s72 offen sc1\n"
"buffer_load_dwordx4 v[140:143], v237, s[48:51], s72 offen sc1\n"
"buffer_load_dwordx4 v[208:211], v236, s[48:51], s73 offen sc1\n"
"buffer_load_dwordx4 v[212:215], v237, s[48:51], s73 offen sc1\n"
"buffer_load_dwordx4 v[216:219], v236, s[48:51], s74 offen sc1\n"
"buffer_load_dwordx4 v[220:223], v237, s[48:51], s74 offen sc1\n"
"buffer_load_dwordx4 v[0:3], v236, s[48:51], s75 offen sc1\n"
"buffer_load_dwordx4 v[4:7], v237, s[48:51], s75 offen sc1\n"
"buffer_load_dwordx4 v[8:11], v236, s[48:51], s76 offen sc1\n"
"buffer_load_dwordx4 v[12:15], v237, s[48:51], s76 offen sc1\n"
"buffer_load_dwordx4 v[80:83], v236, s[48:51], s77 offen sc1\n"
"buffer_load_dwordx4 v[84:87], v237, s[48:51], s77 offen sc1\n"
"buffer_load_dwordx4 v[88:91], v236, s[48:51], s78 offen sc1\n"
"buffer_load_dwordx4 v[92:95], v237, s[48:51], s78 offen sc1\n"
"s_waitcnt vmcnt(0)\n"
"v_max3_u32 v186, v168, v170, v172\n"
"v_max_u32 v186, v186, v174\n"
"v_cmp_eq_u32 vcc, -1, v186\n"
"s_nop 4\n"
"s_cbranch_vccnz Lh11_%=\n"
"s_nop 4\n"
"s_branch Lhr11_%=\n"
"Lh12_%=:\n"
"s_sub_u32 s86, s86, 1\n"
"s_cbranch_scc1 Lhr12_%=\n"
"buffer_load_dwordx4 v[176:179], v234, s[48:51], s77 offen sc1\n"
"buffer_load_dwordx4 v[180:183], v235, s[48:51], s77 offen sc1\n"
"buffer_load_dwordx4 v[128:131], v234, s[48:51], s78 offen sc1\n"
"buffer_load_dwordx4 v[132:135], v235, s[48:51], s78 offen sc1\n"
"buffer_load_dwordx4 v[136:139], v236, s[48:51], s72 offen sc1\n"
"buffer_load_dwordx4 v[140:143], v237, s[48:51], s72 offen sc1\n"
"buffer_load_dwordx4 v[208:211], v236, s[48:51], s73 offen sc1\n"
"buffer_load_dwordx4 v[212:215], v237, s[48:51], s73 offen sc1\n"
"buffer_load_dwordx4 v[216:219], v236, s[48:51], s74 offen sc1\n"
"buffer_load_dwordx4 v[220:223], v237, s[48:51], s74 offen sc1\n"
"buffer_load_dwordx4 v[0:3], v236, s[48:51], s75 offen sc1\n"
"buffer_load_dwordx4 v[4:7], v237, s[48:51], s75 offen sc1\n"
"buffer_load_dwordx4 v[8:11], v236, s[48:51], s76 offen sc1\n"
"buffer_load_dwordx4 v[12:15], v237, s[48:51], s76 offen sc1\n"
"buffer_load_dwordx4 v[80:83], v236, s[48:51], s77 offen sc1\n"
"buffer_load_dwordx4 v[84:87], v237, s[48:51], s77 offen sc1\n"
"buffer_load_dwordx4 v[88:91], v236, s[48:51], s78 offen sc1\n"
"buffer_load_dwordx4 v[92:95], v237, s[48:51], s78 offen sc1\n"
"buffer_load_dwordx4 v[96:99], v238, s[48:51], s72 offen sc1\n"
"buffer_load_dwordx4 v[100:103], v239, s[48:51], s72 offen sc1\n"
"s_waitcnt vmcnt(0)\n"
"v_max3_u32 v186, v176, v178, v180\n"
"v_max_u32 v186, v186, v182\n"
"v_cmp_eq_u32 vcc, -1, v186\n"
"s_nop 4\n"
"s_cbranch_vccnz Lh12_%=\n"
"s_nop 4\n"
"s_branch Lhr12_%=\n"
"Lh13_%=:\n"
"s_sub_u32 s86, s86, 1\n"
"s_cbranch_scc1 Lhr13_%=\n"
"buffer_load_dwordx4 v[128:131], v234, s[48:51], s78 offen sc1\n"
"buffer_load_dwordx4 v[132:135], v235, s[48:51], s78 offen sc1\n"
"buffer_load_dwordx4 v[136:139], v236, s[48:51], s72 offen sc1\n"
"buffer_load_dwordx4 v[140:143], v237, s[48:51], s72 offen sc1\n"
"buffer_load_dwordx4 v[208:211], v236, s[48:51], s73 offen sc1\n"
"buffer_load_dwordx4 v[212:215], v237, s[48:51], s73 offen sc1\n"
"buffer_load_dwordx4 v[216:219], v236, s[48:51], s74 offen sc1\n"
"buffer_load_dwordx4 v[220:223], v237, s[48:51], s74 offen sc1\n"
"buffer_load_dwordx4 v[0:3], v236, s[48:51], s75 offen sc1\n"
"buffer_load_dwordx4 v[4:7], v237, s[48:51], s75 offen sc1\n"
"buffer_load_dwordx4 v[8:11], v236, s[48:51], s76 offen sc1\n"
"buffer_load_dwordx4 v[12:15], v237, s[48:51], s76 offen sc1\n"
"buffer_load_dwordx4 v[80:83], v236, s[48:51], s77 offen sc1\n"
"buffer_load_dwordx4 v[84:87], v237, s[48:51], s77 offen sc1\n"
"buffer_load_dwordx4 v[88:91], v236, s[48:51], s78 offen sc1\n"
"buffer_load_dwordx4 v[92:95], v237, s[48:51], s78 offen sc1\n"
"buffer_load_dwordx4 v[96:99], v238, s[48:51], s72 offen sc1\n"
"buffer_load_dwordx4 v[100:103], v239, s[48:51], s72 offen sc1\n"
"buffer_load_dwordx4 v[104:107], v238, s[48:51], s73 offen sc1\n"
"buffer_load_dwordx4 v[108:111], v239, s[48:51], s73 offen sc1\n"
"s_waitcnt vmcnt(0)\n"
"v_max3_u32 v186, v128, v130, v132\n"
"v_max_u32 v186, v186, v134\n"
"v_cmp_eq_u32 vcc, -1, v186\n"
"s_nop 4\n"
"s_cbranch_vccnz Lh13_%=\n"
"s_nop 4\n"
"s_branch Lhr13_%=\n"
"Lh14_%=:\n"
"s_sub_u32 s86, s86, 1\n"
"s_cbranch_scc1 Lhr14_%=\n"
"buffer_load_dwordx4 v[136:139], v236, s[48:51], s72 offen sc1\n"
"buffer_load_dwordx4 v[140:143], v237, s[48:51], s72 offen sc1\n"
"buffer_load_dwordx4 v[208:211], v236, s[48:51], s73 offen sc1\n"
"buffer_load_dwordx4 v[212:215], v237, s[48:51], s73 offen sc1\n"
"buffer_load_dwordx4 v[216:219], v236, s[48:51], s74 offen sc1\n"
"buffer_load_dwordx4 v[220:223], v237, s[48:51], s74 offen sc1\n"
"buffer_load_dwordx4 v[0:3], v236, s[48:51], s75 offen sc1\n"
"buffer_load_dwordx4 v[4:7], v237, s[48:51], s75 offen sc1\n"
"buffer_load_dwordx4 v[8:11], v236, s[48:51], s76 offen sc1\n"
"buffer_load_dwordx4 v[12:15], v237, s[48:51], s76 offen sc1\n"
"buffer_load_dwordx4 v[80:83], v236, s[48:51], s77 offen sc1\n"
"buffer_load_dwordx4 v[84:87], v237, s[48:51], s77 offen sc1\n"
"buffer_load_dwordx4 v[88:91], v236, s[48:51], s78 offen sc1\n"
"buffer_load_dwordx4 v[92:95], v237, s[48:51], s78 offen sc1\n"
"buffer_load_dwordx4 v[96:99], v238, s[48:51], s72 offen sc1\n"
"buffer_load_dwordx4 v[100:103], v239, s[48:51], s72 offen sc1\n"
"buffer_load_dwordx4 v[104:107], v238, s[48:51], s73 offen sc1\n"
"buffer_load_dwordx4 v[108:111], v239, s[48:51], s73 offen sc1\n"
"buffer_load_dwordx4 v[112:115], v238, s[48:51], s74 offen sc1\n"
"buffer_load_dwordx4 v[116:119], v239, s[48:51], s74 offen sc1\n"
"s_waitcnt vmcnt(0)\n"
"v_max3_u32 v186, v136, v138, v140\n"
"v_max_u32 v186, v186, v142\n"
"v_cmp_eq_u32 vcc, -1, v186\n"
"s_nop 4\n"
"s_cbranch_vccnz Lh14_%=\n"
"s_nop 4\n"
"s_branch Lhr14_%=\n"
"Lh15_%=:\n"
"s_sub_u32 s86, s86, 1\n"
"s_cbranch_scc1 Lhr15_%=\n"
"buffer_load_dwordx4 v[208:211], v236, s[48:51], s73 offen sc1\n"
"buffer_load_dwordx4 v[212:215], v237, s[48:51], s73 offen sc1\n"
"buffer_load_dwordx4 v[216:219], v236, s[48:51], s74 offen sc1\n"
"buffer_load_dwordx4 v[220:223], v237, s[48:51], s74 offen sc1\n"
"buffer_load_dwordx4 v[0:3], v236, s[48:51], s75 offen sc1\n"
"buffer_load_dwordx4 v[4:7], v237, s[48:51], s75 offen sc1\n"
"buffer_load_dwordx4 v[8:11], v236, s[48:51], s76 offen sc1\n"
"buffer_load_dwordx4 v[12:15], v237, s[48:51], s76 offen sc1\n"
"buffer_load_dwordx4 v[80:83], v236, s[48:51], s77 offen sc1\n"
"buffer_load_dwordx4 v[84:87], v237, s[48:51], s77 offen sc1\n"
"buffer_load_dwordx4 v[88:91], v236, s[48:51], s78 offen sc1\n"
"buffer_load_dwordx4 v[92:95], v237, s[48:51], s78 offen sc1\n"
"buffer_load_dwordx4 v[96:99], v238, s[48:51], s72 offen sc1\n"
"buffer_load_dwordx4 v[100:103], v239, s[48:51], s72 offen sc1\n"
"buffer_load_dwordx4 v[104:107], v238, s[48:51], s73 offen sc1\n"
"buffer_load_dwordx4 v[108:111], v239, s[48:51], s73 offen sc1\n"
"buffer_load_dwordx4 v[112:115], v238, s[48:51], s74 offen sc1\n"
"buffer_load_dwordx4 v[116:119], v239, s[48:51], s74 offen sc1\n"
"buffer_load_dwordx4 v[120:123], v238, s[48:51], s75 offen sc1\n"
"buffer_load_dwordx4 v[124:127], v239, s[48:51], s75 offen sc1\n"
"s_waitcnt vmcnt(0)\n"
"v_max3_u32 v186, v208, v210, v212\n"
"v_max_u32 v186, v186, v214\n"
"v_cmp_eq_u32 vcc, -1, v186\n"
"s_nop 4\n"
"s_cbranch_vccnz Lh15_%=\n"
"s_nop 4\n"
"s_branch Lhr15_%=\n"
"Lh16_%=:\n"
"s_sub_u32 s86, s86, 1\n"
"s_cbranch_scc1 Lhr16_%=\n"
"buffer_load_dwordx4 v[216:219], v236, s[48:51], s74 offen sc1\n"
"buffer_load_dwordx4 v[220:223], v237, s[48:51], s74 offen sc1\n"
"buffer_load_dwordx4 v[0:3], v236, s[48:51], s75 offen sc1\n"
"buffer_load_dwordx4 v[4:7], v237, s[48:51], s75 offen sc1\n"
"buffer_load_dwordx4 v[8:11], v236, s[48:51], s76 offen sc1\n"
"buffer_load_dwordx4 v[12:15], v237, s[48:51], s76 offen sc1\n"
"buffer_load_dwordx4 v[80:83], v236, s[48:51], s77 offen sc1\n"
"buffer_load_dwordx4 v[84:87], v237, s[48:51], s77 offen sc1\n"
"buffer_load_dwordx4 v[88:91], v236, s[48:51], s78 offen sc1\n"
"buffer_load_dwordx4 v[92:95], v237, s[48:51], s78 offen sc1\n"
"buffer_load_dwordx4 v[96:99], v238, s[48:51], s72 offen sc1\n"
"buffer_load_dwordx4 v[100:103], v239, s[48:51], s72 offen sc1\n"
"buffer_load_dwordx4 v[104:107], v238, s[48:51], s73 offen sc1\n"
"buffer_load_dwordx4 v[108:111], v239, s[48:51], s73 offen sc1\n"
"buffer_load_dwordx4 v[112:115], v238, s[48:51], s74 offen sc1\n"
"buffer_load_dwordx4 v[116:119], v239, s[48:51], s74 offen sc1\n"
"buffer_load_dwordx4 v[120:123], v238, s[48:51], s75 offen sc1\n"
"buffer_load_dwordx4 v[124:127], v239, s[48:51], s75 offen sc1\n"
"buffer_load_dwordx4 v[160:163], v238, s[48:51], s76 offen sc1\n"
"buffer_load_dwordx4 v[164:167], v239, s[48:51], s76 offen sc1\n"
"s_waitcnt vmcnt(0)\n"
"v_max3_u32 v186, v216, v218, v220\n"
"v_max_u32 v186, v186, v222\n"
"v_cmp_eq_u32 vcc, -1, v186\n"
"s_nop 4\n"
"s_cbranch_vccnz Lh16_%=\n"
"s_nop 4\n"
"s_branch Lhr16_%=\n"
"Lh17_%=:\n"
"s_sub_u32 s86, s86, 1\n"
"s_cbranch_scc1 Lhr17_%=\n"
"buffer_load_dwordx4 v[0:3], v236, s[48:51], s75 offen sc1\n"
"buffer_load_dwordx4 v[4:7], v237, s[48:51], s75 offen sc1\n"
"buffer_load_dwordx4 v[8:11], v236, s[48:51], s76 offen sc1\n"
"buffer_load_dwordx4 v[12:15], v237, s[48:51], s76 offen sc1\n"
"buffer_load_dwordx4 v[80:83], v236, s[48:51], s77 offen sc1\n"
"buffer_load_dwordx4 v[84:87], v237, s[48:51], s77 offen sc1\n"
"buffer_load_dwordx4 v[88:91], v236, s[48:51], s78 offen sc1\n"
"buffer_load_dwordx4 v[92:95], v237, s[48:51], s78 offen sc1\n"
"buffer_load_dwordx4 v[96:99], v238, s[48:51], s72 offen sc1\n"
"buffer_load_dwordx4 v[100:103], v239, s[48:51], s72 offen sc1\n"
"buffer_load_dwordx4 v[104:107], v238, s[48:51], s73 offen sc1\n"
"buffer_load_dwordx4 v[108:111], v239, s[48:51], s73 offen sc1\n"
"buffer_load_dwordx4 v[112:115], v238, s[48:51], s74 offen sc1\n"
"buffer_load_dwordx4 v[116:119], v239, s[48:51], s74 offen sc1\n"
"buffer_load_dwordx4 v[120:123], v238, s[48:51], s75 offen sc1\n"
"buffer_load_dwordx4 v[124:127], v239, s[48:51], s75 offen sc1\n"
"buffer_load_dwordx4 v[160:163], v238, s[48:51], s76 offen sc1\n"
"buffer_load_dwordx4 v[164:167], v239, s[48:51], s76 offen sc1\n"
"buffer_load_dwordx4 v[168:171], v238, s[48:51], s77 offen sc1\n"
"buffer_load_dwordx4 v[172:175], v239, s[48:51], s77 offen sc1\n"
"s_waitcnt vmcnt(0)\n"
"v_max3_u32 v186, v0, v2, v4\n"
"v_max_u32 v186, v186, v6\n"
"v_cmp_eq_u32 vcc, -1, v186\n"
"s_nop 4\n"
"s_cbranch_vccnz Lh17_%=\n"
"s_nop 4\n"
"s_branch Lhr17_%=\n"
"Lh18_%=:\n"
"s_sub_u32 s86, s86, 1\n"
"s_cbranch_scc1 Lhr18_%=\n"
"buffer_load_dwordx4 v[8:11], v236, s[48:51], s76 offen sc1\n"
"buffer_load_dwordx4 v[12:15], v237, s[48:51], s76 offen sc1\n"
"buffer_load_dwordx4 v[80:83], v236, s[48:51], s77 offen sc1\n"
"buffer_load_dwordx4 v[84:87], v237, s[48:51], s77 offen sc1\n"
"buffer_load_dwordx4 v[88:91], v236, s[48:51], s78 offen sc1\n"
"buffer_load_dwordx4 v[92:95], v237, s[48:51], s78 offen sc1\n"
"buffer_load_dwordx4 v[96:99], v238, s[48:51], s72 offen sc1\n"
"buffer_load_dwordx4 v[100:103], v239, s[48:51], s72 offen sc1\n"
"buffer_load_dwordx4 v[104:107], v238, s[48:51], s73 offen sc1\n"
"buffer_load_dwordx4 v[108:111], v239, s[48:51], s73 offen sc1\n"
"buffer_load_dwordx4 v[112:115], v238, s[48:51], s74 offen sc1\n"
"buffer_load_dwordx4 v[116:119], v239, s[48:51], s74 offen sc1\n"
"buffer_load_dwordx4 v[120:123], v238, s[48:51], s75 offen sc1\n"
"buffer_load_dwordx4 v[124:127], v239, s[48:51], s75 offen sc1\n"
"buffer_load_dwordx4 v[160:163], v238, s[48:51], s76 offen sc1\n"
"buffer_load_dwordx4 v[164:167], v239, s[48:51], s76 offen sc1\n"
"buffer_load_dwordx4 v[168:171], v238, s[48:51], s77 offen sc1\n"
"buffer_load_dwordx4 v[172:175], v239, s[48:51], s77 offen sc1\n"
"buffer_load_dwordx4 v[176:179], v238, s[48:51], s78 offen sc1\n"
"buffer_load_dwordx4 v[180:183], v239, s[48:51], s78 offen sc1\n"
"s_waitcnt vmcnt(0)\n"
"v_max3_u32 v186, v8, v10, v12\n"
"v_max_u32 v186, v186, v14\n"
"v_cmp_eq_u32 vcc, -1, v186\n"
"s_nop 4\n"
"s_cbranch_vccnz Lh18_%=\n"
"s_nop 4\n"
"s_branch Lhr18_%=\n"
"Lh19_%=:\n"
"s_sub_u32 s86, s86, 1\n"
"s_cbranch_scc1 Lhr19_%=\n"
"buffer_load_dwordx4 v[80:83], v236, s[48:51], s77 offen sc1\n"
"buffer_load_dwordx4 v[84:87], v237, s[48:51], s77 offen sc1\n"
"buffer_load_dwordx4 v[88:91], v236, s[48:51], s78 offen sc1\n"
"buffer_load_dwordx4 v[92:95], v237, s[48:51], s78 offen sc1\n"
"buffer_load_dwordx4 v[96:99], v238, s[48:51], s72 offen sc1\n"
"buffer_load_dwordx4 v[100:103], v239, s[48:51], s72 offen sc1\n"
"buffer_load_dwordx4 v[104:107], v238, s[48:51], s73 offen sc1\n"
"buffer_load_dwordx4 v[108:111], v239, s[48:51], s73 offen sc1\n"
"buffer_load_dwordx4 v[112:115], v238, s[48:51], s74 offen sc1\n"
"buffer_load_dwordx4 v[116:119], v239, s[48:51], s74 offen sc1\n"
"buffer_load_dwordx4 v[120:123], v238, s[48:51], s75 offen sc1\n"
"buffer_load_dwordx4 v[124:127], v239, s[48:51], s75 offen sc1\n"
"buffer_load_dwordx4 v[160:163], v238, s[48:51], s76 offen sc1\n"
"buffer_load_dwordx4 v[164:167], v239, s[48:51], s76 offen sc1\n"
"buffer_load_dwordx4 v[168:171], v238, s[48:51], s77 offen sc1\n"
"buffer_load_dwordx4 v[172:175], v239, s[48:51], s77 offen sc1\n"
"buffer_load_dwordx4 v[176:179], v238, s[48:51], s78 offen sc1\n"
"buffer_load_dwordx4 v[180:183], v239, s[48:51], s78 offen sc1\n"
"s_waitcnt vmcnt(0)\n"
"v_max3_u32 v186, v80, v82, v84\n"
"v_max_u32 v186, v186, v86\n"
"v_cmp_eq_u32 vcc, -1, v186\n"
"s_nop 4\n"
"s_cbranch_vccnz Lh19_%=\n"
"s_nop 4\n"
"s_branch Lhr19_%=\n"
"Lh20_%=:\n"
"s_sub_u32 s86, s86, 1\n"
"s_cbranch_scc1 Lhr20_%=\n"
"buffer_load_dwordx4 v[88:91], v236, s[48:51], s78 offen sc1\n"
"buffer_load_dwordx4 v[92:95], v237, s[48:51], s78 offen sc1\n"
"buffer_load_dwordx4 v[96:99], v238, s[48:51], s72 offen sc1\n"
"buffer_load_dwordx4 v[100:103], v239, s[48:51], s72 offen sc1\n"
"buffer_load_dwordx4 v[104:107], v238, s[48:51], s73 offen sc1\n"
"buffer_load_dwordx4 v[108:111], v239, s[48:51], s73 offen sc1\n"
"buffer_load_dwordx4 v[112:115], v238, s[48:51], s74 offen sc1\n"
"buffer_load_dwordx4 v[116:119], v239, s[48:51], s74 offen sc1\n"
"buffer_load_dwordx4 v[120:123], v238, s[48:51], s75 offen sc1\n"
"buffer_load_dwordx4 v[124:127], v239, s[48:51], s75 offen sc1\n"
"buffer_load_dwordx4 v[160:163], v238, s[48:51], s76 offen sc1\n"
"buffer_load_dwordx4 v[164:167], v239, s[48:51], s76 offen sc1\n"
"buffer_load_dwordx4 v[168:171], v238, s[48:51], s77 offen sc1\n"
"buffer_load_dwordx4 v[172:175], v239, s[48:51], s77 offen sc1\n"
"buffer_load_dwordx4 v[176:179], v238, s[48:51], s78 offen sc1\n"
"buffer_load_dwordx4 v[180:183], v239, s[48:51], s78 offen sc1\n"
"s_waitcnt vmcnt(0)\n"
"v_max3_u32 v186, v88, v90, v92\n"
"v_max_u32 v186, v186, v94\n"
"v_cmp_eq_u32 vcc, -1, v186\n"
"s_nop 4\n"
"s_cbranch_vccnz Lh20_%=\n"
"s_nop 4\n"
"s_branch Lhr20_%=\n"
"Lh21_%=:\n"
"s_sub_u32 s86, s86, 1\n"
"s_cbranch_scc1 Lhr21_%=\n"
"buffer_load_dwordx4 v[96:99], v238, s[48:51], s72 offen sc1\n"
"buffer_load_dwordx4 v[100:103], v239, s[48:51], s72 offen sc1\n"
"buffer_load_dwordx4 v[104:107], v238, s[48:51], s73 offen sc1\n"
"buffer_load_dwordx4 v[108:111], v239, s[48:51], s73 offen sc1\n"
"buffer_load_dwordx4 v[112:115], v238, s[48:51], s74 offen sc1\n"
"buffer_load_dwordx4 v[116:119], v239, s[48:51], s74 offen sc1\n"
"buffer_load_dwordx4 v[120:123], v238, s[48:51], s75 offen sc1\n"
"buffer_load_dwordx4 v[124:127], v239, s[48:51], s75 offen sc1\n"
"buffer_load_dwordx4 v[160:163], v238, s[48:51], s76 offen sc1\n"
"buffer_load_dwordx4 v[164:167], v239, s[48:51], s76 offen sc1\n"
"buffer_load_dwordx4 v[168:171], v238, s[48:51], s77 offen sc1\n"
"buffer_load_dwordx4 v[172:175], v239, s[48:51], s77 offen sc1\n"
"buffer_load_dwordx4 v[176:179], v238, s[48:51], s78 offen sc1\n"
"buffer_load_dwordx4 v[180:183], v239, s[48:51], s78 offen sc1\n"
"s_waitcnt vmcnt(0)\n"
"v_max3_u32 v186, v96, v98, v100\n"
"v_max_u32 v186, v186, v102\n"
"v_cmp_eq_u32 vcc, -1, v186\n"
"s_nop 4\n"
"s_cbranch_vccnz Lh21_%=\n"
"s_nop 4\n"
"s_branch Lhr21_%=\n"
"Lh22_%=:\n"
"s_sub_u32 s86, s86, 1\n"
"s_cbranch_scc1 Lhr22_%=\n"
"buffer_load_dwordx4 v[104:107], v238, s[48:51], s73 offen sc1\n"
"buffer_load_dwordx4 v[108:111], v239, s[48:51], s73 offen sc1\n"
"buffer_load_dwordx4 v[112:115], v238, s[48:51], s74 offen sc1\n"
"buffer_load_dwordx4 v[116:119], v239, s[48:51], s74 offen sc1\n"
"buffer_load_dwordx4 v[120:123], v238, s[48:51], s75 offen sc1\n"
"buffer_load_dwordx4 v[124:127], v239, s[48:51], s75 offen sc1\n"
"buffer_load_dwordx4 v[160:163], v238, s[48:51], s76 offen sc1\n"
"buffer_load_dwordx4 v[164:167], v239, s[48:51], s76 offen sc1\n"
"buffer_load_dwordx4 v[168:171], v238, s[48:51], s77 offen sc1\n"
"buffer_load_dwordx4 v[172:175], v239, s[48:51], s77 offen sc1\n"
"buffer_load_dwordx4 v[176:179], v238, s[48:51], s78 offen sc1\n"
"buffer_load_dwordx4 v[180:183], v239, s[48:51], s78 offen sc1\n"
"s_waitcnt vmcnt(0)\n"
"v_max3_u32 v186, v104, v106, v108\n"
"v_max_u32 v186, v186, v110\n"
"v_cmp_eq_u32 vcc, -1, v186\n"
"s_nop 4\n"
"s_cbranch_vccnz Lh22_%=\n"
"s_nop 4\n"
"s_branch Lhr22_%=\n"
"Lh23_%=:\n"
"s_sub_u32 s86, s86, 1\n"
"s_cbranch_scc1 Lhr23_%=\n"
"buffer_load_dwordx4 v[112:115], v238, s[48:51], s74 offen sc1\n"
"buffer_load_dwordx4 v[116:119], v239, s[48:51], s74 offen sc1\n"
"buffer_load_dwordx4 v[120:123], v238, s[48:51], s75 offen sc1\n"
"buffer_load_dwordx4 v[124:127], v239, s[48:51], s75 offen sc1\n"
"buffer_load_dwordx4 v[160:163], v238, s[48:51], s76 offen sc1\n"
"buffer_load_dwordx4 v[164:167], v239, s[48:51], s76 offen sc1\n"
"buffer_load_dwordx4 v[168:171], v238, s[48:51], s77 offen sc1\n"
"buffer_load_dwordx4 v[172:175], v239, s[48:51], s77 offen sc1\n"
"buffer_load_dwordx4 v[176:179], v238, s[48:51], s78 offen sc1\n"
"buffer_load_dwordx4 v[180:183], v239, s[48:51], s78 offen sc1\n"
"s_waitcnt vmcnt(0)\n"
"v_max3_u32 v186, v112, v114, v116\n"
"v_max_u32 v186, v186, v118\n"
"v_cmp_eq_u32 vcc, -1, v186\n"
"s_nop 4\n"
"s_cbranch_vccnz Lh23_%=\n"
"s_nop 4\n"
"s_branch Lhr23_%=\n"
"Lh24_%=:\n"
"s_sub_u32 s86, s86, 1\n"
"s_cbranch_scc1 Lhr24_%=\n"
"buffer_load_dwordx4 v[120:123], v238, s[48:51], s75 offen sc1\n"
"buffer_load_dwordx4 v[124:127], v239, s[48:51], s75 offen sc1\n"
"buffer_load_dwordx4 v[160:163], v238, s[48:51], s76 offen sc1\n"
"buffer_load_dwordx4 v[164:167], v239, s[48:51], s76 offen sc1\n"
"buffer_load_dwordx4 v[168:171], v238, s[48:51], s77 offen sc1\n"
"buffer_load_dwordx4 v[172:175], v239, s[48:51], s77 offen sc1\n"
"buffer_load_dwordx4 v[176:179], v238, s[48:51], s78 offen sc1\n"
"buffer_load_dwordx4 v[180:183], v239, s[48:51], s78 offen sc1\n"
"s_waitcnt vmcnt(0)\n"
"v_max3_u32 v186, v120, v122, v124\n"
"v_max_u32 v186, v186, v126\n"
"v_cmp_eq_u32 vcc, -1, v186\n"
"s_nop 4\n"
"s_cbranch_vccnz Lh24_%=\n"
"s_nop 4\n"
"s_branch Lhr24_%=\n"
"Lh25_%=:\n"
"s_sub_u32 s86, s86, 1\n"
"s_cbranch_scc1 Lhr25_%=\n"
"buffer_load_dwordx4 v[160:163], v238, s[48:51], s76 offen sc1\n"
"buffer_load_dwordx4 v[164:167], v239, s[48:51], s76 offen sc1\n"
"buffer_load_dwordx4 v[168:171], v238, s[48:51], s77 offen sc1\n"
"buffer_load_dwordx4 v[172:175], v239, s[48:51], s77 offen sc1\n"
"buffer_load_dwordx4 v[176:179], v238, s[48:51], s78 offen sc1\n"
"buffer_load_dwordx4 v[180:183], v239, s[48:51], s78 offen sc1\n"
"s_waitcnt vmcnt(0)\n"
"v_max3_u32 v186, v160, v162, v164\n"
"v_max_u32 v186, v186, v166\n"
"v_cmp_eq_u32 vcc, -1, v186\n"
"s_nop 4\n"
"s_cbranch_vccnz Lh25_%=\n"
"s_nop 4\n"
"s_branch Lhr25_%=\n"
"Lh26_%=:\n"
"s_sub_u32 s86, s86, 1\n"
"s_cbranch_scc1 Lhr26_%=\n"
"buffer_load_dwordx4 v[168:171], v238, s[48:51], s77 offen sc1\n"
"buffer_load_dwordx4 v[172:175], v239, s[48:51], s77 offen sc1\n"
"buffer_load_dwordx4 v[176:179], v238, s[48:51], s78 offen sc1\n"
"buffer_load_dwordx4 v[180:183], v239, s[48:51], s78 offen sc1\n"
"s_waitcnt vmcnt(0)\n"
"v_max3_u32 v186, v168, v170, v172\n"
"v_max_u32 v186, v186, v174\n"
"v_cmp_eq_u32 vcc, -1, v186\n"
"s_nop 4\n"
"s_cbranch_vccnz Lh26_%=\n"
"s_nop 4\n"
"s_branch Lhr26_%=\n"
"Lh27_%=:\n"
"s_sub_u32 s86, s86, 1\n"
"s_cbranch_scc1 Lhr27_%=\n"
"buffer_load_dwordx4 v[176:179], v238, s[48:51], s78 offen sc1\n"
"buffer_load_dwordx4 v[180:183], v239, s[48:51], s78 offen sc1\n"
"s_waitcnt vmcnt(0)\n"
"v_max3_u32 v186, v176, v178, v180\n"
"v_max_u32 v186, v186, v182\n"
"v_cmp_eq_u32 vcc, -1, v186\n"
"s_nop 4\n"
"s_cbranch_vccnz Lh27_%=\n"
"s_nop 4\n"
"s_branch Lhr27_%=\n"
"Lhelper_%=:\n"
"s_load_dwordx2 s[30:31], s[36:37], 0x60\n"
"v_and_b32 v1, 63, v0\n"
"v_lshrrev_b32 v2, 6, v0\n"
"v_lshrrev_b32 v3, 5, v1\n"
"v_readfirstlane_b32 s43, v2\n"
"v_and_b32 v4, 31, v1\n"
"v_mul_u32_u24 v5, 896, v3\n"
"v_lshl_add_u32 v5, v4, 2, v5\n"
"v_mov_b32 v8, 0\n"
"s_mul_i32 s28, s38, 7168\n"
"s_mul_i32 s29, s43, 1792\n"
"s_add_u32 s99, s28, s29\n"
"v_lshlrev_b32 v232, 4, v1\n"
"v_lshlrev_b32 v233, 2, v1\n"
"v_mov_b32 v24, 0\n"
"v_mov_b32 v25, 0\n"
"v_lshlrev_b32 v6, 1, v0\n"
"v_min_u32 v7, 191, v0\n"
"v_lshlrev_b32 v7, 1, v7\n"
"s_sub_u32 s60, s38, 224\n"
"s_lshr_b32 s61, s60, 1\n"
"s_and_b32 s70, s60, 1\n"
"s_add_u32 s62, s61, 21\n"
"s_sub_u32 s0, s60, 14\n"
"s_sub_u32 s1, s60, 10\n"
"s_cmp_lt_u32 s60, 31\n"
"s_cselect_b32 s1, s1, 41\n"
"s_cmp_lt_u32 s60, 28\n"
"s_cselect_b32 s61, s61, s0\n"
"s_cselect_b32 s62, s62, s1\n"
"s_cselect_b32 s70, s70, 0\n"
"s_cselect_b32 s64, 1, 0\n"
"v_lshrrev_b32 v9, 3, v1\n"
"s_lshl_b32 s0, s43, 3\n"
"v_add_u32 v9, s0, v9\n"
"v_lshlrev_b32 v10, 2, v3\n"
"v_and_b32 v11, 7, v1\n"
"v_xor_b32 v10, v10, v11\n"
"v_lshlrev_b32 v10, 4, v10\n"
"v_mul_u32_u24 v12, 5376, v9\n"
"v_add_u32 v12, v12, v10\n"
"s_mul_i32 s0, s70, 1204224\n"
"v_add_u32 v12, s0, v12\n"
"v_mul_u32_u24 v13, 3584, v9\n"
"v_add_u32 v13, v13, v10\n"
"s_lshl_b32 s66, s43, 10\n"
"s_mul_i32 s71, s70, 24576\n"
"s_mul_i32 s0, s43, 1824\n"
"v_mul_u32_u24 v9, 912, v3\n"
"v_lshl_add_u32 v9, v4, 2, v9\n"
"v_add_u32 v38, s0, v9\n"
"v_and_b32 v9, 3, v0\n"
"v_mul_u32_u24 v9, 1824, v9\n"
"v_lshrrev_b32 v14, 2, v0\n"
"v_lshl_add_u32 v39, v14, 2, v9\n"
"v_lshlrev_b32 v40, 8, v14\n"
"v_and_b32 v9, 3, v0\n"
"v_lshl_add_u32 v40, v9, 4, v40\n"
"s_lshr_b32 s0, s38, 2\n"
"s_mul_i32 s94, s0, 57344\n"
"s_and_b32 s0, s38, 3\n"
"s_lshl_b32 s0, s0, 6\n"
"s_add_u32 s94, s94, s0\n"
"s_add_u32 s94, s94, 81920\n"
"s_lshl_b32 s2, s38, 5\n"
"s_lshr_b32 s0, s43, 1\n"
"s_lshl_b32 s0, s0, 4\n"
"s_add_u32 s2, s2, s0\n"
"s_and_b32 s0, s43, 1\n"
"s_lshl_b32 s0, s0, 13\n"
"s_add_u32 s2, s2, s0\n"
"s_add_u32 s2, s2, 8192\n"
"v_lshlrev_b32 v41, 3, v3\n"
"v_mov_b32 v42, 0\n"
"v_mov_b32 v43, 0\n"
"v_mov_b32 v44, 0x3f803f80\n"
"v_and_b32 v9, 0xf0, v0\n"
"v_bfe_u32 v10, v0, 1, 1\n"
"v_lshl_add_u32 v9, v10, 3, v9\n"
"v_bfe_u32 v10, v0, 2, 2\n"
"v_lshl_add_u32 v9, v10, 1, v9\n"
"v_and_b32 v10, 1, v0\n"
"v_add_u32 v26, v9, v10\n"
"v_add_u32 v27, 256, v26\n"
"v_cmp_gt_u32 vcc, 192, v0\n"
"v_mov_b32 v9, 480\n"
"s_and_b32 s29, s43, 1\n"
"v_cndmask_b32 v27, v9, v27, vcc\n"
"v_bfe_u32 v9, v1, 2, 1\n"
"v_lshl_add_u32 v9, v3, 1, v9\n"
"v_bfe_u32 v10, v1, 3, 1\n"
"v_lshl_add_u32 v9, v10, 2, v9\n"
"v_lshlrev_b32 v9, 7, v9\n"
"v_xor_b32 v10, s29, v10\n"
"v_lshl_add_u32 v9, v10, 6, v9\n"
"v_bfe_u32 v10, v1, 4, 1\n"
"v_lshl_add_u32 v9, v10, 5, v9\n"
"v_and_b32 v10, 3, v1\n"
"v_lshl_add_u32 v9, v10, 3, v9\n"
"s_lshr_b32 s28, s43, 1\n"
"s_lshl_b32 s0, s28, 11\n"
"s_add_u32 s0, s0, 20480\n"
"v_add_u32 v28, s0, v9\n"
"v_add_u32 v29, 57344, v28\n"
"s_waitcnt lgkmcnt(0)\n"
"global_load_short_d16_hi v24, v6, s[30:31]\n"
"global_load_short_d16_hi v25, v7, s[30:31] offset:512\n"
"s_add_u32 s4, s4, s99\n"
"s_addc_u32 s5, s5, 0\n"
"global_load_dword v23, v8, s[24:25] sc1\n"
"global_load_dword v16, v5, s[4:5] offset:0\n"
"global_load_dword v17, v5, s[4:5] offset:128\n"
"global_load_dword v18, v5, s[4:5] offset:256\n"
"global_load_dword v19, v5, s[4:5] offset:384\n"
"global_load_dword v20, v5, s[4:5] offset:512\n"
"global_load_dword v21, v5, s[4:5] offset:640\n"
"global_load_dword v22, v5, s[4:5] offset:768\n"
"s_mov_b32 s52, s44\n"
"s_mov_b32 s53, s45\n"
"s_mov_b32 s54, 2408448\n"
"s_mov_b32 s55, 0x20000\n"
"s_lshl_b32 s1, s61, 7\n"
"s_add_u32 m0, s66, 20480\n"
"s_nop 0\n"
"buffer_load_dwordx4 v12, s[52:55], s1 offen lds\n"
"s_add_u32 m0, s66, 24576\n"
"s_add_u32 s1, s1, 172032\n"
"buffer_load_dwordx4 v12, s[52:55], s1 offen lds\n"
"s_add_u32 m0, s66, 28672\n"
"s_add_u32 s1, s1, 172032\n"
"buffer_load_dwordx4 v12, s[52:55], s1 offen lds\n"
"s_add_u32 m0, s66, 32768\n"
"s_add_u32 s1, s1, 172032\n"
"buffer_load_dwordx4 v12, s[52:55], s1 offen lds\n"
"s_add_u32 m0, s66, 36864\n"
"s_add_u32 s1, s1, 172032\n"
"buffer_load_dwordx4 v12, s[52:55], s1 offen lds\n"
"s_add_u32 m0, s66, 40960\n"
"s_add_u32 s1, s1, 172032\n"
"buffer_load_dwordx4 v12, s[52:55], s1 offen lds\n"
"s_add_u32 m0, s66, 45056\n"
"s_add_u32 s1, s1, 172032\n"
"buffer_load_dwordx4 v12, s[52:55], s1 offen lds\n"
"s_mov_b32 s48, s24\n"
"s_mov_b32 s49, s25\n"
"s_mov_b32 s50, 14532608\n"
"s_mov_b32 s51, 0x20000\n"
"s_mov_b32 s56, s46\n"
"s_mov_b32 s57, s47\n"
"s_mov_b32 s58, 1605632\n"
"s_mov_b32 s59, 0x20000\n"
"v_cmp_eq_u32 vcc, 1, v4\n"
"v_mov_b32 v9, 1024\n"
"s_lshl_b32 s0, s28, 5\n"
"v_cndmask_b32 v9, 0, v9, vcc\n"
"v_lshl_add_u32 v9, v3, 4, v9\n"
"v_add_u32 v30, s0, v9\n"
"s_mul_i32 s0, s70, 448\n"
"v_add_u32 v2, s0, v30\n"
"s_lshl_b32 s0, s43, 9\n"
"v_lshl_add_u32 v31, v1, 3, s0\n"
"s_lshl_b32 s0, s29, 9\n"
"v_lshl_add_u32 v32, v4, 3, s0\n"
"v_lshlrev_b32 v33, 9, v3\n"
"v_lshl_add_u32 v33, v4, 3, v33\n"
"v_lshlrev_b32 v34, 3, v1\n"
"s_waitcnt vmcnt(16)\n"
"v_mul_f32 v35, 0xbfb8aa3b, v24\n"
"v_exp_f32 v35, v35\n"
"v_lshlrev_b32 v36, 1, v26\n"
"v_add_f32 v35, 1.0, v35\n"
"v_rcp_f32 v35, v35\n"
"s_nop 0\n"
"v_mul_f32 v35, v35, v24\n"
"v_cvt_pk_bf16_f32 v35, v35, v35\n"
"ds_write_b16 v36, v35 offset:8192\n"
"s_waitcnt vmcnt(15)\n"
"v_mul_f32 v35, 0xbfb8aa3b, v25\n"
"v_exp_f32 v35, v35\n"
"v_lshlrev_b32 v36, 1, v27\n"
"v_add_f32 v35, 1.0, v35\n"
"v_rcp_f32 v35, v35\n"
"s_nop 0\n"
"v_mul_f32 v35, v35, v25\n"
"v_cvt_pk_bf16_f32 v35, v35, v35\n"
"ds_write_b16 v36, v35 offset:8192\n"
"s_cmp_eq_u32 s64, 0\n"
"s_cbranch_scc1 Lgate_%=\n"
"s_lshl_b32 s1, s62, 7\n"
"s_add_u32 m0, s66, 49152\n"
"s_nop 0\n"
"buffer_load_dwordx4 v12, s[52:55], s1 offen lds\n"
"s_add_u32 m0, s66, 53248\n"
"s_add_u32 s1, s1, 172032\n"
"buffer_load_dwordx4 v12, s[52:55], s1 offen lds\n"
"s_add_u32 m0, s66, 57344\n"
"s_add_u32 s1, s1, 172032\n"
"buffer_load_dwordx4 v12, s[52:55], s1 offen lds\n"
"s_waitcnt vmcnt(17)\n"
"v_add_u32 v23, 1, v23\n"
"s_waitcnt vmcnt(16)\n"
"ds_write_b32 v38, v16 offset:0\n"
"s_waitcnt vmcnt(15)\n"
"ds_write_b32 v38, v17 offset:128\n"
"s_waitcnt vmcnt(14)\n"
"ds_write_b32 v38, v18 offset:256\n"
"s_waitcnt vmcnt(13)\n"
"ds_write_b32 v38, v19 offset:384\n"
"s_waitcnt vmcnt(12)\n"
"ds_write_b32 v38, v20 offset:512\n"
"s_waitcnt vmcnt(11)\n"
"ds_write_b32 v38, v21 offset:640\n"
"s_waitcnt vmcnt(10)\n"
"ds_write_b32 v38, v22 offset:768\n"
"v_readfirstlane_b32 s39, v23\n"
"v_dot2c_f32_bf16 v42, v16, v44\n"
"v_dot2c_f32_bf16 v43, v16, v16\n"
"v_dot2c_f32_bf16 v42, v17, v44\n"
"v_dot2c_f32_bf16 v43, v17, v17\n"
"v_dot2c_f32_bf16 v42, v18, v44\n"
"v_dot2c_f32_bf16 v43, v18, v18\n"
"v_dot2c_f32_bf16 v42, v19, v44\n"
"v_dot2c_f32_bf16 v43, v19, v19\n"
"v_dot2c_f32_bf16 v42, v20, v44\n"
"v_dot2c_f32_bf16 v43, v20, v20\n"
"v_dot2c_f32_bf16 v42, v21, v44\n"
"v_dot2c_f32_bf16 v43, v21, v21\n"
"v_dot2c_f32_bf16 v42, v22, v44\n"
"v_dot2c_f32_bf16 v43, v22, v22\n"
"s_waitcnt lgkmcnt(0)\n"
"s_barrier\n"
"ds_read_b32 v96, v39 offset:0\n"
"ds_read_b32 v98, v39 offset:912\n"
"ds_read_b32 v100, v39 offset:256\n"
"ds_read_b32 v102, v39 offset:1168\n"
"ds_read_b32 v104, v39 offset:512\n"
"ds_read_b32 v106, v39 offset:1424\n"
"ds_read_b32 v108, v39 offset:768\n"
"ds_read_b32 v110, v39 offset:1680\n"
"v_add_f32_dpp v42, v42, v42 quad_perm:[1,0,3,2] row_mask:0xf bank_mask:0xf bound_ctrl:1\n"
"v_add_f32_dpp v43, v43, v43 quad_perm:[1,0,3,2] row_mask:0xf bank_mask:0xf bound_ctrl:1\n"
"v_mov_b32 v97, v23\n"
"v_mov_b32 v99, v23\n"
"v_add_f32_dpp v42, v42, v42 quad_perm:[2,3,0,1] row_mask:0xf bank_mask:0xf bound_ctrl:1\n"
"v_add_f32_dpp v43, v43, v43 quad_perm:[2,3,0,1] row_mask:0xf bank_mask:0xf bound_ctrl:1\n"
"v_mov_b32 v101, v23\n"
"v_mov_b32 v103, v23\n"
"v_add_f32_dpp v42, v42, v42 row_half_mirror row_mask:0xf bank_mask:0xf bound_ctrl:1\n"
"v_add_f32_dpp v43, v43, v43 row_half_mirror row_mask:0xf bank_mask:0xf bound_ctrl:1\n"
"v_mov_b32 v105, v23\n"
"v_mov_b32 v107, v23\n"
"v_add_f32_dpp v42, v42, v42 row_mirror row_mask:0xf bank_mask:0xf bound_ctrl:1\n"
"v_add_f32_dpp v43, v43, v43 row_mirror row_mask:0xf bank_mask:0xf bound_ctrl:1\n"
"v_mov_b32 v109, v23\n"
"v_mov_b32 v111, v23\n"
"v_mov_b32 v45, v42\n"
"v_mov_b32 v44, v43\n"
"s_add_u32 s0, s94, 0\n"
"s_waitcnt lgkmcnt(6)\n"
"buffer_store_dwordx4 v[96:99], v40, s[48:51], s0 offen sc1\n"
"v_permlane16_swap_b32 v42, v45\n"
"v_permlane16_swap_b32 v43, v44\n"
"v_add_f32 v42, v42, v45\n"
"v_add_f32 v43, v43, v44\n"
"s_add_u32 s0, s94, 16384\n"
"s_waitcnt lgkmcnt(4)\n"
"buffer_store_dwordx4 v[100:103], v40, s[48:51], s0 offen sc1\n"
"s_add_u32 s0, s94, 32768\n"
"s_waitcnt lgkmcnt(2)\n"
"buffer_store_dwordx4 v[104:107], v40, s[48:51], s0 offen sc1\n"
"s_cmp_gt_u32 s43, 1\n"
"s_cbranch_scc1 Lxh_%=\n"
"s_add_u32 s0, s94, 49152\n"
"s_waitcnt lgkmcnt(0)\n"
"buffer_store_dwordx4 v[108:111], v40, s[48:51], s0 offen sc1\n"
"Lxh_%=:\n"
"v_mul_f32 v42, 0x3b124925, v42\n"
"v_mul_f32 v43, 0x3b124925, v43\n"
"v_fma_f32 v43, -v42, v42, v43\n"
"v_max_f32 v43, 0, v43\n"
"v_cvt_pk_bf16_f32 v43, v43, v43\n"
"v_lshlrev_b32 v43, 16, v43\n"
"v_add_f32 v43, 0x3727c5ac, v43\n"
"v_cvt_pk_bf16_f32 v43, v43, v43\n"
"v_lshlrev_b32 v43, 16, v43\n"
"v_sqrt_f32 v43, v43\n"
"s_nop 0\n"
"v_cvt_pk_bf16_f32 v22, v42, v43\n"
"s_mov_b32 exec_lo, 1\n"
"s_mov_b32 exec_hi, 1\n"
"buffer_store_dwordx2 v[22:23], v41, s[48:51], s2 offen sc1\n"
"s_mov_b64 exec, -1\n"
"ds_read_b128 v[120:123], v2 offset:8192\n"
"ds_read_b128 v[124:127], v2 offset:8256\n"
"ds_read_b128 v[128:131], v2 offset:8320\n"
"ds_read_b128 v[132:135], v2 offset:8384\n"
"ds_read_b128 v[136:139], v2 offset:8448\n"
"ds_read_b128 v[140:143], v2 offset:8512\n"
"ds_read_b128 v[144:147], v2 offset:8576\n"
"s_add_u32 m0, s66, 61440\n"
"s_add_u32 s1, s1, 172032\n"
"buffer_load_dwordx4 v12, s[52:55], s1 offen lds\n"
"s_add_u32 m0, s66, 65536\n"
"s_add_u32 s1, s1, 172032\n"
"buffer_load_dwordx4 v12, s[52:55], s1 offen lds\n"
"s_add_u32 m0, s66, 69632\n"
"s_add_u32 s1, s1, 172032\n"
"buffer_load_dwordx4 v12, s[52:55], s1 offen lds\n"
"s_add_u32 m0, s66, 73728\n"
"s_add_u32 s1, s1, 172032\n"
"buffer_load_dwordx4 v12, s[52:55], s1 offen lds\n"
"s_waitcnt vmcnt(11)\n"
"s_barrier\n"
"ds_read_b64_tr_b16 v[64:65], v28 offset:0\n"
"ds_read_b64_tr_b16 v[66:67], v28 offset:1024\n"
"ds_read_b64_tr_b16 v[68:69], v28 offset:4096\n"
"ds_read_b64_tr_b16 v[70:71], v28 offset:5120\n"
"ds_read_b64_tr_b16 v[72:73], v28 offset:8192\n"
"ds_read_b64_tr_b16 v[74:75], v28 offset:9216\n"
"ds_read_b64_tr_b16 v[76:77], v28 offset:12288\n"
"ds_read_b64_tr_b16 v[78:79], v28 offset:13312\n"
"s_waitcnt lgkmcnt(6)\n"
"v_mfma_f32_32x32x16_bf16 v[176:191], v[120:123], v[64:67], 0\n"
"ds_read_b64_tr_b16 v[80:81], v28 offset:16384\n"
"ds_read_b64_tr_b16 v[82:83], v28 offset:17408\n"
"s_waitcnt lgkmcnt(6)\n"
"v_mfma_f32_32x32x16_bf16 v[176:191], v[124:127], v[68:71], v[176:191]\n"
"ds_read_b64_tr_b16 v[84:85], v28 offset:20480\n"
"ds_read_b64_tr_b16 v[86:87], v28 offset:21504\n"
"s_waitcnt lgkmcnt(6)\n"
"v_mfma_f32_32x32x16_bf16 v[176:191], v[128:131], v[72:75], v[176:191]\n"
"ds_read_b64_tr_b16 v[88:89], v28 offset:24576\n"
"ds_read_b64_tr_b16 v[90:91], v28 offset:25600\n"
"s_waitcnt lgkmcnt(6)\n"
"v_mfma_f32_32x32x16_bf16 v[176:191], v[132:135], v[76:79], v[176:191]\n"
"s_waitcnt lgkmcnt(4)\n"
"v_mfma_f32_32x32x16_bf16 v[176:191], v[136:139], v[80:83], v[176:191]\n"
"s_waitcnt lgkmcnt(2)\n"
"v_mfma_f32_32x32x16_bf16 v[176:191], v[140:143], v[84:87], v[176:191]\n"
"s_waitcnt lgkmcnt(0)\n"
"v_mfma_f32_32x32x16_bf16 v[176:191], v[144:147], v[88:91], v[176:191]\n"
"s_nop 11\n"
"ds_write_b64 v31, v[176:177] offset:13312\n"
"s_waitcnt lgkmcnt(0)\n"
"s_barrier\n"
"s_cmp_lg_u32 s43, 0\n"
"s_cbranch_scc1 Lpb0_%=\n"
"ds_read_b32 v35, v33 offset:13312\n"
"ds_read_b32 v36, v33 offset:14336\n"
"s_lshl_b32 s0, s61, 9\n"
"s_add_u32 s0, s0, s71\n"
"s_add_u32 s0, s0, 32768\n"
"s_waitcnt lgkmcnt(0)\n"
"v_add_f32 v22, v35, v36\n"
"buffer_store_dwordx2 v[22:23], v34, s[48:51], s0 offen sc1\n"
"Lpb0_%=:\n"
"s_lshl_b32 s1, s60, 7\n"
"s_add_u32 m0, s66, 77824\n"
"s_nop 0\n"
"buffer_load_dwordx4 v13, s[56:59], s1 offen lds\n"
"s_add_u32 m0, s66, 81920\n"
"s_add_u32 s1, s1, 114688\n"
"buffer_load_dwordx4 v13, s[56:59], s1 offen lds\n"
"s_add_u32 m0, s66, 86016\n"
"s_add_u32 s1, s1, 114688\n"
"buffer_load_dwordx4 v13, s[56:59], s1 offen lds\n"
"s_add_u32 m0, s66, 90112\n"
"s_add_u32 s1, s1, 114688\n"
"buffer_load_dwordx4 v13, s[56:59], s1 offen lds\n"
"s_add_u32 m0, s66, 94208\n"
"s_add_u32 s1, s1, 114688\n"
"buffer_load_dwordx4 v13, s[56:59], s1 offen lds\n"
"s_add_u32 m0, s66, 98304\n"
"s_add_u32 s1, s1, 114688\n"
"buffer_load_dwordx4 v13, s[56:59], s1 offen lds\n"
"s_add_u32 m0, s66, 102400\n"
"s_add_u32 s1, s1, 114688\n"
"buffer_load_dwordx4 v13, s[56:59], s1 offen lds\n"
"s_add_u32 m0, s66, 106496\n"
"s_add_u32 s1, s1, 114688\n"
"buffer_load_dwordx4 v13, s[56:59], s1 offen lds\n"
"s_add_u32 m0, s66, 110592\n"
"s_add_u32 s1, s1, 114688\n"
"buffer_load_dwordx4 v13, s[56:59], s1 offen lds\n"
"s_add_u32 m0, s66, 114688\n"
"s_add_u32 s1, s1, 114688\n"
"buffer_load_dwordx4 v13, s[56:59], s1 offen lds\n"
"s_add_u32 m0, s66, 118784\n"
"s_add_u32 s1, s1, 114688\n"
"buffer_load_dwordx4 v13, s[56:59], s1 offen lds\n"
"s_add_u32 m0, s66, 122880\n"
"s_add_u32 s1, s1, 114688\n"
"buffer_load_dwordx4 v13, s[56:59], s1 offen lds\n"
"s_add_u32 m0, s66, 126976\n"
"s_add_u32 s1, s1, 114688\n"
"buffer_load_dwordx4 v13, s[56:59], s1 offen lds\n"
"s_add_u32 m0, s66, 131072\n"
"s_add_u32 s1, s1, 114688\n"
"buffer_load_dwordx4 v13, s[56:59], s1 offen lds\n"
"s_waitcnt vmcnt(14)\n"
"s_barrier\n"
"ds_read_b64_tr_b16 v[64:65], v28 offset:28672\n"
"ds_read_b64_tr_b16 v[66:67], v28 offset:29696\n"
"ds_read_b64_tr_b16 v[68:69], v28 offset:32768\n"
"ds_read_b64_tr_b16 v[70:71], v28 offset:33792\n"
"ds_read_b64_tr_b16 v[72:73], v28 offset:36864\n"
"ds_read_b64_tr_b16 v[74:75], v28 offset:37888\n"
"ds_read_b64_tr_b16 v[76:77], v28 offset:40960\n"
"ds_read_b64_tr_b16 v[78:79], v28 offset:41984\n"
"s_waitcnt lgkmcnt(6)\n"
"v_mfma_f32_32x32x16_bf16 v[176:191], v[120:123], v[64:67], 0\n"
"ds_read_b64_tr_b16 v[80:81], v28 offset:45056\n"
"ds_read_b64_tr_b16 v[82:83], v28 offset:46080\n"
"s_waitcnt lgkmcnt(6)\n"
"v_mfma_f32_32x32x16_bf16 v[176:191], v[124:127], v[68:71], v[176:191]\n"
"ds_read_b64_tr_b16 v[84:85], v28 offset:49152\n"
"ds_read_b64_tr_b16 v[86:87], v28 offset:50176\n"
"s_waitcnt lgkmcnt(6)\n"
"v_mfma_f32_32x32x16_bf16 v[176:191], v[128:131], v[72:75], v[176:191]\n"
"ds_read_b64_tr_b16 v[88:89], v28 offset:53248\n"
"ds_read_b64_tr_b16 v[90:91], v28 offset:54272\n"
"s_waitcnt lgkmcnt(6)\n"
"v_mfma_f32_32x32x16_bf16 v[176:191], v[132:135], v[76:79], v[176:191]\n"
"s_waitcnt lgkmcnt(4)\n"
"v_mfma_f32_32x32x16_bf16 v[176:191], v[136:139], v[80:83], v[176:191]\n"
"s_waitcnt lgkmcnt(2)\n"
"v_mfma_f32_32x32x16_bf16 v[176:191], v[140:143], v[84:87], v[176:191]\n"
"s_waitcnt lgkmcnt(0)\n"
"v_mfma_f32_32x32x16_bf16 v[176:191], v[144:147], v[88:91], v[176:191]\n"
"s_nop 11\n"
"ds_write_b64 v31, v[176:177] offset:15360\n"
"s_waitcnt lgkmcnt(0)\n"
"s_barrier\n"
"s_cmp_lg_u32 s43, 0\n"
"s_cbranch_scc1 Lpb1_%=\n"
"ds_read_b32 v35, v33 offset:15360\n"
"ds_read_b32 v36, v33 offset:16384\n"
"s_lshl_b32 s0, s62, 9\n"
"s_add_u32 s0, s0, s71\n"
"s_add_u32 s0, s0, 32768\n"
"s_waitcnt lgkmcnt(0)\n"
"v_add_f32 v22, v35, v36\n"
"buffer_store_dwordx2 v[22:23], v34, s[48:51], s0 offen sc1\n"
"Lpb1_%=:\n"
"v_lshlrev_b32 v6, 2, v6\n"
"v_lshlrev_b32 v7, 2, v7\n"
"s_mov_b32 s68, 43520\n"
"s_mov_b32 s69, 47104\n"
"s_mov_b32 s72, 68096\n"
"s_mov_b32 s73, 71680\n"
"s_mov_b32 s86, 0x40000\n"
"v_lshlrev_b32 v9, 5, v3\n"
"s_lshl_b32 s0, s28, 6\n"
"v_add_u32 v9, s0, v9\n"
"s_lshl_b32 s18, s60, 8\n"
"s_lshl_b32 s1, s29, 7\n"
"s_add_u32 s18, s18, s1\n"
"s_add_u32 s18, s18, 24576\n"
"v_lshlrev_b32 v10, 2, v4\n"
"s_lshr_b32 s14, s60, 1\n"
"s_mul_i32 s14, s14, 114688\n"
"s_and_b32 s1, s60, 1\n"
"s_lshl_b32 s1, s1, 1\n"
"s_add_u32 s1, s1, s29\n"
"s_mul_i32 s1, s1, 7168\n"
"s_add_u32 s14, s14, s1\n"
"s_add_u32 s14, s14, 5586944\n"
"s_mul_i32 s1, s28, 28672\n"
"s_add_u32 s14, s14, s1\n"
"s_add_u32 s15, s14, 57344\n"
"s_add_u32 s16, s14, 4096\n"
"s_add_u32 s17, s15, 4096\n"
"v_lshlrev_b32 v11, 1, v26\n"
"v_lshlrev_b32 v12, 1, v27\n"
"v_lshlrev_b32 v26, 2, v26\n"
"v_lshlrev_b32 v27, 2, v27\n"
"buffer_load_dwordx2 v[40:41], v6, s[48:51], s68 offen offset:0 sc1\n"
"buffer_load_dwordx2 v[42:43], v6, s[48:51], s72 offen offset:0 sc1\n"
"buffer_load_dwordx2 v[44:45], v6, s[48:51], s69 offen offset:0 sc1\n"
"buffer_load_dwordx2 v[46:47], v6, s[48:51], s73 offen offset:0 sc1\n"
"buffer_load_dwordx2 v[48:49], v7, s[48:51], s68 offen offset:2048 sc1\n"
"buffer_load_dwordx2 v[50:51], v7, s[48:51], s72 offen offset:2048 sc1\n"
"buffer_load_dwordx2 v[52:53], v7, s[48:51], s69 offen offset:2048 sc1\n"
"buffer_load_dwordx2 v[54:55], v7, s[48:51], s73 offen offset:2048 sc1\n"
"s_waitcnt vmcnt(8)\n"
"s_barrier\n"
"ds_read_b64_tr_b16 v[64:65], v29 offset:0\n"
"ds_read_b64_tr_b16 v[66:67], v29 offset:1024\n"
"ds_read_b64_tr_b16 v[68:69], v29 offset:4096\n"
"ds_read_b64_tr_b16 v[70:71], v29 offset:5120\n"
"ds_read_b64_tr_b16 v[72:73], v29 offset:8192\n"
"ds_read_b64_tr_b16 v[74:75], v29 offset:9216\n"
"ds_read_b64_tr_b16 v[76:77], v29 offset:12288\n"
"ds_read_b64_tr_b16 v[78:79], v29 offset:13312\n"
"ds_read_b64_tr_b16 v[80:81], v29 offset:16384\n"
"ds_read_b64_tr_b16 v[82:83], v29 offset:17408\n"
"ds_read_b64_tr_b16 v[84:85], v29 offset:20480\n"
"ds_read_b64_tr_b16 v[86:87], v29 offset:21504\n"
"ds_read_b64_tr_b16 v[88:89], v29 offset:24576\n"
"ds_read_b64_tr_b16 v[90:91], v29 offset:25600\n"
"ds_read_b64_tr_b16 v[92:93], v29 offset:28672\n"
"ds_read_b64_tr_b16 v[94:95], v29 offset:29696\n"
"ds_read_b64_tr_b16 v[96:97], v29 offset:32768\n"
"ds_read_b64_tr_b16 v[98:99], v29 offset:33792\n"
"ds_read_b64_tr_b16 v[100:101], v29 offset:36864\n"
"ds_read_b64_tr_b16 v[102:103], v29 offset:37888\n"
"ds_read_b64_tr_b16 v[104:105], v29 offset:40960\n"
"ds_read_b64_tr_b16 v[106:107], v29 offset:41984\n"
"ds_read_b64_tr_b16 v[108:109], v29 offset:45056\n"
"ds_read_b64_tr_b16 v[110:111], v29 offset:46080\n"
"ds_read_b64_tr_b16 v[112:113], v29 offset:49152\n"
"ds_read_b64_tr_b16 v[114:115], v29 offset:50176\n"
"ds_read_b64_tr_b16 v[116:117], v29 offset:53248\n"
"ds_read_b64_tr_b16 v[118:119], v29 offset:54272\n"
"s_waitcnt lgkmcnt(14)\n"
"v_lshlrev_b32 v120, 16, v64\n"
"v_and_b32 v121, 0xffff0000, v64\n"
"v_lshlrev_b32 v122, 16, v65\n"
"v_and_b32 v123, 0xffff0000, v65\n"
"v_lshlrev_b32 v124, 16, v66\n"
"v_and_b32 v125, 0xffff0000, v66\n"
"v_lshlrev_b32 v126, 16, v67\n"
"v_and_b32 v127, 0xffff0000, v67\n"
"v_lshlrev_b32 v128, 16, v68\n"
"v_and_b32 v129, 0xffff0000, v68\n"
"v_lshlrev_b32 v130, 16, v69\n"
"v_and_b32 v131, 0xffff0000, v69\n"
"v_lshlrev_b32 v132, 16, v70\n"
"v_and_b32 v133, 0xffff0000, v70\n"
"v_lshlrev_b32 v134, 16, v71\n"
"v_and_b32 v135, 0xffff0000, v71\n"
"v_lshlrev_b32 v136, 16, v72\n"
"v_and_b32 v137, 0xffff0000, v72\n"
"v_lshlrev_b32 v138, 16, v73\n"
"v_and_b32 v139, 0xffff0000, v73\n"
"v_lshlrev_b32 v140, 16, v74\n"
"v_and_b32 v141, 0xffff0000, v74\n"
"v_lshlrev_b32 v142, 16, v75\n"
"v_and_b32 v143, 0xffff0000, v75\n"
"v_lshlrev_b32 v144, 16, v76\n"
"v_and_b32 v145, 0xffff0000, v76\n"
"v_lshlrev_b32 v146, 16, v77\n"
"v_and_b32 v147, 0xffff0000, v77\n"
"v_lshlrev_b32 v148, 16, v78\n"
"v_and_b32 v149, 0xffff0000, v78\n"
"v_lshlrev_b32 v150, 16, v79\n"
"v_and_b32 v151, 0xffff0000, v79\n"
"v_lshlrev_b32 v152, 16, v80\n"
"v_and_b32 v153, 0xffff0000, v80\n"
"v_lshlrev_b32 v154, 16, v81\n"
"v_and_b32 v155, 0xffff0000, v81\n"
"v_lshlrev_b32 v156, 16, v82\n"
"v_and_b32 v157, 0xffff0000, v82\n"
"v_lshlrev_b32 v158, 16, v83\n"
"v_and_b32 v159, 0xffff0000, v83\n"
"v_lshlrev_b32 v160, 16, v84\n"
"v_and_b32 v161, 0xffff0000, v84\n"
"v_lshlrev_b32 v162, 16, v85\n"
"v_and_b32 v163, 0xffff0000, v85\n"
"v_lshlrev_b32 v164, 16, v86\n"
"v_and_b32 v165, 0xffff0000, v86\n"
"v_lshlrev_b32 v166, 16, v87\n"
"v_and_b32 v167, 0xffff0000, v87\n"
"v_lshlrev_b32 v168, 16, v88\n"
"v_and_b32 v169, 0xffff0000, v88\n"
"v_lshlrev_b32 v170, 16, v89\n"
"v_and_b32 v171, 0xffff0000, v89\n"
"v_lshlrev_b32 v172, 16, v90\n"
"v_and_b32 v173, 0xffff0000, v90\n"
"v_lshlrev_b32 v174, 16, v91\n"
"v_and_b32 v175, 0xffff0000, v91\n"
"s_waitcnt lgkmcnt(13)\n"
"v_lshlrev_b32 v176, 16, v92\n"
"v_and_b32 v177, 0xffff0000, v92\n"
"v_lshlrev_b32 v178, 16, v93\n"
"v_and_b32 v179, 0xffff0000, v93\n"
"s_waitcnt lgkmcnt(12)\n"
"v_lshlrev_b32 v180, 16, v94\n"
"v_and_b32 v181, 0xffff0000, v94\n"
"v_lshlrev_b32 v182, 16, v95\n"
"v_and_b32 v183, 0xffff0000, v95\n"
"s_waitcnt lgkmcnt(11)\n"
"v_lshlrev_b32 v184, 16, v96\n"
"v_and_b32 v185, 0xffff0000, v96\n"
"v_lshlrev_b32 v186, 16, v97\n"
"v_and_b32 v187, 0xffff0000, v97\n"
"s_waitcnt lgkmcnt(10)\n"
"v_lshlrev_b32 v188, 16, v98\n"
"v_and_b32 v189, 0xffff0000, v98\n"
"v_lshlrev_b32 v190, 16, v99\n"
"v_and_b32 v191, 0xffff0000, v99\n"
"s_waitcnt lgkmcnt(9)\n"
"v_lshlrev_b32 v192, 16, v100\n"
"v_and_b32 v193, 0xffff0000, v100\n"
"v_lshlrev_b32 v194, 16, v101\n"
"v_and_b32 v195, 0xffff0000, v101\n"
"s_waitcnt lgkmcnt(8)\n"
"v_lshlrev_b32 v196, 16, v102\n"
"v_and_b32 v197, 0xffff0000, v102\n"
"v_lshlrev_b32 v198, 16, v103\n"
"v_and_b32 v199, 0xffff0000, v103\n"
"s_waitcnt lgkmcnt(7)\n"
"v_lshlrev_b32 v200, 16, v104\n"
"v_and_b32 v201, 0xffff0000, v104\n"
"v_lshlrev_b32 v202, 16, v105\n"
"v_and_b32 v203, 0xffff0000, v105\n"
"s_waitcnt lgkmcnt(6)\n"
"v_lshlrev_b32 v204, 16, v106\n"
"v_and_b32 v205, 0xffff0000, v106\n"
"v_lshlrev_b32 v206, 16, v107\n"
"v_and_b32 v207, 0xffff0000, v107\n"
"s_waitcnt lgkmcnt(5)\n"
"v_lshlrev_b32 v208, 16, v108\n"
"v_and_b32 v209, 0xffff0000, v108\n"
"v_lshlrev_b32 v210, 16, v109\n"
"v_and_b32 v211, 0xffff0000, v109\n"
"s_waitcnt lgkmcnt(4)\n"
"v_lshlrev_b32 v212, 16, v110\n"
"v_and_b32 v213, 0xffff0000, v110\n"
"v_lshlrev_b32 v214, 16, v111\n"
"v_and_b32 v215, 0xffff0000, v111\n"
"s_waitcnt lgkmcnt(3)\n"
"v_lshlrev_b32 v216, 16, v112\n"
"v_and_b32 v217, 0xffff0000, v112\n"
"v_lshlrev_b32 v218, 16, v113\n"
"v_and_b32 v219, 0xffff0000, v113\n"
"s_waitcnt lgkmcnt(2)\n"
"v_lshlrev_b32 v220, 16, v114\n"
"v_and_b32 v221, 0xffff0000, v114\n"
"v_lshlrev_b32 v222, 16, v115\n"
"v_and_b32 v223, 0xffff0000, v115\n"
"s_waitcnt lgkmcnt(1)\n"
"v_lshlrev_b32 v224, 16, v116\n"
"v_and_b32 v225, 0xffff0000, v116\n"
"v_lshlrev_b32 v226, 16, v117\n"
"v_and_b32 v227, 0xffff0000, v117\n"
"s_waitcnt lgkmcnt(0)\n"
"v_lshlrev_b32 v228, 16, v118\n"
"v_and_b32 v229, 0xffff0000, v118\n"
"v_lshlrev_b32 v230, 16, v119\n"
"v_and_b32 v231, 0xffff0000, v119\n"
"s_waitcnt vmcnt(7)\n"
"v_xor_b32 v35, v41, v23\n"
"s_waitcnt vmcnt(6)\n"
"v_bitop3_b32 v35, v35, v43, v23 bitop3:0xf6\n"
"s_waitcnt vmcnt(5)\n"
"v_bitop3_b32 v35, v35, v45, v23 bitop3:0xf6\n"
"s_waitcnt vmcnt(4)\n"
"v_bitop3_b32 v35, v35, v47, v23 bitop3:0xf6\n"
"s_waitcnt vmcnt(3)\n"
"v_bitop3_b32 v35, v35, v49, v23 bitop3:0xf6\n"
"s_waitcnt vmcnt(2)\n"
"v_bitop3_b32 v35, v35, v51, v23 bitop3:0xf6\n"
"s_waitcnt vmcnt(1)\n"
"v_bitop3_b32 v35, v35, v53, v23 bitop3:0xf6\n"
"s_waitcnt vmcnt(0)\n"
"v_bitop3_b32 v35, v35, v55, v23 bitop3:0xf6\n"
"v_cmp_ne_u32 vcc, 0, v35\n"
"s_nop 4\n"
"s_cbranch_vccz Lcsd_%=\n"
"Lcs_%=:\n"
"s_sub_u32 s86, s86, 1\n"
"s_cbranch_scc1 Lcsd_%=\n"
"buffer_load_dwordx2 v[40:41], v6, s[48:51], s68 offen offset:0 sc1\n"
"buffer_load_dwordx2 v[42:43], v6, s[48:51], s72 offen offset:0 sc1\n"
"buffer_load_dwordx2 v[44:45], v6, s[48:51], s69 offen offset:0 sc1\n"
"buffer_load_dwordx2 v[46:47], v6, s[48:51], s73 offen offset:0 sc1\n"
"buffer_load_dwordx2 v[48:49], v7, s[48:51], s68 offen offset:2048 sc1\n"
"buffer_load_dwordx2 v[50:51], v7, s[48:51], s72 offen offset:2048 sc1\n"
"buffer_load_dwordx2 v[52:53], v7, s[48:51], s69 offen offset:2048 sc1\n"
"buffer_load_dwordx2 v[54:55], v7, s[48:51], s73 offen offset:2048 sc1\n"
"s_waitcnt vmcnt(7)\n"
"v_xor_b32 v35, v41, v23\n"
"s_waitcnt vmcnt(6)\n"
"v_bitop3_b32 v35, v35, v43, v23 bitop3:0xf6\n"
"s_waitcnt vmcnt(5)\n"
"v_bitop3_b32 v35, v35, v45, v23 bitop3:0xf6\n"
"s_waitcnt vmcnt(4)\n"
"v_bitop3_b32 v35, v35, v47, v23 bitop3:0xf6\n"
"s_waitcnt vmcnt(3)\n"
"v_bitop3_b32 v35, v35, v49, v23 bitop3:0xf6\n"
"s_waitcnt vmcnt(2)\n"
"v_bitop3_b32 v35, v35, v51, v23 bitop3:0xf6\n"
"s_waitcnt vmcnt(1)\n"
"v_bitop3_b32 v35, v35, v53, v23 bitop3:0xf6\n"
"s_waitcnt vmcnt(0)\n"
"v_bitop3_b32 v35, v35, v55, v23 bitop3:0xf6\n"
"v_cmp_ne_u32 vcc, 0, v35\n"
"s_nop 4\n"
"s_cbranch_vccnz Lcs_%=\n"
"Lcsd_%=:\n"
"v_add_f32 v40, v40, v42\n"
"v_add_f32 v44, v44, v46\n"
"v_cvt_pk_bf16_f32 v40, v40, v44\n"
"v_and_b32 v44, 0xffff0000, v40\n"
"v_add_f32 v44, 1.0, v44\n"
"v_cvt_pk_bf16_f32 v44, v44, v44\n"
"ds_write_b16 v11, v40 offset:10240\n"
"v_lshlrev_b32 v46, 16, v44\n"
"ds_write_b16 v11, v44 offset:9216\n"
"ds_write_b32 v26, v46 offset:11264\n"
"v_add_f32 v48, v48, v50\n"
"v_add_f32 v52, v52, v54\n"
"v_cvt_pk_bf16_f32 v48, v48, v52\n"
"v_and_b32 v52, 0xffff0000, v48\n"
"v_add_f32 v52, 1.0, v52\n"
"v_cvt_pk_bf16_f32 v52, v52, v52\n"
"ds_write_b16 v12, v48 offset:10240\n"
"v_lshlrev_b32 v54, 16, v52\n"
"ds_write_b16 v12, v52 offset:9216\n"
"ds_write_b32 v27, v54 offset:11264\n"
"s_waitcnt lgkmcnt(0)\n"
"s_barrier\n"
"ds_read_b128 v[48:51], v30 offset:9216\n"
"ds_read_b128 v[52:55], v30 offset:9280\n"
"ds_read_b128 v[56:59], v30 offset:9344\n"
"s_waitcnt lgkmcnt(2)\n"
"v_mfma_f32_32x32x16_bf16 a[0:15], v[48:51], v[64:67], 0\n"
"ds_read_b128 v[60:63], v30 offset:9408\n"
"s_waitcnt lgkmcnt(2)\n"
"v_mfma_f32_32x32x16_bf16 a[0:15], v[52:55], v[68:71], a[0:15]\n"
"ds_read_b128 v[48:51], v30 offset:9472\n"
"s_waitcnt lgkmcnt(2)\n"
"v_mfma_f32_32x32x16_bf16 a[0:15], v[56:59], v[72:75], a[0:15]\n"
"ds_read_b128 v[52:55], v30 offset:9536\n"
"s_waitcnt lgkmcnt(2)\n"
"v_mfma_f32_32x32x16_bf16 a[0:15], v[60:63], v[76:79], a[0:15]\n"
"ds_read_b128 v[56:59], v30 offset:9600\n"
"s_waitcnt lgkmcnt(2)\n"
"v_mfma_f32_32x32x16_bf16 a[0:15], v[48:51], v[80:83], a[0:15]\n"
"ds_read_b128 v[60:63], v30 offset:9664\n"
"s_waitcnt lgkmcnt(2)\n"
"v_mfma_f32_32x32x16_bf16 a[0:15], v[52:55], v[84:87], a[0:15]\n"
"ds_read_b128 v[48:51], v30 offset:9728\n"
"s_waitcnt lgkmcnt(2)\n"
"v_mfma_f32_32x32x16_bf16 a[0:15], v[56:59], v[88:91], a[0:15]\n"
"ds_read_b128 v[52:55], v30 offset:9792\n"
"s_waitcnt lgkmcnt(2)\n"
"v_mfma_f32_32x32x16_bf16 a[0:15], v[60:63], v[92:95], a[0:15]\n"
"ds_read_b128 v[56:59], v30 offset:9856\n"
"s_waitcnt lgkmcnt(2)\n"
"v_mfma_f32_32x32x16_bf16 a[0:15], v[48:51], v[96:99], a[0:15]\n"
"ds_read_b128 v[60:63], v30 offset:9920\n"
"s_waitcnt lgkmcnt(2)\n"
"v_mfma_f32_32x32x16_bf16 a[0:15], v[52:55], v[100:103], a[0:15]\n"
"ds_read_b128 v[48:51], v30 offset:9984\n"
"s_waitcnt lgkmcnt(2)\n"
"v_mfma_f32_32x32x16_bf16 a[0:15], v[56:59], v[104:107], a[0:15]\n"
"ds_read_b128 v[52:55], v30 offset:10048\n"
"s_waitcnt lgkmcnt(2)\n"
"v_mfma_f32_32x32x16_bf16 a[0:15], v[60:63], v[108:111], a[0:15]\n"
"s_waitcnt lgkmcnt(1)\n"
"v_mfma_f32_32x32x16_bf16 a[0:15], v[48:51], v[112:115], a[0:15]\n"
"s_waitcnt lgkmcnt(0)\n"
"v_mfma_f32_32x32x16_bf16 a[0:15], v[52:55], v[116:119], a[0:15]\n"
"ds_read_b128 v[234:237], v9 offset:11264\n"
"ds_read_b128 v[238:241], v9 offset:11280\n"
"ds_read_b128 v[242:245], v9 offset:11392\n"
"ds_read_b128 v[246:249], v9 offset:11408\n"
"s_nop 7\n"
"ds_write_b64 v31, a[0:1] offset:13312\n"
"s_waitcnt lgkmcnt(0)\n"
"s_barrier\n"
"ds_read_b64 v[36:37], v32 offset:13312\n"
"ds_read_b64 v[38:39], v32 offset:14336\n"
"s_waitcnt lgkmcnt(0)\n"
"v_pk_add_f32 v[36:37], v[36:37], v[38:39]\n"
"v_mul_f32 v37, 0xbfb8aa3b, v37\n"
"v_mul_f32 v36, 0xbb124925, v36\n"
"v_pk_fma_f32 v[120:121], v[120:121], v[234:235], v[36:37] op_sel_hi:[1,1,0]\n"
"v_pk_fma_f32 v[122:123], v[122:123], v[236:237], v[36:37] op_sel_hi:[1,1,0]\n"
"v_pk_fma_f32 v[124:125], v[124:125], v[238:239], v[36:37] op_sel_hi:[1,1,0]\n"
"v_pk_fma_f32 v[126:127], v[126:127], v[240:241], v[36:37] op_sel_hi:[1,1,0]\n"
"ds_read_b128 v[234:237], v9 offset:11520\n"
"ds_read_b128 v[238:241], v9 offset:11536\n"
"v_cvt_pk_bf16_f32 v64, v120, v121\n"
"v_cvt_pk_bf16_f32 v65, v122, v123\n"
"v_cvt_pk_bf16_f32 v66, v124, v125\n"
"v_cvt_pk_bf16_f32 v67, v126, v127\n"
"buffer_store_dwordx4 v[64:67], v232, s[48:51], s14 offen offset:0 sc1\n"
"v_pk_fma_f32 v[128:129], v[128:129], v[242:243], v[36:37] op_sel_hi:[1,1,0]\n"
"v_pk_fma_f32 v[130:131], v[130:131], v[244:245], v[36:37] op_sel_hi:[1,1,0]\n"
"v_pk_fma_f32 v[132:133], v[132:133], v[246:247], v[36:37] op_sel_hi:[1,1,0]\n"
"v_pk_fma_f32 v[134:135], v[134:135], v[248:249], v[36:37] op_sel_hi:[1,1,0]\n"
"ds_read_b128 v[242:245], v9 offset:11648\n"
"ds_read_b128 v[246:249], v9 offset:11664\n"
"v_cvt_pk_bf16_f32 v68, v128, v129\n"
"v_cvt_pk_bf16_f32 v69, v130, v131\n"
"v_cvt_pk_bf16_f32 v70, v132, v133\n"
"v_cvt_pk_bf16_f32 v71, v134, v135\n"
"buffer_store_dwordx4 v[68:71], v232, s[48:51], s15 offen offset:0 sc1\n"
"s_waitcnt lgkmcnt(3)\n"
"v_pk_fma_f32 v[136:137], v[136:137], v[234:235], v[36:37] op_sel_hi:[1,1,0]\n"
"v_pk_fma_f32 v[138:139], v[138:139], v[236:237], v[36:37] op_sel_hi:[1,1,0]\n"
"s_waitcnt lgkmcnt(2)\n"
"v_pk_fma_f32 v[140:141], v[140:141], v[238:239], v[36:37] op_sel_hi:[1,1,0]\n"
"v_pk_fma_f32 v[142:143], v[142:143], v[240:241], v[36:37] op_sel_hi:[1,1,0]\n"
"ds_read_b128 v[234:237], v9 offset:11776\n"
"ds_read_b128 v[238:241], v9 offset:11792\n"
"v_cvt_pk_bf16_f32 v72, v136, v137\n"
"v_cvt_pk_bf16_f32 v73, v138, v139\n"
"v_cvt_pk_bf16_f32 v74, v140, v141\n"
"v_cvt_pk_bf16_f32 v75, v142, v143\n"
"buffer_store_dwordx4 v[72:75], v232, s[48:51], s14 offen offset:1024 sc1\n"
"s_waitcnt lgkmcnt(3)\n"
"v_pk_fma_f32 v[144:145], v[144:145], v[242:243], v[36:37] op_sel_hi:[1,1,0]\n"
"v_pk_fma_f32 v[146:147], v[146:147], v[244:245], v[36:37] op_sel_hi:[1,1,0]\n"
"s_waitcnt lgkmcnt(2)\n"
"v_pk_fma_f32 v[148:149], v[148:149], v[246:247], v[36:37] op_sel_hi:[1,1,0]\n"
"v_pk_fma_f32 v[150:151], v[150:151], v[248:249], v[36:37] op_sel_hi:[1,1,0]\n"
"ds_read_b128 v[242:245], v9 offset:11904\n"
"ds_read_b128 v[246:249], v9 offset:11920\n"
"v_cvt_pk_bf16_f32 v76, v144, v145\n"
"v_cvt_pk_bf16_f32 v77, v146, v147\n"
"v_cvt_pk_bf16_f32 v78, v148, v149\n"
"v_cvt_pk_bf16_f32 v79, v150, v151\n"
"buffer_store_dwordx4 v[76:79], v232, s[48:51], s15 offen offset:1024 sc1\n"
"s_waitcnt lgkmcnt(3)\n"
"v_pk_fma_f32 v[152:153], v[152:153], v[234:235], v[36:37] op_sel_hi:[1,1,0]\n"
"v_pk_fma_f32 v[154:155], v[154:155], v[236:237], v[36:37] op_sel_hi:[1,1,0]\n"
"s_waitcnt lgkmcnt(2)\n"
"v_pk_fma_f32 v[156:157], v[156:157], v[238:239], v[36:37] op_sel_hi:[1,1,0]\n"
"v_pk_fma_f32 v[158:159], v[158:159], v[240:241], v[36:37] op_sel_hi:[1,1,0]\n"
"ds_read_b128 v[234:237], v9 offset:12032\n"
"ds_read_b128 v[238:241], v9 offset:12048\n"
"v_cvt_pk_bf16_f32 v80, v152, v153\n"
"v_cvt_pk_bf16_f32 v81, v154, v155\n"
"v_cvt_pk_bf16_f32 v82, v156, v157\n"
"v_cvt_pk_bf16_f32 v83, v158, v159\n"
"buffer_store_dwordx4 v[80:83], v232, s[48:51], s14 offen offset:2048 sc1\n"
"s_waitcnt lgkmcnt(3)\n"
"v_pk_fma_f32 v[160:161], v[160:161], v[242:243], v[36:37] op_sel_hi:[1,1,0]\n"
"v_pk_fma_f32 v[162:163], v[162:163], v[244:245], v[36:37] op_sel_hi:[1,1,0]\n"
"s_waitcnt lgkmcnt(2)\n"
"v_pk_fma_f32 v[164:165], v[164:165], v[246:247], v[36:37] op_sel_hi:[1,1,0]\n"
"v_pk_fma_f32 v[166:167], v[166:167], v[248:249], v[36:37] op_sel_hi:[1,1,0]\n"
"ds_read_b128 v[242:245], v9 offset:12160\n"
"ds_read_b128 v[246:249], v9 offset:12176\n"
"v_cvt_pk_bf16_f32 v84, v160, v161\n"
"v_cvt_pk_bf16_f32 v85, v162, v163\n"
"v_cvt_pk_bf16_f32 v86, v164, v165\n"
"v_cvt_pk_bf16_f32 v87, v166, v167\n"
"buffer_store_dwordx4 v[84:87], v232, s[48:51], s15 offen offset:2048 sc1\n"
"s_waitcnt lgkmcnt(3)\n"
"v_pk_fma_f32 v[168:169], v[168:169], v[234:235], v[36:37] op_sel_hi:[1,1,0]\n"
"v_pk_fma_f32 v[170:171], v[170:171], v[236:237], v[36:37] op_sel_hi:[1,1,0]\n"
"s_waitcnt lgkmcnt(2)\n"
"v_pk_fma_f32 v[172:173], v[172:173], v[238:239], v[36:37] op_sel_hi:[1,1,0]\n"
"v_pk_fma_f32 v[174:175], v[174:175], v[240:241], v[36:37] op_sel_hi:[1,1,0]\n"
"ds_read_b128 v[234:237], v9 offset:12288\n"
"ds_read_b128 v[238:241], v9 offset:12304\n"
"v_cvt_pk_bf16_f32 v88, v168, v169\n"
"v_cvt_pk_bf16_f32 v89, v170, v171\n"
"v_cvt_pk_bf16_f32 v90, v172, v173\n"
"v_cvt_pk_bf16_f32 v91, v174, v175\n"
"buffer_store_dwordx4 v[88:91], v232, s[48:51], s14 offen offset:3072 sc1\n"
"s_waitcnt lgkmcnt(3)\n"
"v_pk_fma_f32 v[176:177], v[176:177], v[242:243], v[36:37] op_sel_hi:[1,1,0]\n"
"v_pk_fma_f32 v[178:179], v[178:179], v[244:245], v[36:37] op_sel_hi:[1,1,0]\n"
"s_waitcnt lgkmcnt(2)\n"
"v_pk_fma_f32 v[180:181], v[180:181], v[246:247], v[36:37] op_sel_hi:[1,1,0]\n"
"v_pk_fma_f32 v[182:183], v[182:183], v[248:249], v[36:37] op_sel_hi:[1,1,0]\n"
"ds_read_b128 v[242:245], v9 offset:12416\n"
"ds_read_b128 v[246:249], v9 offset:12432\n"
"v_cvt_pk_bf16_f32 v92, v176, v177\n"
"v_cvt_pk_bf16_f32 v93, v178, v179\n"
"v_cvt_pk_bf16_f32 v94, v180, v181\n"
"v_cvt_pk_bf16_f32 v95, v182, v183\n"
"buffer_store_dwordx4 v[92:95], v232, s[48:51], s15 offen offset:3072 sc1\n"
"s_waitcnt lgkmcnt(3)\n"
"v_pk_fma_f32 v[184:185], v[184:185], v[234:235], v[36:37] op_sel_hi:[1,1,0]\n"
"v_pk_fma_f32 v[186:187], v[186:187], v[236:237], v[36:37] op_sel_hi:[1,1,0]\n"
"s_waitcnt lgkmcnt(2)\n"
"v_pk_fma_f32 v[188:189], v[188:189], v[238:239], v[36:37] op_sel_hi:[1,1,0]\n"
"v_pk_fma_f32 v[190:191], v[190:191], v[240:241], v[36:37] op_sel_hi:[1,1,0]\n"
"ds_read_b128 v[234:237], v9 offset:12544\n"
"ds_read_b128 v[238:241], v9 offset:12560\n"
"v_cvt_pk_bf16_f32 v96, v184, v185\n"
"v_cvt_pk_bf16_f32 v97, v186, v187\n"
"v_cvt_pk_bf16_f32 v98, v188, v189\n"
"v_cvt_pk_bf16_f32 v99, v190, v191\n"
"buffer_store_dwordx4 v[96:99], v232, s[48:51], s16 offen offset:0 sc1\n"
"s_waitcnt lgkmcnt(3)\n"
"v_pk_fma_f32 v[192:193], v[192:193], v[242:243], v[36:37] op_sel_hi:[1,1,0]\n"
"v_pk_fma_f32 v[194:195], v[194:195], v[244:245], v[36:37] op_sel_hi:[1,1,0]\n"
"s_waitcnt lgkmcnt(2)\n"
"v_pk_fma_f32 v[196:197], v[196:197], v[246:247], v[36:37] op_sel_hi:[1,1,0]\n"
"v_pk_fma_f32 v[198:199], v[198:199], v[248:249], v[36:37] op_sel_hi:[1,1,0]\n"
"ds_read_b128 v[242:245], v9 offset:12672\n"
"ds_read_b128 v[246:249], v9 offset:12688\n"
"v_cvt_pk_bf16_f32 v100, v192, v193\n"
"v_cvt_pk_bf16_f32 v101, v194, v195\n"
"v_cvt_pk_bf16_f32 v102, v196, v197\n"
"v_cvt_pk_bf16_f32 v103, v198, v199\n"
"buffer_store_dwordx4 v[100:103], v232, s[48:51], s17 offen offset:0 sc1\n"
"s_waitcnt lgkmcnt(3)\n"
"v_pk_fma_f32 v[200:201], v[200:201], v[234:235], v[36:37] op_sel_hi:[1,1,0]\n"
"v_pk_fma_f32 v[202:203], v[202:203], v[236:237], v[36:37] op_sel_hi:[1,1,0]\n"
"s_waitcnt lgkmcnt(2)\n"
"v_pk_fma_f32 v[204:205], v[204:205], v[238:239], v[36:37] op_sel_hi:[1,1,0]\n"
"v_pk_fma_f32 v[206:207], v[206:207], v[240:241], v[36:37] op_sel_hi:[1,1,0]\n"
"ds_read_b128 v[234:237], v9 offset:12800\n"
"ds_read_b128 v[238:241], v9 offset:12816\n"
"v_cvt_pk_bf16_f32 v104, v200, v201\n"
"v_cvt_pk_bf16_f32 v105, v202, v203\n"
"v_cvt_pk_bf16_f32 v106, v204, v205\n"
"v_cvt_pk_bf16_f32 v107, v206, v207\n"
"buffer_store_dwordx4 v[104:107], v232, s[48:51], s16 offen offset:1024 sc1\n"
"s_waitcnt lgkmcnt(3)\n"
"v_pk_fma_f32 v[208:209], v[208:209], v[242:243], v[36:37] op_sel_hi:[1,1,0]\n"
"v_pk_fma_f32 v[210:211], v[210:211], v[244:245], v[36:37] op_sel_hi:[1,1,0]\n"
"s_waitcnt lgkmcnt(2)\n"
"v_pk_fma_f32 v[212:213], v[212:213], v[246:247], v[36:37] op_sel_hi:[1,1,0]\n"
"v_pk_fma_f32 v[214:215], v[214:215], v[248:249], v[36:37] op_sel_hi:[1,1,0]\n"
"ds_read_b128 v[242:245], v9 offset:12928\n"
"ds_read_b128 v[246:249], v9 offset:12944\n"
"v_cvt_pk_bf16_f32 v108, v208, v209\n"
"v_cvt_pk_bf16_f32 v109, v210, v211\n"
"v_cvt_pk_bf16_f32 v110, v212, v213\n"
"v_cvt_pk_bf16_f32 v111, v214, v215\n"
"buffer_store_dwordx4 v[108:111], v232, s[48:51], s17 offen offset:1024 sc1\n"
"s_waitcnt lgkmcnt(3)\n"
"v_pk_fma_f32 v[216:217], v[216:217], v[234:235], v[36:37] op_sel_hi:[1,1,0]\n"
"v_pk_fma_f32 v[218:219], v[218:219], v[236:237], v[36:37] op_sel_hi:[1,1,0]\n"
"s_waitcnt lgkmcnt(2)\n"
"v_pk_fma_f32 v[220:221], v[220:221], v[238:239], v[36:37] op_sel_hi:[1,1,0]\n"
"v_pk_fma_f32 v[222:223], v[222:223], v[240:241], v[36:37] op_sel_hi:[1,1,0]\n"
"v_cvt_pk_bf16_f32 v112, v216, v217\n"
"v_cvt_pk_bf16_f32 v113, v218, v219\n"
"v_cvt_pk_bf16_f32 v114, v220, v221\n"
"v_cvt_pk_bf16_f32 v115, v222, v223\n"
"buffer_store_dwordx4 v[112:115], v232, s[48:51], s16 offen offset:2048 sc1\n"
"s_waitcnt lgkmcnt(1)\n"
"v_pk_fma_f32 v[224:225], v[224:225], v[242:243], v[36:37] op_sel_hi:[1,1,0]\n"
"v_pk_fma_f32 v[226:227], v[226:227], v[244:245], v[36:37] op_sel_hi:[1,1,0]\n"
"s_waitcnt lgkmcnt(0)\n"
"v_pk_fma_f32 v[228:229], v[228:229], v[246:247], v[36:37] op_sel_hi:[1,1,0]\n"
"v_pk_fma_f32 v[230:231], v[230:231], v[248:249], v[36:37] op_sel_hi:[1,1,0]\n"
"v_cvt_pk_bf16_f32 v116, v224, v225\n"
"v_cvt_pk_bf16_f32 v117, v226, v227\n"
"v_cvt_pk_bf16_f32 v118, v228, v229\n"
"v_cvt_pk_bf16_f32 v119, v230, v231\n"
"buffer_store_dwordx4 v[116:119], v232, s[48:51], s17 offen offset:2048 sc1\n"
"buffer_store_dword v37, v10, s[48:51], s18 offen sc1\n"
"s_waitcnt vmcnt(0)\n"
"s_barrier\n"
"s_lshl_b32 s0, s60, 7\n"
"s_add_u32 s0, s0, 4096\n"
"s_cmp_lg_u32 s43, 0\n"
"s_cbranch_scc1 Lhend_%=\n"
"s_mov_b64 exec, 1\n"
"buffer_store_dword v23, v8, s[48:51], s0 offen sc1\n"
"s_mov_b64 exec, -1\n"
"s_branch Lhend_%=\n"
"Lgate_%=:\n"
"s_add_u32 s1, s1, 172032\n"
"s_add_u32 m0, s66, 49152\n"
"s_nop 0\n"
"buffer_load_dwordx4 v12, s[52:55], s1 offen lds\n"
"s_add_u32 s1, s1, 172032\n"
"s_add_u32 m0, s66, 53248\n"
"s_nop 0\n"
"buffer_load_dwordx4 v12, s[52:55], s1 offen lds\n"
"s_add_u32 s1, s1, 172032\n"
"s_add_u32 m0, s66, 57344\n"
"s_nop 0\n"
"buffer_load_dwordx4 v12, s[52:55], s1 offen lds\n"
"s_add_u32 s1, s1, 172032\n"
"s_mov_b32 s74, s1\n"
"s_waitcnt vmcnt(17)\n"
"v_add_u32 v23, 1, v23\n"
"s_waitcnt vmcnt(16)\n"
"ds_write_b32 v38, v16 offset:0\n"
"s_waitcnt vmcnt(15)\n"
"ds_write_b32 v38, v17 offset:128\n"
"s_waitcnt vmcnt(14)\n"
"ds_write_b32 v38, v18 offset:256\n"
"s_waitcnt vmcnt(13)\n"
"ds_write_b32 v38, v19 offset:384\n"
"s_waitcnt vmcnt(12)\n"
"ds_write_b32 v38, v20 offset:512\n"
"s_waitcnt vmcnt(11)\n"
"ds_write_b32 v38, v21 offset:640\n"
"s_waitcnt vmcnt(10)\n"
"ds_write_b32 v38, v22 offset:768\n"
"v_readfirstlane_b32 s39, v23\n"
"v_dot2c_f32_bf16 v42, v16, v44\n"
"v_dot2c_f32_bf16 v43, v16, v16\n"
"v_dot2c_f32_bf16 v42, v17, v44\n"
"v_dot2c_f32_bf16 v43, v17, v17\n"
"v_dot2c_f32_bf16 v42, v18, v44\n"
"v_dot2c_f32_bf16 v43, v18, v18\n"
"v_dot2c_f32_bf16 v42, v19, v44\n"
"v_dot2c_f32_bf16 v43, v19, v19\n"
"v_dot2c_f32_bf16 v42, v20, v44\n"
"v_dot2c_f32_bf16 v43, v20, v20\n"
"v_dot2c_f32_bf16 v42, v21, v44\n"
"v_dot2c_f32_bf16 v43, v21, v21\n"
"v_dot2c_f32_bf16 v42, v22, v44\n"
"v_dot2c_f32_bf16 v43, v22, v22\n"
"s_waitcnt lgkmcnt(0)\n"
"s_barrier\n"
"ds_read_b32 v96, v39 offset:0\n"
"ds_read_b32 v98, v39 offset:912\n"
"ds_read_b32 v100, v39 offset:256\n"
"ds_read_b32 v102, v39 offset:1168\n"
"ds_read_b32 v104, v39 offset:512\n"
"ds_read_b32 v106, v39 offset:1424\n"
"ds_read_b32 v108, v39 offset:768\n"
"ds_read_b32 v110, v39 offset:1680\n"
"v_add_f32_dpp v42, v42, v42 quad_perm:[1,0,3,2] row_mask:0xf bank_mask:0xf bound_ctrl:1\n"
"v_add_f32_dpp v43, v43, v43 quad_perm:[1,0,3,2] row_mask:0xf bank_mask:0xf bound_ctrl:1\n"
"v_mov_b32 v97, v23\n"
"v_mov_b32 v99, v23\n"
"v_add_f32_dpp v42, v42, v42 quad_perm:[2,3,0,1] row_mask:0xf bank_mask:0xf bound_ctrl:1\n"
"v_add_f32_dpp v43, v43, v43 quad_perm:[2,3,0,1] row_mask:0xf bank_mask:0xf bound_ctrl:1\n"
"v_mov_b32 v101, v23\n"
"v_mov_b32 v103, v23\n"
"v_add_f32_dpp v42, v42, v42 row_half_mirror row_mask:0xf bank_mask:0xf bound_ctrl:1\n"
"v_add_f32_dpp v43, v43, v43 row_half_mirror row_mask:0xf bank_mask:0xf bound_ctrl:1\n"
"v_mov_b32 v105, v23\n"
"v_mov_b32 v107, v23\n"
"v_add_f32_dpp v42, v42, v42 row_mirror row_mask:0xf bank_mask:0xf bound_ctrl:1\n"
"v_add_f32_dpp v43, v43, v43 row_mirror row_mask:0xf bank_mask:0xf bound_ctrl:1\n"
"v_mov_b32 v109, v23\n"
"v_mov_b32 v111, v23\n"
"v_mov_b32 v45, v42\n"
"v_mov_b32 v44, v43\n"
"s_add_u32 s0, s94, 0\n"
"s_waitcnt lgkmcnt(6)\n"
"buffer_store_dwordx4 v[96:99], v40, s[48:51], s0 offen sc1\n"
"v_permlane16_swap_b32 v42, v45\n"
"v_permlane16_swap_b32 v43, v44\n"
"v_add_f32 v42, v42, v45\n"
"v_add_f32 v43, v43, v44\n"
"s_add_u32 s0, s94, 16384\n"
"s_waitcnt lgkmcnt(4)\n"
"buffer_store_dwordx4 v[100:103], v40, s[48:51], s0 offen sc1\n"
"s_add_u32 s0, s94, 32768\n"
"s_waitcnt lgkmcnt(2)\n"
"buffer_store_dwordx4 v[104:107], v40, s[48:51], s0 offen sc1\n"
"s_cmp_gt_u32 s43, 1\n"
"s_cbranch_scc1 Lxi_%=\n"
"s_add_u32 s0, s94, 49152\n"
"s_waitcnt lgkmcnt(0)\n"
"buffer_store_dwordx4 v[108:111], v40, s[48:51], s0 offen sc1\n"
"Lxi_%=:\n"
"v_mul_f32 v42, 0x3b124925, v42\n"
"v_mul_f32 v43, 0x3b124925, v43\n"
"v_fma_f32 v43, -v42, v42, v43\n"
"v_max_f32 v43, 0, v43\n"
"v_cvt_pk_bf16_f32 v43, v43, v43\n"
"v_lshlrev_b32 v43, 16, v43\n"
"v_add_f32 v43, 0x3727c5ac, v43\n"
"v_cvt_pk_bf16_f32 v43, v43, v43\n"
"v_lshlrev_b32 v43, 16, v43\n"
"v_sqrt_f32 v43, v43\n"
"s_nop 0\n"
"v_cvt_pk_bf16_f32 v22, v42, v43\n"
"s_mov_b32 exec_lo, 1\n"
"s_mov_b32 exec_hi, 1\n"
"buffer_store_dwordx2 v[22:23], v41, s[48:51], s2 offen sc1\n"
"s_mov_b64 exec, -1\n"
"ds_read_b128 v[120:123], v2 offset:8192\n"
"ds_read_b128 v[124:127], v2 offset:8256\n"
"ds_read_b128 v[128:131], v2 offset:8320\n"
"ds_read_b128 v[132:135], v2 offset:8384\n"
"ds_read_b128 v[136:139], v2 offset:8448\n"
"ds_read_b128 v[140:143], v2 offset:8512\n"
"ds_read_b128 v[144:147], v2 offset:8576\n"
"ds_read_b128 v[148:151], v2 offset:8640\n"
"ds_read_b128 v[152:155], v2 offset:8704\n"
"ds_read_b128 v[156:159], v2 offset:8768\n"
"ds_read_b128 v[160:163], v2 offset:8832\n"
"ds_read_b128 v[164:167], v2 offset:8896\n"
"ds_read_b128 v[168:171], v2 offset:8960\n"
"ds_read_b128 v[172:175], v2 offset:9024\n"
"s_mov_b32 s1, s74\n"
"s_add_u32 m0, s66, 61440\n"
"s_nop 0\n"
"buffer_load_dwordx4 v12, s[52:55], s1 offen lds\n"
"s_add_u32 s1, s1, 172032\n"
"s_add_u32 m0, s66, 65536\n"
"s_nop 0\n"
"buffer_load_dwordx4 v12, s[52:55], s1 offen lds\n"
"s_add_u32 s1, s1, 172032\n"
"s_add_u32 m0, s66, 69632\n"
"s_nop 0\n"
"buffer_load_dwordx4 v12, s[52:55], s1 offen lds\n"
"s_add_u32 s1, s1, 172032\n"
"s_add_u32 m0, s66, 73728\n"
"s_nop 0\n"
"buffer_load_dwordx4 v12, s[52:55], s1 offen lds\n"
"s_add_u32 s1, s1, 172032\n"
"s_lshl_b32 s1, s62, 7\n"
"s_add_u32 m0, s66, 77824\n"
"s_nop 0\n"
"buffer_load_dwordx4 v12, s[52:55], s1 offen lds\n"
"s_add_u32 m0, s66, 81920\n"
"s_add_u32 s1, s1, 172032\n"
"buffer_load_dwordx4 v12, s[52:55], s1 offen lds\n"
"s_add_u32 m0, s66, 86016\n"
"s_add_u32 s1, s1, 172032\n"
"buffer_load_dwordx4 v12, s[52:55], s1 offen lds\n"
"s_add_u32 m0, s66, 90112\n"
"s_add_u32 s1, s1, 172032\n"
"buffer_load_dwordx4 v12, s[52:55], s1 offen lds\n"
"s_add_u32 m0, s66, 94208\n"
"s_add_u32 s1, s1, 172032\n"
"buffer_load_dwordx4 v12, s[52:55], s1 offen lds\n"
"s_add_u32 m0, s66, 98304\n"
"s_add_u32 s1, s1, 172032\n"
"buffer_load_dwordx4 v12, s[52:55], s1 offen lds\n"
"s_waitcnt vmcnt(6)\n"
"s_barrier\n"
"ds_read_b64_tr_b16 v[64:65], v28 offset:0\n"
"ds_read_b64_tr_b16 v[66:67], v28 offset:1024\n"
"ds_read_b64_tr_b16 v[68:69], v28 offset:4096\n"
"ds_read_b64_tr_b16 v[70:71], v28 offset:5120\n"
"ds_read_b64_tr_b16 v[72:73], v28 offset:8192\n"
"ds_read_b64_tr_b16 v[74:75], v28 offset:9216\n"
"ds_read_b64_tr_b16 v[76:77], v28 offset:12288\n"
"ds_read_b64_tr_b16 v[78:79], v28 offset:13312\n"
"s_waitcnt lgkmcnt(6)\n"
"v_mfma_f32_32x32x16_bf16 v[176:191], v[120:123], v[64:67], 0\n"
"ds_read_b64_tr_b16 v[80:81], v28 offset:16384\n"
"ds_read_b64_tr_b16 v[82:83], v28 offset:17408\n"
"s_waitcnt lgkmcnt(6)\n"
"v_mfma_f32_32x32x16_bf16 v[176:191], v[124:127], v[68:71], v[176:191]\n"
"ds_read_b64_tr_b16 v[84:85], v28 offset:20480\n"
"ds_read_b64_tr_b16 v[86:87], v28 offset:21504\n"
"s_waitcnt lgkmcnt(6)\n"
"v_mfma_f32_32x32x16_bf16 v[176:191], v[128:131], v[72:75], v[176:191]\n"
"ds_read_b64_tr_b16 v[88:89], v28 offset:24576\n"
"ds_read_b64_tr_b16 v[90:91], v28 offset:25600\n"
"s_waitcnt lgkmcnt(6)\n"
"v_mfma_f32_32x32x16_bf16 v[176:191], v[132:135], v[76:79], v[176:191]\n"
"ds_read_b64_tr_b16 v[92:93], v28 offset:28672\n"
"ds_read_b64_tr_b16 v[94:95], v28 offset:29696\n"
"s_waitcnt lgkmcnt(6)\n"
"v_mfma_f32_32x32x16_bf16 v[176:191], v[136:139], v[80:83], v[176:191]\n"
"ds_read_b64_tr_b16 v[96:97], v28 offset:32768\n"
"ds_read_b64_tr_b16 v[98:99], v28 offset:33792\n"
"s_waitcnt lgkmcnt(6)\n"
"v_mfma_f32_32x32x16_bf16 v[176:191], v[140:143], v[84:87], v[176:191]\n"
"ds_read_b64_tr_b16 v[100:101], v28 offset:36864\n"
"ds_read_b64_tr_b16 v[102:103], v28 offset:37888\n"
"s_waitcnt lgkmcnt(6)\n"
"v_mfma_f32_32x32x16_bf16 v[176:191], v[144:147], v[88:91], v[176:191]\n"
"ds_read_b64_tr_b16 v[104:105], v28 offset:40960\n"
"ds_read_b64_tr_b16 v[106:107], v28 offset:41984\n"
"s_waitcnt lgkmcnt(6)\n"
"v_mfma_f32_32x32x16_bf16 v[176:191], v[148:151], v[92:95], v[176:191]\n"
"ds_read_b64_tr_b16 v[108:109], v28 offset:45056\n"
"ds_read_b64_tr_b16 v[110:111], v28 offset:46080\n"
"s_waitcnt lgkmcnt(6)\n"
"v_mfma_f32_32x32x16_bf16 v[176:191], v[152:155], v[96:99], v[176:191]\n"
"ds_read_b64_tr_b16 v[112:113], v28 offset:49152\n"
"ds_read_b64_tr_b16 v[114:115], v28 offset:50176\n"
"s_waitcnt lgkmcnt(6)\n"
"v_mfma_f32_32x32x16_bf16 v[176:191], v[156:159], v[100:103], v[176:191]\n"
"ds_read_b64_tr_b16 v[116:117], v28 offset:53248\n"
"ds_read_b64_tr_b16 v[118:119], v28 offset:54272\n"
"s_waitcnt lgkmcnt(6)\n"
"v_mfma_f32_32x32x16_bf16 v[176:191], v[160:163], v[104:107], v[176:191]\n"
"s_waitcnt lgkmcnt(4)\n"
"v_mfma_f32_32x32x16_bf16 v[176:191], v[164:167], v[108:111], v[176:191]\n"
"s_waitcnt lgkmcnt(2)\n"
"v_mfma_f32_32x32x16_bf16 v[176:191], v[168:171], v[112:115], v[176:191]\n"
"s_waitcnt lgkmcnt(0)\n"
"v_mfma_f32_32x32x16_bf16 v[176:191], v[172:175], v[116:119], v[176:191]\n"
"s_nop 11\n"
"ds_write_b64 v31, v[176:177] offset:13312\n"
"s_waitcnt lgkmcnt(0)\n"
"s_barrier\n"
"s_cmp_lg_u32 s43, 0\n"
"s_cbranch_scc1 Lpb2_%=\n"
"ds_read_b32 v35, v33 offset:13312\n"
"ds_read_b32 v36, v33 offset:14336\n"
"s_lshl_b32 s0, s61, 9\n"
"s_add_u32 s0, s0, s71\n"
"s_add_u32 s0, s0, 32768\n"
"s_waitcnt lgkmcnt(0)\n"
"v_add_f32 v22, v35, v36\n"
"s_cmp_ge_u32 s61, 35\n"
"s_cselect_b64 vcc, -1, 0\n"
"v_cvt_pk_bf16_f32 v22, v22, v22\n"
"v_lshlrev_b32 v22, 16, v22\n"
"v_mul_f32 v35, 0xbf317218, v22\n"
"v_cndmask_b32 v22, v22, v35, vcc\n"
"buffer_store_dwordx2 v[22:23], v34, s[48:51], s0 offen sc1\n"
"Lpb2_%=:\n"
"s_add_u32 m0, s66, 102400\n"
"s_add_u32 s1, s1, 172032\n"
"buffer_load_dwordx4 v12, s[52:55], s1 offen lds\n"
"s_add_u32 m0, s66, 106496\n"
"s_add_u32 s1, s1, 172032\n"
"buffer_load_dwordx4 v12, s[52:55], s1 offen lds\n"
"s_add_u32 m0, s66, 110592\n"
"s_add_u32 s1, s1, 172032\n"
"buffer_load_dwordx4 v12, s[52:55], s1 offen lds\n"
"s_add_u32 m0, s66, 114688\n"
"s_add_u32 s1, s1, 172032\n"
"buffer_load_dwordx4 v12, s[52:55], s1 offen lds\n"
"s_add_u32 m0, s66, 118784\n"
"s_add_u32 s1, s1, 172032\n"
"buffer_load_dwordx4 v12, s[52:55], s1 offen lds\n"
"s_add_u32 m0, s66, 122880\n"
"s_add_u32 s1, s1, 172032\n"
"buffer_load_dwordx4 v12, s[52:55], s1 offen lds\n"
"s_add_u32 m0, s66, 126976\n"
"s_add_u32 s1, s1, 172032\n"
"buffer_load_dwordx4 v12, s[52:55], s1 offen lds\n"
"s_add_u32 m0, s66, 131072\n"
"s_add_u32 s1, s1, 172032\n"
"buffer_load_dwordx4 v12, s[52:55], s1 offen lds\n"
"s_lshl_b32 s68, s60, 1\n"
"s_sub_u32 s68, s68, 21\n"
"s_add_u32 s69, s68, 1\n"
"s_min_u32 s69, s69, 41\n"
"s_lshl_b32 s1, s68, 7\n"
"s_add_u32 m0, s66, 20480\n"
"s_nop 0\n"
"buffer_load_dwordx4 v12, s[52:55], s1 offen lds\n"
"s_add_u32 m0, s66, 24576\n"
"s_add_u32 s1, s1, 172032\n"
"buffer_load_dwordx4 v12, s[52:55], s1 offen lds\n"
"s_waitcnt vmcnt(2)\n"
"s_barrier\n"
"ds_read_b64_tr_b16 v[64:65], v29 offset:0\n"
"ds_read_b64_tr_b16 v[66:67], v29 offset:1024\n"
"ds_read_b64_tr_b16 v[68:69], v29 offset:4096\n"
"ds_read_b64_tr_b16 v[70:71], v29 offset:5120\n"
"ds_read_b64_tr_b16 v[72:73], v29 offset:8192\n"
"ds_read_b64_tr_b16 v[74:75], v29 offset:9216\n"
"ds_read_b64_tr_b16 v[76:77], v29 offset:12288\n"
"ds_read_b64_tr_b16 v[78:79], v29 offset:13312\n"
"s_waitcnt lgkmcnt(6)\n"
"v_mfma_f32_32x32x16_bf16 v[176:191], v[120:123], v[64:67], 0\n"
"ds_read_b64_tr_b16 v[80:81], v29 offset:16384\n"
"ds_read_b64_tr_b16 v[82:83], v29 offset:17408\n"
"s_waitcnt lgkmcnt(6)\n"
"v_mfma_f32_32x32x16_bf16 v[176:191], v[124:127], v[68:71], v[176:191]\n"
"ds_read_b64_tr_b16 v[84:85], v29 offset:20480\n"
"ds_read_b64_tr_b16 v[86:87], v29 offset:21504\n"
"s_waitcnt lgkmcnt(6)\n"
"v_mfma_f32_32x32x16_bf16 v[176:191], v[128:131], v[72:75], v[176:191]\n"
"ds_read_b64_tr_b16 v[88:89], v29 offset:24576\n"
"ds_read_b64_tr_b16 v[90:91], v29 offset:25600\n"
"s_waitcnt lgkmcnt(6)\n"
"v_mfma_f32_32x32x16_bf16 v[176:191], v[132:135], v[76:79], v[176:191]\n"
"ds_read_b64_tr_b16 v[92:93], v29 offset:28672\n"
"ds_read_b64_tr_b16 v[94:95], v29 offset:29696\n"
"s_waitcnt lgkmcnt(6)\n"
"v_mfma_f32_32x32x16_bf16 v[176:191], v[136:139], v[80:83], v[176:191]\n"
"ds_read_b64_tr_b16 v[96:97], v29 offset:32768\n"
"ds_read_b64_tr_b16 v[98:99], v29 offset:33792\n"
"s_waitcnt lgkmcnt(6)\n"
"v_mfma_f32_32x32x16_bf16 v[176:191], v[140:143], v[84:87], v[176:191]\n"
"ds_read_b64_tr_b16 v[100:101], v29 offset:36864\n"
"ds_read_b64_tr_b16 v[102:103], v29 offset:37888\n"
"s_waitcnt lgkmcnt(6)\n"
"v_mfma_f32_32x32x16_bf16 v[176:191], v[144:147], v[88:91], v[176:191]\n"
"ds_read_b64_tr_b16 v[104:105], v29 offset:40960\n"
"ds_read_b64_tr_b16 v[106:107], v29 offset:41984\n"
"s_waitcnt lgkmcnt(6)\n"
"v_mfma_f32_32x32x16_bf16 v[176:191], v[148:151], v[92:95], v[176:191]\n"
"ds_read_b64_tr_b16 v[108:109], v29 offset:45056\n"
"ds_read_b64_tr_b16 v[110:111], v29 offset:46080\n"
"s_waitcnt lgkmcnt(6)\n"
"v_mfma_f32_32x32x16_bf16 v[176:191], v[152:155], v[96:99], v[176:191]\n"
"ds_read_b64_tr_b16 v[112:113], v29 offset:49152\n"
"ds_read_b64_tr_b16 v[114:115], v29 offset:50176\n"
"s_waitcnt lgkmcnt(6)\n"
"v_mfma_f32_32x32x16_bf16 v[176:191], v[156:159], v[100:103], v[176:191]\n"
"ds_read_b64_tr_b16 v[116:117], v29 offset:53248\n"
"ds_read_b64_tr_b16 v[118:119], v29 offset:54272\n"
"s_waitcnt lgkmcnt(6)\n"
"v_mfma_f32_32x32x16_bf16 v[176:191], v[160:163], v[104:107], v[176:191]\n"
"s_waitcnt lgkmcnt(4)\n"
"v_mfma_f32_32x32x16_bf16 v[176:191], v[164:167], v[108:111], v[176:191]\n"
"s_waitcnt lgkmcnt(2)\n"
"v_mfma_f32_32x32x16_bf16 v[176:191], v[168:171], v[112:115], v[176:191]\n"
"s_waitcnt lgkmcnt(0)\n"
"v_mfma_f32_32x32x16_bf16 v[176:191], v[172:175], v[116:119], v[176:191]\n"
"s_nop 11\n"
"ds_write_b64 v31, v[176:177] offset:15360\n"
"s_waitcnt lgkmcnt(0)\n"
"s_barrier\n"
"s_cmp_lg_u32 s43, 0\n"
"s_cbranch_scc1 Lpb3_%=\n"
"ds_read_b32 v35, v33 offset:15360\n"
"ds_read_b32 v36, v33 offset:16384\n"
"s_lshl_b32 s0, s62, 9\n"
"s_add_u32 s0, s0, s71\n"
"s_add_u32 s0, s0, 32768\n"
"s_waitcnt lgkmcnt(0)\n"
"v_add_f32 v22, v35, v36\n"
"s_cmp_ge_u32 s62, 35\n"
"s_cselect_b64 vcc, -1, 0\n"
"v_cvt_pk_bf16_f32 v22, v22, v22\n"
"v_lshlrev_b32 v22, 16, v22\n"
"v_mul_f32 v35, 0xbf317218, v22\n"
"v_cndmask_b32 v22, v22, v35, vcc\n"
"buffer_store_dwordx2 v[22:23], v34, s[48:51], s0 offen sc1\n"
"Lpb3_%=:\n"
"s_add_u32 m0, s66, 28672\n"
"s_add_u32 s1, s1, 172032\n"
"buffer_load_dwordx4 v12, s[52:55], s1 offen lds\n"
"s_add_u32 m0, s66, 32768\n"
"s_add_u32 s1, s1, 172032\n"
"buffer_load_dwordx4 v12, s[52:55], s1 offen lds\n"
"s_add_u32 m0, s66, 36864\n"
"s_add_u32 s1, s1, 172032\n"
"buffer_load_dwordx4 v12, s[52:55], s1 offen lds\n"
"s_add_u32 m0, s66, 40960\n"
"s_add_u32 s1, s1, 172032\n"
"buffer_load_dwordx4 v12, s[52:55], s1 offen lds\n"
"s_add_u32 m0, s66, 45056\n"
"s_add_u32 s1, s1, 172032\n"
"buffer_load_dwordx4 v12, s[52:55], s1 offen lds\n"
"s_add_u32 m0, s66, 49152\n"
"s_add_u32 s1, s1, 172032\n"
"buffer_load_dwordx4 v12, s[52:55], s1 offen lds\n"
"s_add_u32 m0, s66, 53248\n"
"s_add_u32 s1, s1, 172032\n"
"buffer_load_dwordx4 v12, s[52:55], s1 offen lds\n"
"s_add_u32 m0, s66, 57344\n"
"s_add_u32 s1, s1, 172032\n"
"buffer_load_dwordx4 v12, s[52:55], s1 offen lds\n"
"s_add_u32 m0, s66, 61440\n"
"s_add_u32 s1, s1, 172032\n"
"buffer_load_dwordx4 v12, s[52:55], s1 offen lds\n"
"s_add_u32 m0, s66, 65536\n"
"s_add_u32 s1, s1, 172032\n"
"buffer_load_dwordx4 v12, s[52:55], s1 offen lds\n"
"s_add_u32 m0, s66, 69632\n"
"s_add_u32 s1, s1, 172032\n"
"buffer_load_dwordx4 v12, s[52:55], s1 offen lds\n"
"s_add_u32 m0, s66, 73728\n"
"s_add_u32 s1, s1, 172032\n"
"buffer_load_dwordx4 v12, s[52:55], s1 offen lds\n"
"s_lshl_b32 s1, s69, 7\n"
"s_add_u32 m0, s66, 77824\n"
"s_nop 0\n"
"buffer_load_dwordx4 v12, s[52:55], s1 offen lds\n"
"s_add_u32 m0, s66, 81920\n"
"s_add_u32 s1, s1, 172032\n"
"buffer_load_dwordx4 v12, s[52:55], s1 offen lds\n"
"s_add_u32 m0, s66, 86016\n"
"s_add_u32 s1, s1, 172032\n"
"buffer_load_dwordx4 v12, s[52:55], s1 offen lds\n"
"s_add_u32 m0, s66, 90112\n"
"s_add_u32 s1, s1, 172032\n"
"buffer_load_dwordx4 v12, s[52:55], s1 offen lds\n"
"s_add_u32 m0, s66, 94208\n"
"s_add_u32 s1, s1, 172032\n"
"buffer_load_dwordx4 v12, s[52:55], s1 offen lds\n"
"s_add_u32 m0, s66, 98304\n"
"s_add_u32 s1, s1, 172032\n"
"buffer_load_dwordx4 v12, s[52:55], s1 offen lds\n"
"s_add_u32 m0, s66, 102400\n"
"s_add_u32 s1, s1, 172032\n"
"buffer_load_dwordx4 v12, s[52:55], s1 offen lds\n"
"s_add_u32 m0, s66, 106496\n"
"s_add_u32 s1, s1, 172032\n"
"buffer_load_dwordx4 v12, s[52:55], s1 offen lds\n"
"s_add_u32 m0, s66, 110592\n"
"s_add_u32 s1, s1, 172032\n"
"buffer_load_dwordx4 v12, s[52:55], s1 offen lds\n"
"s_add_u32 m0, s66, 114688\n"
"s_add_u32 s1, s1, 172032\n"
"buffer_load_dwordx4 v12, s[52:55], s1 offen lds\n"
"s_add_u32 m0, s66, 118784\n"
"s_add_u32 s1, s1, 172032\n"
"buffer_load_dwordx4 v12, s[52:55], s1 offen lds\n"
"s_add_u32 m0, s66, 122880\n"
"s_add_u32 s1, s1, 172032\n"
"buffer_load_dwordx4 v12, s[52:55], s1 offen lds\n"
"s_add_u32 m0, s66, 126976\n"
"s_add_u32 s1, s1, 172032\n"
"buffer_load_dwordx4 v12, s[52:55], s1 offen lds\n"
"s_add_u32 m0, s66, 131072\n"
"s_add_u32 s1, s1, 172032\n"
"buffer_load_dwordx4 v12, s[52:55], s1 offen lds\n"
"s_waitcnt vmcnt(14)\n"
"s_barrier\n"
"ds_read_b64_tr_b16 v[64:65], v28 offset:0\n"
"ds_read_b64_tr_b16 v[66:67], v28 offset:1024\n"
"ds_read_b64_tr_b16 v[68:69], v28 offset:4096\n"
"ds_read_b64_tr_b16 v[70:71], v28 offset:5120\n"
"ds_read_b64_tr_b16 v[72:73], v28 offset:8192\n"
"ds_read_b64_tr_b16 v[74:75], v28 offset:9216\n"
"ds_read_b64_tr_b16 v[76:77], v28 offset:12288\n"
"ds_read_b64_tr_b16 v[78:79], v28 offset:13312\n"
"s_waitcnt lgkmcnt(6)\n"
"v_mfma_f32_32x32x16_bf16 v[176:191], v[120:123], v[64:67], 0\n"
"ds_read_b64_tr_b16 v[80:81], v28 offset:16384\n"
"ds_read_b64_tr_b16 v[82:83], v28 offset:17408\n"
"s_waitcnt lgkmcnt(6)\n"
"v_mfma_f32_32x32x16_bf16 v[176:191], v[124:127], v[68:71], v[176:191]\n"
"ds_read_b64_tr_b16 v[84:85], v28 offset:20480\n"
"ds_read_b64_tr_b16 v[86:87], v28 offset:21504\n"
"s_waitcnt lgkmcnt(6)\n"
"v_mfma_f32_32x32x16_bf16 v[176:191], v[128:131], v[72:75], v[176:191]\n"
"ds_read_b64_tr_b16 v[88:89], v28 offset:24576\n"
"ds_read_b64_tr_b16 v[90:91], v28 offset:25600\n"
"s_waitcnt lgkmcnt(6)\n"
"v_mfma_f32_32x32x16_bf16 v[176:191], v[132:135], v[76:79], v[176:191]\n"
"ds_read_b64_tr_b16 v[92:93], v28 offset:28672\n"
"ds_read_b64_tr_b16 v[94:95], v28 offset:29696\n"
"s_waitcnt lgkmcnt(6)\n"
"v_mfma_f32_32x32x16_bf16 v[176:191], v[136:139], v[80:83], v[176:191]\n"
"ds_read_b64_tr_b16 v[96:97], v28 offset:32768\n"
"ds_read_b64_tr_b16 v[98:99], v28 offset:33792\n"
"s_waitcnt lgkmcnt(6)\n"
"v_mfma_f32_32x32x16_bf16 v[176:191], v[140:143], v[84:87], v[176:191]\n"
"ds_read_b64_tr_b16 v[100:101], v28 offset:36864\n"
"ds_read_b64_tr_b16 v[102:103], v28 offset:37888\n"
"s_waitcnt lgkmcnt(6)\n"
"v_mfma_f32_32x32x16_bf16 v[176:191], v[144:147], v[88:91], v[176:191]\n"
"ds_read_b64_tr_b16 v[104:105], v28 offset:40960\n"
"ds_read_b64_tr_b16 v[106:107], v28 offset:41984\n"
"s_waitcnt lgkmcnt(6)\n"
"v_mfma_f32_32x32x16_bf16 v[176:191], v[148:151], v[92:95], v[176:191]\n"
"ds_read_b64_tr_b16 v[108:109], v28 offset:45056\n"
"ds_read_b64_tr_b16 v[110:111], v28 offset:46080\n"
"s_waitcnt lgkmcnt(6)\n"
"v_mfma_f32_32x32x16_bf16 v[176:191], v[152:155], v[96:99], v[176:191]\n"
"ds_read_b64_tr_b16 v[112:113], v28 offset:49152\n"
"ds_read_b64_tr_b16 v[114:115], v28 offset:50176\n"
"s_waitcnt lgkmcnt(6)\n"
"v_mfma_f32_32x32x16_bf16 v[176:191], v[156:159], v[100:103], v[176:191]\n"
"ds_read_b64_tr_b16 v[116:117], v28 offset:53248\n"
"ds_read_b64_tr_b16 v[118:119], v28 offset:54272\n"
"s_waitcnt lgkmcnt(6)\n"
"v_mfma_f32_32x32x16_bf16 v[176:191], v[160:163], v[104:107], v[176:191]\n"
"s_waitcnt lgkmcnt(4)\n"
"v_mfma_f32_32x32x16_bf16 v[176:191], v[164:167], v[108:111], v[176:191]\n"
"s_waitcnt lgkmcnt(2)\n"
"v_mfma_f32_32x32x16_bf16 v[176:191], v[168:171], v[112:115], v[176:191]\n"
"s_waitcnt lgkmcnt(0)\n"
"v_mfma_f32_32x32x16_bf16 v[176:191], v[172:175], v[116:119], v[176:191]\n"
"s_nop 11\n"
"ds_write_b64 v31, v[176:177] offset:13312\n"
"s_waitcnt lgkmcnt(0)\n"
"s_barrier\n"
"s_cmp_lg_u32 s43, 0\n"
"s_cbranch_scc1 Lpb4_%=\n"
"ds_read_b32 v35, v33 offset:13312\n"
"ds_read_b32 v36, v33 offset:14336\n"
"s_lshl_b32 s0, s68, 9\n"
"s_add_u32 s0, s0, s71\n"
"s_add_u32 s0, s0, 32768\n"
"s_waitcnt lgkmcnt(0)\n"
"v_add_f32 v22, v35, v36\n"
"s_cmp_ge_u32 s68, 35\n"
"s_cselect_b64 vcc, -1, 0\n"
"v_cvt_pk_bf16_f32 v22, v22, v22\n"
"v_lshlrev_b32 v22, 16, v22\n"
"v_mul_f32 v35, 0xbf317218, v22\n"
"v_cndmask_b32 v22, v22, v35, vcc\n"
"buffer_store_dwordx2 v[22:23], v34, s[48:51], s0 offen sc1\n"
"Lpb4_%=:\n"
"s_waitcnt vmcnt(0)\n"
"s_barrier\n"
"ds_read_b64_tr_b16 v[64:65], v29 offset:0\n"
"ds_read_b64_tr_b16 v[66:67], v29 offset:1024\n"
"ds_read_b64_tr_b16 v[68:69], v29 offset:4096\n"
"ds_read_b64_tr_b16 v[70:71], v29 offset:5120\n"
"ds_read_b64_tr_b16 v[72:73], v29 offset:8192\n"
"ds_read_b64_tr_b16 v[74:75], v29 offset:9216\n"
"ds_read_b64_tr_b16 v[76:77], v29 offset:12288\n"
"ds_read_b64_tr_b16 v[78:79], v29 offset:13312\n"
"s_waitcnt lgkmcnt(6)\n"
"v_mfma_f32_32x32x16_bf16 v[176:191], v[120:123], v[64:67], 0\n"
"ds_read_b64_tr_b16 v[80:81], v29 offset:16384\n"
"ds_read_b64_tr_b16 v[82:83], v29 offset:17408\n"
"s_waitcnt lgkmcnt(6)\n"
"v_mfma_f32_32x32x16_bf16 v[176:191], v[124:127], v[68:71], v[176:191]\n"
"ds_read_b64_tr_b16 v[84:85], v29 offset:20480\n"
"ds_read_b64_tr_b16 v[86:87], v29 offset:21504\n"
"s_waitcnt lgkmcnt(6)\n"
"v_mfma_f32_32x32x16_bf16 v[176:191], v[128:131], v[72:75], v[176:191]\n"
"ds_read_b64_tr_b16 v[88:89], v29 offset:24576\n"
"ds_read_b64_tr_b16 v[90:91], v29 offset:25600\n"
"s_waitcnt lgkmcnt(6)\n"
"v_mfma_f32_32x32x16_bf16 v[176:191], v[132:135], v[76:79], v[176:191]\n"
"ds_read_b64_tr_b16 v[92:93], v29 offset:28672\n"
"ds_read_b64_tr_b16 v[94:95], v29 offset:29696\n"
"s_waitcnt lgkmcnt(6)\n"
"v_mfma_f32_32x32x16_bf16 v[176:191], v[136:139], v[80:83], v[176:191]\n"
"ds_read_b64_tr_b16 v[96:97], v29 offset:32768\n"
"ds_read_b64_tr_b16 v[98:99], v29 offset:33792\n"
"s_waitcnt lgkmcnt(6)\n"
"v_mfma_f32_32x32x16_bf16 v[176:191], v[140:143], v[84:87], v[176:191]\n"
"ds_read_b64_tr_b16 v[100:101], v29 offset:36864\n"
"ds_read_b64_tr_b16 v[102:103], v29 offset:37888\n"
"s_waitcnt lgkmcnt(6)\n"
"v_mfma_f32_32x32x16_bf16 v[176:191], v[144:147], v[88:91], v[176:191]\n"
"ds_read_b64_tr_b16 v[104:105], v29 offset:40960\n"
"ds_read_b64_tr_b16 v[106:107], v29 offset:41984\n"
"s_waitcnt lgkmcnt(6)\n"
"v_mfma_f32_32x32x16_bf16 v[176:191], v[148:151], v[92:95], v[176:191]\n"
"ds_read_b64_tr_b16 v[108:109], v29 offset:45056\n"
"ds_read_b64_tr_b16 v[110:111], v29 offset:46080\n"
"s_waitcnt lgkmcnt(6)\n"
"v_mfma_f32_32x32x16_bf16 v[176:191], v[152:155], v[96:99], v[176:191]\n"
"ds_read_b64_tr_b16 v[112:113], v29 offset:49152\n"
"ds_read_b64_tr_b16 v[114:115], v29 offset:50176\n"
"s_waitcnt lgkmcnt(6)\n"
"v_mfma_f32_32x32x16_bf16 v[176:191], v[156:159], v[100:103], v[176:191]\n"
"ds_read_b64_tr_b16 v[116:117], v29 offset:53248\n"
"ds_read_b64_tr_b16 v[118:119], v29 offset:54272\n"
"s_waitcnt lgkmcnt(6)\n"
"v_mfma_f32_32x32x16_bf16 v[176:191], v[160:163], v[104:107], v[176:191]\n"
"s_waitcnt lgkmcnt(4)\n"
"v_mfma_f32_32x32x16_bf16 v[176:191], v[164:167], v[108:111], v[176:191]\n"
"s_waitcnt lgkmcnt(2)\n"
"v_mfma_f32_32x32x16_bf16 v[176:191], v[168:171], v[112:115], v[176:191]\n"
"s_waitcnt lgkmcnt(0)\n"
"v_mfma_f32_32x32x16_bf16 v[176:191], v[172:175], v[116:119], v[176:191]\n"
"s_nop 11\n"
"ds_write_b64 v31, v[176:177] offset:15360\n"
"s_waitcnt lgkmcnt(0)\n"
"s_barrier\n"
"s_cmp_lg_u32 s43, 0\n"
"s_cbranch_scc1 Lpb5_%=\n"
"ds_read_b32 v35, v33 offset:15360\n"
"ds_read_b32 v36, v33 offset:16384\n"
"s_lshl_b32 s0, s69, 9\n"
"s_add_u32 s0, s0, s71\n"
"s_add_u32 s0, s0, 32768\n"
"s_waitcnt lgkmcnt(0)\n"
"v_add_f32 v22, v35, v36\n"
"s_cmp_ge_u32 s69, 35\n"
"s_cselect_b64 vcc, -1, 0\n"
"v_cvt_pk_bf16_f32 v22, v22, v22\n"
"v_lshlrev_b32 v22, 16, v22\n"
"v_mul_f32 v35, 0xbf317218, v22\n"
"v_cndmask_b32 v22, v22, v35, vcc\n"
"buffer_store_dwordx2 v[22:23], v34, s[48:51], s0 offen sc1\n"
"Lpb5_%=:\n"
"Lhend_%=:\n"
"s_endpgm\n"
"Lcodeend_%=:\n"
      : "+{v0}"(tid), "+{s[36:37]}"(ka), "+{s38}"(blk)
      : "v"((LAS char*)smem_)
      : "memory", "vcc", "v255", "a255", "s99");
  __builtin_amdgcn_endpgm();
}

extern "C" void kernel_launch(void* const* d_in, const int* in_sizes, int n_in, void* d_out, int out_size, void* d_ws,
                              size_t ws_size, hipStream_t stream) {
  Args a;
  a.x = (const u16*)d_in[0]; a.Are = (const u16*)d_in[1]; a.Aim = (const u16*)d_in[2]; a.Bre = (const u16*)d_in[3];
  a.Bim = (const u16*)d_in[4]; a.Cre = (const u16*)d_in[5]; a.Cim = (const u16*)d_in[6]; a.logdt = (const u16*)d_in[7];
  a.Dskip = (const u16*)d_in[8]; a.w2 = (const u16*)d_in[11]; a.ws = (char*)d_ws + WSHIFT; a.out = (u16*)d_out;
  a.c = (const u16*)d_in[12]; a.wmod = (const u16*)d_in[13]; a.w1 = (const u16*)d_in[10];
  k_fused<<<256, 256, 0, stream>>>(a);
}
```
